```python
import torch

def _softmax(x, axis=-1):
    return torch.softmax(x, dim=axis)
def _gelu(x):
    return torch.nn.functional.gelu(x, approximate="tanh")
def _silu(x):
    return torch.nn.functional.silu(x)
def _sigmoid(x):
    return torch.sigmoid(x)
def _layernorm(x, axis=-1, eps=1e-5):
    m = x.mean(dim=axis, keepdim=True)
    v = x.var(dim=axis, keepdim=True, unbiased=False)
    return (x - m) / torch.sqrt(v + eps)
def _scatter_add(tgt, idx, src, axis=0):
    return tgt.clone().index_add_(axis, idx, src)
def _segment_sum(src, seg, n):
    out = torch.zeros((n,) + src.shape[1:], dtype=src.dtype)
    return out.index_add_(0, seg, src)
def _pad(x, pw):
    flat = tuple(v for pair in reversed(pw) for v in pair)
    return torch.nn.functional.pad(x, flat)

kernel_name = 'sequential_preLN__ssm_conv_h3__glu_gated_geglu__prompt'

def setup_inputs():

    _w = (lambda a: (lambda t: t.to(torch.bfloat16) if t.is_floating_point() else t)(torch.as_tensor(a)))
    def _A_setup():
        B, S, D = 1, 2048, 576
        ks = 4
        return {
            "q": _w((lambda *s: torch.randn(*s, dtype=torch.bfloat16) * 0.05)(B, S, D)),
            "k": _w((lambda *s: torch.randn(*s, dtype=torch.bfloat16) * 0.2)(B, S, D)),
            "v": _w((lambda *s: torch.randn(*s, dtype=torch.bfloat16) * 0.2)(B, S, D)),
            "shift_k": _w((lambda *s: torch.randn(*s, dtype=torch.bfloat16) * 0.2)(D, ks) * 0.5),
            "ssm_k": _w((lambda *s: torch.randn(*s, dtype=torch.bfloat16) * 0.4)(D, S) * (1.0 / S ** 0.5)),
            "ssm_D": _w((lambda *s: torch.randn(*s, dtype=torch.bfloat16) * 0.2)(D)),
        }
    _A_in = _A_setup()

    def _B_setup():
        B, S, Dm, Df = 1, 2048, 576, 4608
        return {
            "x": _w((lambda *s: torch.randn(*s, dtype=torch.bfloat16) * 0.05)(B, S, Dm)),
            "w_gate": _w((lambda *s: torch.randn(*s, dtype=torch.bfloat16) * 0.4)(Dm, Df) * (Dm ** -0.5)),
            "w_up": _w((lambda *s: torch.randn(*s, dtype=torch.bfloat16) * 0.4)(Dm, Df) * (Dm ** -0.5)),
            "w_down": _w((lambda *s: torch.randn(*s, dtype=torch.bfloat16) * 0.4)(Df, Dm) * (Df ** -0.5)),
        }
    _B_in = _B_setup()

    return {
        "A_q": _A_in["q"],
        "A_k": _A_in["k"],
        "A_v": _A_in["v"],
        "A_shift_k": _A_in["shift_k"],
        "A_ssm_k": _A_in["ssm_k"],
        "A_ssm_D": _A_in["ssm_D"],
        "B_x": _B_in["x"],
        "B_w_gate": _B_in["w_gate"],
        "B_w_up": _B_in["w_up"],
        "B_w_down": _B_in["w_down"],
    }

def reference(A_q, A_k, A_v, A_shift_k, A_ssm_k, A_ssm_D, B_x, B_w_gate, B_w_up, B_w_down):
    def _A_ref(q, k, v, shift_k, ssm_k, ssm_D):
        B, S, D = q.shape
        ks = shift_k.shape[-1]
        k_pad = _pad(k, ((0, 0), (ks - 1, 0), (0, 0)))
        k_shift = torch.zeros_like(k)
        for t in range(ks):
            k_shift = k_shift + k_pad[:, t:t + S, :] * shift_k[:, ks - 1 - t][None, None, :]
        kv = k_shift * v
        L = 2 * S
        kv_pad = _pad(kv, ((0, 0), (0, S), (0, 0)))
        kf_pad = _pad(ssm_k, ((0, 0), (0, S)))
        kv_f = (lambda x, axes=(-2,-1): torch.fft.rfft2(x.float(), dim=axes))(kv_pad[:, None, :, :], axes=(1, 2))
        kf_f = (lambda x, axes=(-2,-1): torch.fft.rfft2(x.float(), dim=axes))(kf_pad[:, None, :], axes=(1, 2))
        kf_f = (lambda x, *d: x.permute(*d))(kf_f, 1, 2, 0)
        y_f = kv_f * kf_f[None, :, :, :]
        y = (lambda x, s, axes=(-2,-1): torch.fft.irfft2(x, s=s, dim=axes).to(torch.bfloat16))(y_f, (1, L), axes=(1, 2))[:, 0, :S, :]
        y = y + kv * ssm_D[None, None, :]
        return q * y

    def _B_ref(x, w_gate, w_up, w_down):
        g = torch.einsum("bsd,df->bsf", x, w_gate)
        u = torch.einsum("bsd,df->bsf", x, w_up)
        h = _gelu(g) * u
        return torch.einsum("bsf,fd->bsd", h, w_down)

    x = A_q
    h = x + _A_ref(_layernorm(x, axis=-1), A_k, A_v, A_shift_k, A_ssm_k, A_ssm_D)
    h2 = h + _B_ref(_layernorm(h, axis=-1), B_w_gate, B_w_up, B_w_down)
    return h2
```

```cpp
#include <hip/hip_runtime.h>
#include <hip/hip_bf16.h>
#include <stdint.h>

typedef unsigned short u16;
typedef unsigned int u32;
typedef __attribute__((ext_vector_type(2))) unsigned u32x2;
typedef __attribute__((ext_vector_type(4))) unsigned u32x4;
typedef __attribute__((ext_vector_type(2))) float f32x2;
typedef __attribute__((ext_vector_type(4))) float f32x4;
typedef __attribute__((ext_vector_type(16))) float f32x16;
typedef __attribute__((ext_vector_type(8))) __bf16 bf16x8;
typedef __attribute__((ext_vector_type(2))) __bf16 bf16x2;
typedef __attribute__((ext_vector_type(4))) short s16x4;
#define GAS __attribute__((address_space(1)))
#define LAS __attribute__((address_space(3)))
#define DI __device__ __forceinline__

constexpr int S_ = 2048, D_ = 576, DF_ = 4608;
constexpr size_t OFF_KVT = 0;
constexpr size_t OFF_XP  = OFF_KVT + (size_t)D_ * S_ * 2;
constexpr size_t OFF_HF  = OFF_XP + (size_t)D_ * S_ * 2;
constexpr size_t OFF_ST1 = OFF_HF + (size_t)D_ * S_ * 2;
constexpr size_t OFF_CS  = OFF_ST1 + 2048 * 8;
constexpr size_t OFF_FLG = OFF_CS + 2 * 4608 * 4;
constexpr size_t OFF_HH  = OFF_FLG + 65536;
constexpr size_t OFF_PART = OFF_HH + (size_t)S_ * DF_ * 2;

DI float bflo(u32 w) { return __uint_as_float(w << 16); }
DI float bfhi(u32 w) { return __uint_as_float(w & 0xffff0000u); }
DI u32 pack2(float a, float b) { bf16x2 p; p[0] = (__bf16)a; p[1] = (__bf16)b; return __builtin_bit_cast(u32, p); }

__global__ __launch_bounds__(256) void k0_prep(const u16* __restrict__ q, const u16* __restrict__ k, const u16* __restrict__ v,
                                               const u16* __restrict__ shk, u16* __restrict__ kvt, u16* __restrict__ xp,
                                               f32x2* __restrict__ st1) {
  const int b = blockIdx.x, tid = threadIdx.x, w = tid >> 6, l = tid & 63;
  if (b < 288) {
    const int tb = b / 9, cb = b - tb * 9;
    const int s0 = tb * 64 + w * 16 + (l >> 4) * 4;
    const int c0 = cb * 64 + (l & 15) * 4;
    u32x2 kr[7], vr[4], qr[4];
#pragma unroll
    for (int i = 0; i < 7; ++i) {
      const int s = s0 - 3 + i;
      u32x2 z = {0u, 0u};
      kr[i] = (s >= 0) ? *(const u32x2*)(k + (size_t)s * D_ + c0) : z;
    }
#pragma unroll
    for (int i = 0; i < 4; ++i) { vr[i] = *(const u32x2*)(v + (size_t)(s0 + i) * D_ + c0); qr[i] = *(const u32x2*)(q + (size_t)(s0 + i) * D_ + c0); }
    const u32x4 w0 = *(const u32x4*)(shk + c0 * 4), w1 = *(const u32x4*)(shk + c0 * 4 + 8);
    u32x4 xa = {qr[0][0], qr[0][1], qr[1][0], qr[1][1]}, xb = {qr[2][0], qr[2][1], qr[3][0], qr[3][1]};
    u16* xdst = xp + ((size_t)(c0 >> 2) * S_ + s0) * 4;
    *(u32x4*)xdst = xa; *(u32x4*)(xdst + 8) = xb;
#pragma unroll
    for (int c = 0; c < 4; ++c) {
      const u32 wa = (c < 2) ? w0[2 * (c & 1)] : w1[2 * (c & 1)], wb = (c < 2) ? w0[2 * (c & 1) + 1] : w1[2 * (c & 1) + 1];
      const float f0 = bflo(wa), f1 = bfhi(wa), f2 = bflo(wb), f3 = bfhi(wb);
      float kk[7], kvv[4];
#pragma unroll
      for (int i = 0; i < 7; ++i) kk[i] = (c & 1) ? bfhi(kr[i][c >> 1]) : bflo(kr[i][c >> 1]);
#pragma unroll
      for (int i = 0; i < 4; ++i) {
        const float ks = kk[i + 3] * f0 + kk[i + 2] * f1 + kk[i + 1] * f2 + kk[i] * f3;
        const float vv = (c & 1) ? bfhi(vr[i][c >> 1]) : bflo(vr[i][c >> 1]);
        kvv[i] = ks * vv;
      }
      u32x2 o = {pack2(kvv[3], kvv[2]), pack2(kvv[1], kvv[0])};
      u16* dst = kvt + (size_t)(c0 + c) * S_ + (s0 >> 3) * 8 + ((s0 & 4) ? 0 : 4);
      *(u32x2*)dst = o;
    }
  } else {
    const int row = (b - 288) * 32 + w * 8 + (l >> 3);
    const u16* p = q + (size_t)row * D_ + (l & 7) * 8;
    u32x4 x[9];
#pragma unroll
    for (int i = 0; i < 9; ++i) x[i] = *(const u32x4*)(p + i * 64);
    float sum = 0.f;
#pragma unroll
    for (int i = 0; i < 9; ++i)
#pragma unroll
      for (int j = 0; j < 4; ++j) sum += bflo(x[i][j]) + bfhi(x[i][j]);
    sum += __shfl_xor(sum, 1); sum += __shfl_xor(sum, 2); sum += __shfl_xor(sum, 4);
    const float mu = sum * (1.f / D_);
    float sq = 0.f;
#pragma unroll
    for (int i = 0; i < 9; ++i)
#pragma unroll
      for (int j = 0; j < 4; ++j) { const float a = bflo(x[i][j]) - mu, c = bfhi(x[i][j]) - mu; sq += a * a + c * c; }
    sq += __shfl_xor(sq, 1); sq += __shfl_xor(sq, 2); sq += __shfl_xor(sq, 4);
    if ((l & 7) == 0) { f32x2 o = {mu, rsqrtf(sq * (1.f / D_) + 1e-5f)}; st1[row] = o; }
  }
}

constexpr int K1_UPLANE = 2304, K1_UCH = 4608, K1_TOFF = 4 * K1_UCH, K1_TCOPY = 4160, K1_TCH = 4 * K1_TCOPY;
constexpr int K1_YROW = 80, K1_YBUF = 128 * K1_YROW;
constexpr int K1_LDS = K1_TOFF + 8 * K1_YBUF;

__global__ __launch_bounds__(512) void k1_conv(const u16* __restrict__ ssm, const u16* __restrict__ ssmD, const u16* __restrict__ kvt,
                                               const u16* __restrict__ xp, const f32x2* __restrict__ st1, u16* __restrict__ hf,
                                               const u16* __restrict__ wg, const u16* __restrict__ wu, float* __restrict__ cs) {
  extern __shared__ __attribute__((aligned(16))) char lds[];
  const int tid = threadIdx.x, l = tid & 63;
  const int w = __builtin_amdgcn_readfirstlane(tid >> 6);
  const int b = blockIdx.x;
  if (b >= 144) {
    const int j = b - 144;
    const u16* W = (j < 36) ? wg : wu;
    const int col0 = (j < 36 ? j : j - 36) * 128;
    const u16* p = W + (size_t)(w * 72 + (l >> 4)) * DF_ + col0 + (l & 15) * 8;
    u32x4 x[18];
#pragma unroll
    for (int i = 0; i < 18; ++i) x[i] = *(const u32x4*)(p + (size_t)i * 4 * DF_);
    float a[8];
#pragma unroll
    for (int e = 0; e < 8; ++e) a[e] = 0.f;
#pragma unroll
    for (int i = 0; i < 18; ++i)
#pragma unroll
      for (int e = 0; e < 4; ++e) { a[2 * e] += bflo(x[i][e]); a[2 * e + 1] += bfhi(x[i][e]); }
    float* red = (float*)lds;
    f32x4 a0 = {a[0], a[1], a[2], a[3]}, a1 = {a[4], a[5], a[6], a[7]};
    *(f32x4*)(red + (w * 4 + (l >> 4)) * 128 + (l & 15) * 8) = a0;
    *(f32x4*)(red + (w * 4 + (l >> 4)) * 128 + (l & 15) * 8 + 4) = a1;
    __syncthreads();
    if (tid < 128) {
      float s = 0.f;
#pragma unroll
      for (int r = 0; r < 32; ++r) s += red[r * 128 + tid];
      cs[(j < 36 ? 0 : DF_) + col0 + tid] = s;
    }
    return;
  }
  const int g = b, ch = w & 3, hf_ = w >> 2;
  const int gch = g * 4 + ch;
  {
    const char* src = (const char*)(kvt + (size_t)gch * S_) + (2 * l + hf_) * 16;
    char* dst = lds + ch * K1_UCH + hf_ * K1_UPLANE + 256;
    __builtin_amdgcn_global_load_lds((const GAS void*)src, (LAS void*)dst, 16, 0, 0);
    __builtin_amdgcn_global_load_lds((const GAS void*)(src + 2048), (LAS void*)(dst + 1024), 16, 0, 0);
  }
  const u16* krow = ssm + (size_t)gch * S_;
  u32x4 wv[2]; u32x2 wn[2];
#pragma unroll
  for (int it = 0; it < 2; ++it) {
    const int idx = hf_ * 128 + it * 64 + l;
    u32x4 z4 = {0u, 0u, 0u, 0u};
    wv[it] = (idx > 0) ? *(const u32x4*)(krow + (idx - 1) * 8) : z4;
    wn[it] = *(const u32x2*)(krow + idx * 8);
  }
  u32x4 wlast = {0u, 0u, 0u, 0u};
  if (hf_ == 1 && l == 1) wlast = *(const u32x4*)(krow + 255 * 8);
  u32x2 xr[4]; f32x2 st[4];
#pragma unroll
  for (int i = 0; i < 4; ++i) { xr[i] = *(const u32x2*)(xp + ((size_t)g * S_ + tid + 512 * i) * 4); st[i] = st1[tid + 512 * i]; }
  const u32x2 dv = *(const u32x2*)(ssmD + g * 4);
  if (l < 16) { u32x4 z4 = {0u, 0u, 0u, 0u}; *(u32x4*)(lds + ch * K1_UCH + hf_ * K1_UPLANE + l * 16) = z4; }
  char* tb = lds + K1_TOFF + ch * K1_TCH;
  auto put = [&](int pos  , u32x4 a, u32x2 n) {
    const u32 o0 = __builtin_amdgcn_alignbit(a[1], a[0], 16), o1 = __builtin_amdgcn_alignbit(a[2], a[1], 16), o2 = __builtin_amdgcn_alignbit(a[3], a[2], 16),
              o3 = __builtin_amdgcn_alignbit(n[0], a[3], 16), o4 = __builtin_amdgcn_alignbit(n[1], n[0], 16);
    u32x4 c1 = {o0, o1, o2, o3}, c2 = {a[1], a[2], a[3], n[0]}, c3 = {o1, o2, o3, o4};
    *(u32x4*)(tb + pos * 16) = a;
    *(u32x4*)(tb + K1_TCOPY + pos * 16) = c1;
    *(u32x4*)(tb + 2 * K1_TCOPY + pos * 16) = c2;
    *(u32x4*)(tb + 3 * K1_TCOPY + pos * 16) = c3;
  };
#pragma unroll
  for (int it = 0; it < 2; ++it) put(hf_ * 128 + it * 64 + l + 1, wv[it], wn[it]);
  if (hf_ == 1 && l < 2) { u32x2 z2 = {0u, 0u}; put(l ? 257 : 0, wlast, z2); }
  asm volatile("s_waitcnt vmcnt(0)" ::: "memory");
  __syncthreads();
  const int a_ = l & 15, q_ = l >> 4;
  const int cpy = (1 + a_) & 3;
  const u32 ldsb = (u32)(uintptr_t)(LAS char*)lds;
  u32 taddr = ldsb + K1_TOFF + ch * K1_TCH + cpy * K1_TCOPY + 2 * (1 + a_ + 8 * q_ - cpy) + hf_ * 256;
  u32 taddr2 = taddr + 8;
  asm volatile("" : "+v"(taddr2));
  const u32 uaddr = ldsb + ch * K1_UCH + ((q_ + 1) & 1) * K1_UPLANE + (16 + a_ - (q_ >> 1) - 8 * hf_ - 6) * 16;
  f32x4 acc[8];
#pragma unroll
  for (int n = 0; n < 8; ++n) acc[n] = (f32x4){0.f, 0.f, 0.f, 0.f};
#pragma unroll
  for (int e = 0; e < 4; ++e) {
    bf16x8 U[8], T[8];
#pragma unroll
    for (int j = 0; j < 8; ++j) U[j] = *(const LAS bf16x8*)(uintptr_t)(uaddr + (3 - e) * 32 + 256 * j);
#pragma unroll
    for (int m = 0; m < 8; ++m) {
      const u32x2 lo = *(volatile const LAS u32x2*)(uintptr_t)(taddr + 64 * e + 512 * m), hi = *(volatile const LAS u32x2*)(uintptr_t)(taddr + 8 + 64 * e + 512 * m);
      u32x4 t = {lo[0], lo[1], hi[0], hi[1]};
      T[m] = __builtin_bit_cast(bf16x8, t);
    }
#pragma unroll
    for (int m = 0; m < 8; ++m)
#pragma unroll
      for (int n = m; n < 8; ++n) acc[n] = __builtin_amdgcn_mfma_f32_16x16x32_bf16(T[m], U[n - m], acc[n], 0, 0, 0);
  }
  __syncthreads();
  {
    char* yb = lds + K1_TOFF + (hf_ * 4 + ch) * K1_YBUF + a_ * K1_YROW + q_ * 16;
#pragma unroll
    for (int n = 0; n < 8; ++n) *(f32x4*)(yb + n * 16 * K1_YROW) = acc[n];
  }
  __syncthreads();
  const float dd[4] = {bflo(dv[0]), bfhi(dv[0]), bflo(dv[1]), bfhi(dv[1])};
#pragma unroll
  for (int i = 0; i < 4; ++i) {
    const int s = tid + 512 * i;
    const char* yb = lds + K1_TOFF + s * 4 + (s >> 4) * 16;
    const int chi = s >> 3;
    const char* ub = lds + (chi & 1) * K1_UPLANE + (16 + (chi >> 1)) * 16 + (7 - (s & 7)) * 2;
    float hv[4];
#pragma unroll
    for (int c = 0; c < 4; ++c) {
      const float y = *(const float*)(yb + c * K1_YBUF) + *(const float*)(yb + (4 + c) * K1_YBUF);
      const float kv = __uint_as_float((u32)(*(const u16*)(ub + c * K1_UCH)) << 16);
      const float x = (c & 1) ? bfhi(xr[i][c >> 1]) : bflo(xr[i][c >> 1]);
      const float y2 = y + kv * dd[c];
      hv[c] = x + (x - st[i][0]) * st[i][1] * y2;
    }
    u32x2 o = {pack2(hv[0], hv[1]), pack2(hv[2], hv[3])};
    *(u32x2*)(hf + ((((size_t)(s >> 5) * 36 + (g >> 2)) * 64 + ((g >> 1) & 1) * 32 + (s & 31)) * 8 + (g & 1) * 4)) = o;
  }
}

DI float bf2f(u16 x) { return __uint_as_float((u32)x << 16); }
DI size_t hf_idx(int s, int d) { return (((size_t)(s >> 5) * 36 + (d >> 4)) * 64 + ((d >> 3) & 1) * 32 + (s & 31)) * 8 + (d & 7); }
DI int f_chunk(int f) { return f < 4096 ? (f >> 7) * 9 + ((f >> 4) & 7) : ((f - 4096) >> 4) * 9 + 8; }
DI size_t hh_idx(int s, int f) { const int r = f & 15; return (((size_t)(s >> 5) * 288 + f_chunk(f)) * 64 + ((r >> 2) & 1) * 32 + (s & 31)) * 8 + (r >> 3) * 4 + (r & 3); }
__global__ __launch_bounds__(256) void g1_naive(const u16* hf, const u16* wg, const u16* wu, const float* cs, u16* hh) {
  __shared__ float hs[D_]; __shared__ float red[2];
  const int s = blockIdx.x;
  for (int d = threadIdx.x; d < D_; d += 256) hs[d] = bf2f(hf[hf_idx(s, d)]);
  __syncthreads();
  if (threadIdx.x == 0) { float a = 0, b = 0; for (int d = 0; d < D_; ++d) { a += hs[d]; b += hs[d] * hs[d]; } float mu = a / D_; red[0] = mu; red[1] = rsqrtf(fmaxf(b / D_ - mu * mu, 0.f) + 1e-5f); }
  __syncthreads();
  for (int f = threadIdx.x; f < DF_; f += 256) {
    float g = 0, u = 0;
    for (int d = 0; d < D_; ++d) { g += hs[d] * bf2f(wg[(size_t)d * DF_ + f]); u += hs[d] * bf2f(wu[(size_t)d * DF_ + f]); }
    g = (g - red[0] * cs[f]) * red[1]; u = (u - red[0] * cs[DF_ + f]) * red[1];
    const float t = g * (1.f + 0.044715f * g * g) * 1.5957691216f;
    const float ge = g / (1.f + __expf(-t));
    hh[hh_idx(s, f)] = (u16)(pack2(ge * u, 0.f) & 0xffff);
  }
}
__global__ __launch_bounds__(192) void g2_naive(const u16* hf, const u16* hh, const u16* wd, u16* out) {
  __shared__ float hs[DF_];
  const int s = blockIdx.x;
  for (int f = threadIdx.x; f < DF_; f += 192) hs[f] = bf2f(hh[hh_idx(s, f)]);
  __syncthreads();
  for (int d = threadIdx.x; d < D_; d += 192) {
    float a = 0;
    for (int f = 0; f < DF_; ++f) a += hs[f] * bf2f(wd[(size_t)f * D_ + d]);
    out[(size_t)s * D_ + d] = (u16)(pack2(a + bf2f(hf[hf_idx(s, d)]), 0.f) & 0xffff);
  }
}

extern "C" void kernel_launch(void* const* d_in, const int* in_sizes, int n_in, void* d_out, int out_size, void* d_ws, size_t ws_size,
                              hipStream_t stream) {
  const u16* q = (const u16*)d_in[0]; const u16* k = (const u16*)d_in[1]; const u16* v = (const u16*)d_in[2];
  const u16* shk = (const u16*)d_in[3]; const u16* ssm = (const u16*)d_in[4]; const u16* ssmD = (const u16*)d_in[5];
  const u16* wg = (const u16*)d_in[7]; const u16* wu = (const u16*)d_in[8]; const u16* wd = (const u16*)d_in[9];
  char* ws = (char*)d_ws;
  u16* kvt = (u16*)(ws + OFF_KVT); u16* xp = (u16*)(ws + OFF_XP); u16* hf = (u16*)(ws + OFF_HF);
  f32x2* st1 = (f32x2*)(ws + OFF_ST1); float* cs = (float*)(ws + OFF_CS); u16* hh = (u16*)(ws + OFF_HH);
  (void)hipFuncSetAttribute((const void*)k1_conv, hipFuncAttributeMaxDynamicSharedMemorySize, K1_LDS);
  k0_prep<<<352, 256, 0, stream>>>(q, k, v, shk, kvt, xp, st1);
  k1_conv<<<216, 512, K1_LDS, stream>>>(ssm, ssmD, kvt, xp, st1, hf, wg, wu, cs);
  g1_naive<<<2048, 256, 0, stream>>>(hf, wg, wu, cs, hh);
  g2_naive<<<2048, 192, 0, stream>>>(hf, hh, wd, (u16*)d_out);
}
```

```cpp
#include <hip/hip_runtime.h>
#include <hip/hip_bf16.h>
#include <stdint.h>

typedef unsigned short u16;
typedef unsigned int u32;
typedef __attribute__((ext_vector_type(2))) unsigned u32x2;
typedef __attribute__((ext_vector_type(4))) unsigned u32x4;
typedef __attribute__((ext_vector_type(2))) float f32x2;
typedef __attribute__((ext_vector_type(4))) float f32x4;
typedef __attribute__((ext_vector_type(8))) __bf16 bf16x8;
typedef __attribute__((ext_vector_type(2))) __bf16 bf16x2;
typedef __attribute__((ext_vector_type(4))) short s16x4;
#define GAS __attribute__((address_space(1)))
#define LAS __attribute__((address_space(3)))
#define DI __device__ __forceinline__
typedef LAS char* ldsp;

constexpr int S_ = 2048, D_ = 576, DF_ = 4608;
constexpr size_t OFF_KVT = 0;
constexpr size_t OFF_XP  = OFF_KVT + (size_t)D_ * S_ * 2;
constexpr size_t OFF_HF  = OFF_XP + (size_t)D_ * S_ * 2;
constexpr size_t OFF_ST1 = OFF_HF + (size_t)D_ * S_ * 2;
constexpr size_t OFF_CS  = OFF_ST1 + 2048 * 8;
constexpr size_t OFF_FLG = OFF_CS + 4 * 2 * 4608 * 4;
constexpr size_t OFF_ST2 = OFF_FLG + 32768;
constexpr size_t OFF_HH  = OFF_ST2 + 131072;
constexpr size_t OFF_PART = OFF_HH + (size_t)S_ * DF_ * 2;

DI float bflo(u32 w) { return __uint_as_float(w << 16); }
DI float bfhi(u32 w) { return __uint_as_float(w & 0xffff0000u); }
DI u32 pack2(float a, float b) { bf16x2 p; p[0] = (__bf16)a; p[1] = (__bf16)b; return __builtin_bit_cast(u32, p); }

template <int I> struct IC { static constexpr int value = I; };
template <int B, int E, class F> DI void static_for(F&& f) { if constexpr (B < E) { f(IC<B>{}); static_for<B + 1, E>(f); } }
template <int N> DI void wait_vm() { asm volatile("s_waitcnt vmcnt(%0)" ::"n"(N) : "memory"); }
#define BARRIER() do { asm volatile("" ::: "memory"); __builtin_amdgcn_s_barrier(); asm volatile("" ::: "memory"); } while (0)
#define LDS_BARRIER() do { asm volatile("s_waitcnt lgkmcnt(0)" ::: "memory"); __builtin_amdgcn_s_barrier(); asm volatile("" ::: "memory"); } while (0)
#define SCHED0() __builtin_amdgcn_sched_barrier(0)
constexpr int cmin(int a, int b) { return a < b ? a : b; }

DI u32x4 tr_frag(ldsp a0, ldsp a1) {
  const s16x4 lo = __builtin_amdgcn_ds_read_tr16_b64_v4i16((LAS s16x4*)a0);
  const s16x4 hi = __builtin_amdgcn_ds_read_tr16_b64_v4i16((LAS s16x4*)a1);
  return __builtin_bit_cast(u32x4, __builtin_shufflevector(lo, hi, 0, 1, 2, 3, 4, 5, 6, 7));
}
DI f32x4 mfma16(const u32x4& a, const u32x4& b, const f32x4& c) {
  return __builtin_amdgcn_mfma_f32_16x16x32_bf16(__builtin_bit_cast(bf16x8, a), __builtin_bit_cast(bf16x8, b), c, 0, 0, 0);
}
template <bool FIRST> DI void mma(f32x4& c, const u32x4& a, const u32x4& b) {
  if constexpr (FIRST) asm volatile("v_mfma_f32_16x16x32_bf16 %0, %1, %2, 0" : "=&v"(c) : "v"(a), "v"(b));
  else asm volatile("v_mfma_f32_16x16x32_bf16 %0, %1, %2, %0" : "+v"(c) : "v"(a), "v"(b));
}
#define MFMA_DRAIN() asm volatile("s_nop 15" ::: "memory")
template <int OFF> DI void gload16(u32x4& dst, u32 voff, const char* sbase) {
  asm volatile("global_load_dwordx4 %0, %1, %2 offset:%3" : "=v"(dst) : "v"(voff), "s"(sbase), "n"(OFF) : "memory");
}
DI void gload8(u32x2& dst, u32 voff, const char* sbase) { asm volatile("global_load_dwordx2 %0, %1, %2" : "=v"(dst) : "v"(voff), "s"(sbase) : "memory"); }
template <int N> DI void wait_vm2(u32x4& a, u32x4& b) { asm volatile("s_waitcnt vmcnt(%2)" : "+v"(a), "+v"(b) : "n"(N) : "memory"); }
DI void dma16(const char* sbase, u32 voff, u32 lds_dst) {
  asm volatile("s_mov_b32 m0, %2\n\ts_nop 0\n\tglobal_load_lds_dwordx4 %0, %1" ::"v"(voff), "s"(sbase), "s"(lds_dst) : "memory");
}
template <int VAL> DI void dma16_if(int sel, const char* sbase, u32 voff, u32 lds_dst) {
  asm volatile("s_cmp_lg_u32 %3, %4\n\ts_cbranch_scc1 1f\n\ts_mov_b32 m0, %2\n\ts_nop 0\n\tglobal_load_lds_dwordx4 %0, %1\n1:" ::"v"(voff), "s"(sbase), "s"(lds_dst), "s"(sel), "n"(VAL) : "memory", "scc");
}
DI ldsp opaque(ldsp p) { asm volatile("" : "+v"(p)); return p; }
DI u32 opq(u32 p) { asm volatile("" : "+v"(p)); return p; }
DI void pin(u32& p) { asm volatile("" : "+v"(p)); }
DI void pinf(float& p) { asm volatile("" : "+v"(p)); }
DI float hide(float p) { asm("" : "+v"(p)); return p; }
DI float swap16_sum(float x) { const auto r = __builtin_amdgcn_permlane16_swap(__float_as_uint(x), __float_as_uint(x), false, false); return __uint_as_float(r[0]) + __uint_as_float(r[1]); }
DI float swap32_sum(float x) { const auto r = __builtin_amdgcn_permlane32_swap(__float_as_uint(x), __float_as_uint(x), false, false); return __uint_as_float(r[0]) + __uint_as_float(r[1]); }
DI void stat_acc(int skip, u32 v, float& s1, float& s2) {
  float x, y;
  asm volatile("s_cmp_gt_u32 %6, 5\n\ts_cbranch_scc1 1f\n\tv_lshlrev_b32_e32 %0, 16, %4\n\tv_and_b32_e32 %1, %5, %4\n\tv_add_f32_e32 %2, %2, %0\n\tv_fmac_f32_e32 %3, %0, %0\n\tv_add_f32_e32 %2, %2, %1\n\tv_fmac_f32_e32 %3, %1, %1\n1:"
               : "=&v"(x), "=&v"(y), "+v"(s1), "+v"(s2) : "v"(v), "s"(0xffff0000u), "s"(skip) : "scc");
}
DI void ld_sc1_x4(u32x4& d, u32 voff, const char* sb) { asm volatile("global_load_dwordx4 %0, %1, %2 sc1" : "=v"(d) : "v"(voff), "s"(sb) : "memory"); }
template <int OFF> DI void ld_sc1_x2(u32x2& d, u32 voff, const char* sb) { asm volatile("global_load_dwordx2 %0, %1, %2 offset:%3 sc1" : "=v"(d) : "v"(voff), "s"(sb), "n"(OFF) : "memory"); }
DI void wait0_x6(u32x4& a, u32x2& b) { asm volatile("s_waitcnt vmcnt(0)" : "+v"(a), "+v"(b)::"memory"); }
DI void st_sc1_x4(char* sb, u32 voff, const u32x4& v) { asm volatile("global_store_dwordx4 %0, %1, %2 sc1\n\ts_nop 1" ::"v"(voff), "v"(v), "s"(sb) : "memory"); }
DI void gstore16(char* sb, u32 voff, const u32x4& v) { asm volatile("global_store_dwordx4 %0, %1, %2\n\ts_nop 1" ::"v"(voff), "v"(v), "s"(sb) : "memory"); }
DI void gstore8(char* sb, u32 voff, const u32x2& v) { asm volatile("global_store_dwordx2 %0, %1, %2" ::"v"(voff), "v"(v), "s"(sb) : "memory"); }
DI void st_sc1_x2_if(int skip, const char* sb, u32 voff, const u32x2& v) {
  asm volatile("s_cmp_gt_u32 %3, 5\n\ts_cbranch_scc1 1f\n\tglobal_store_dwordx2 %0, %1, %2 sc1\n1:" ::"v"(voff), "v"(v), "s"(sb), "s"(skip) : "memory", "scc");
}
DI void st_sc1_x2(char* sb, u32 voff, const u32x2& v) { asm volatile("global_store_dwordx2 %0, %1, %2 sc1" ::"v"(voff), "v"(v), "s"(sb) : "memory"); }

#define STR(i)
#define ST(i)
#define ST_DECL
#define ST_FLUSH(dbg, wv)
#define ST_DRAIN()

constexpr int K0_GRID = 288 + 64;
__global__ __launch_bounds__(256) void k0_prep(const u16* __restrict__ q, const u16* __restrict__ k, const u16* __restrict__ v,
                                               const u16* __restrict__ shk, u16* __restrict__ kvt, u16* __restrict__ xp,
                                               f32x2* __restrict__ st1, u32x4* __restrict__ flg, const u16* __restrict__ ssm, u32* dbg) {
  ST_DECL; ST(0);
  const int b = blockIdx.x, tid = threadIdx.x, w = tid >> 6, l = tid & 63;
  if (b < 288) {
    const int tb = b / 9, cb = b - tb * 9;
    const int s0 = tb * 64 + w * 16 + (l >> 4) * 4;
    const int c0 = cb * 64 + (l & 15) * 4;
    u32x2 kr[7], vr[4], qr[4];
#pragma unroll
    for (int i = 0; i < 7; ++i) {
      const int s = s0 - 3 + i;
      u32x2 z = {0u, 0u};
      kr[i] = (s >= 0) ? *(const u32x2*)(k + (size_t)s * D_ + c0) : z;
    }
#pragma unroll
    for (int i = 0; i < 4; ++i) { vr[i] = *(const u32x2*)(v + (size_t)(s0 + i) * D_ + c0); qr[i] = *(const u32x2*)(q + (size_t)(s0 + i) * D_ + c0); }
    const u32x4 w0 = *(const u32x4*)(shk + c0 * 4), w1 = *(const u32x4*)(shk + c0 * 4 + 8);
    const u32x4 pf0 = ((const u32x4*)ssm)[b * 512 + tid], pf1 = ((const u32x4*)ssm)[b * 512 + 256 + tid];
#pragma unroll
    for (int c = 0; c < 4; ++c) {
      const u32 wa = (c < 2) ? w0[2 * (c & 1)] : w1[2 * (c & 1)], wb = (c < 2) ? w0[2 * (c & 1) + 1] : w1[2 * (c & 1) + 1];
      const float f0 = bflo(wa), f1 = bfhi(wa), f2 = bflo(wb), f3 = bfhi(wb);
      float kk[7], kvv[4];
#pragma unroll
      for (int i = 0; i < 7; ++i) kk[i] = (c & 1) ? bfhi(kr[i][c >> 1]) : bflo(kr[i][c >> 1]);
#pragma unroll
      for (int i = 0; i < 4; ++i) {
        const float ks = kk[i + 3] * f0 + kk[i + 2] * f1 + kk[i + 1] * f2 + kk[i] * f3;
        const float vv = (c & 1) ? bfhi(vr[i][c >> 1]) : bflo(vr[i][c >> 1]);
        kvv[i] = ks * vv;
      }
      u32x2 o = {pack2(kvv[3], kvv[2]), pack2(kvv[1], kvv[0])};
      u16* dst = kvt + (size_t)(c0 + c) * S_ + (s0 >> 3) * 8 + ((s0 & 4) ? 0 : 4);
      *(u32x2*)dst = o;
    }
    u32x4 xa = {qr[0][0], qr[0][1], qr[1][0], qr[1][1]}, xb = {qr[2][0], qr[2][1], qr[3][0], qr[3][1]};
    u16* xdst = xp + ((size_t)(c0 >> 2) * S_ + s0) * 4;
    *(u32x4*)xdst = xa; *(u32x4*)(xdst + 8) = xb;
    if (b < 40) {
      const u32 f = b < 8 ? 0u : 0xffffffffu;
      u32x4 z = {f, f, f, f};
      flg[b * 256 + tid] = z;
    }
    asm volatile("" ::"v"(pf0), "v"(pf1));
  } else {
    const int row = (b - 288) * 32 + w * 8 + (l >> 3);
    const u16* p = q + (size_t)row * D_ + (l & 7) * 8;
    u32x4 x[9];
#pragma unroll
    for (int i = 0; i < 9; ++i) x[i] = *(const u32x4*)(p + i * 64);
    float sum = 0.f;
#pragma unroll
    for (int i = 0; i < 9; ++i)
#pragma unroll
      for (int j = 0; j < 4; ++j) sum += bflo(x[i][j]) + bfhi(x[i][j]);
    sum += __shfl_xor(sum, 1); sum += __shfl_xor(sum, 2); sum += __shfl_xor(sum, 4);
    const float mu = sum * (1.f / D_);
    float sq = 0.f;
#pragma unroll
    for (int i = 0; i < 9; ++i)
#pragma unroll
      for (int j = 0; j < 4; ++j) { const float a = bflo(x[i][j]) - mu, c = bfhi(x[i][j]) - mu; sq += a * a + c * c; }
    sq += __shfl_xor(sq, 1); sq += __shfl_xor(sq, 2); sq += __shfl_xor(sq, 4);
    if ((l & 7) == 0) { f32x2 o = {mu, rsqrtf(sq * (1.f / D_) + 1e-5f)}; st1[row] = o; }
  }
  ST(1); ST_DRAIN(); ST(2);
  ST_FLUSH(dbg, b * 4 + w);
}

constexpr int K1_UPLANE = 2304, K1_UCH = 4608, K1_TOFF = 4 * K1_UCH, K1_TCOPY = 4160, K1_TCH = 4 * K1_TCOPY;
constexpr int K1_YOFF = K1_TOFF + 4 * K1_TCH, K1_YBUF = 8192;
constexpr int K1_LDS = K1_YOFF + 8 * K1_YBUF;
constexpr int K1_GRID = 256, K1_SIDE = K1_GRID - 144;

__global__ __launch_bounds__(512) void k1_conv(const u16* __restrict__ ssm, const u16* __restrict__ ssmD, const u16* __restrict__ kvt,
                                               const u16* __restrict__ xp, const f32x2* __restrict__ st1, u16* __restrict__ hf,
                                               const u16* __restrict__ wg, const u16* __restrict__ wu, float* __restrict__ cs, u32* dbg) {
  extern __shared__ __attribute__((aligned(128))) char lds[];
  ST_DECL; ST(0);
  const int tid = threadIdx.x, l = tid & 63;
  const int w = __builtin_amdgcn_readfirstlane(tid >> 6);
  const int b = blockIdx.x;
  if (b >= 144) {
    const int sl = (b - 144) + K1_SIDE * w;
    if (sl < 576) {
      const int mat = sl & 1, qr = (sl >> 1) & 3, cb = sl >> 3;
      const u16* p = (mat ? wu : wg) + (size_t)(qr * 144 + (l >> 3)) * DF_ + cb * 64 + (l & 7) * 8;
      u32x4 x[18];
#pragma unroll
      for (int i = 0; i < 18; ++i) x[i] = *(const u32x4*)(p + (size_t)i * 8 * DF_);
      float a[8];
#pragma unroll
      for (int e = 0; e < 8; ++e) a[e] = 0.f;
#pragma unroll
      for (int i = 0; i < 18; ++i)
#pragma unroll
        for (int e = 0; e < 4; ++e) { a[2 * e] += bflo(x[i][e]); a[2 * e + 1] += bfhi(x[i][e]); }
#pragma unroll
      for (int e = 0; e < 8; ++e) { a[e] += __shfl_xor(a[e], 8); a[e] = swap32_sum(swap16_sum(a[e])); }
      if (l < 8) {
        float* d = cs + qr * 9216 + (cb * 32 + l * 4) * 4 + mat * 2;
#pragma unroll
        for (int e = 0; e < 4; ++e) { f32x2 o = {a[2 * e], a[2 * e + 1]}; *(f32x2*)(d + e * 4) = o; }
      }
    }
    ST(6);
    ST_FLUSH(dbg, b * 8 + w);
    return;
  }
  const int g = b, ch = w & 3, hf_ = w >> 2;
  const int gch = g * 4 + ch;
  const ldsp LP = (ldsp)lds;
  const u32 L0 = (u32)(uintptr_t)LP;
  {
    const char* src = (const char*)(kvt + (size_t)gch * S_);
    const u32 vo = (2 * l + hf_) * 16;
    const u32 dst = L0 + ch * K1_UCH + hf_ * K1_UPLANE + 256;
    dma16(src, vo, dst);
    dma16(src + 2048, vo, dst + 1024);
  }
  const u16* krow = ssm + (size_t)gch * S_;
  u32x4 wv[2]; u32x2 wn[2];
#pragma unroll
  for (int it = 0; it < 2; ++it) {
    const int idx = hf_ * 128 + it * 64 + l;
    wv[it] = *(const u32x4*)(krow + (idx > 0 ? idx - 1 : 0) * 8);
    wn[it] = *(const u32x2*)(krow + idx * 8);
  }
  const u32x4 wl = *(const u32x4*)(krow + 255 * 8);
  const u32 dv1 = ssmD[gch];
  SCHED0();
  u32x2 xr[4]; f32x2 st[4];
#pragma unroll
  for (int i = 0; i < 4; ++i) { xr[i] = *(const u32x2*)(xp + ((size_t)g * S_ + tid + 512 * i) * 4); st[i] = st1[tid + 512 * i]; }
  SCHED0();
  const u32x4 z4 = {0u, 0u, 0u, 0u};
  if (l < 16) *(LAS u32x4*)(LP + (ch * K1_UCH + hf_ * K1_UPLANE + l * 16)) = z4;
  const ldsp tb = LP + (K1_TOFF + ch * K1_TCH);
  auto put = [&](int pos  , u32x4 a, u32x2 n) {
    const u32 o0 = __builtin_amdgcn_alignbit(a[1], a[0], 16), o1 = __builtin_amdgcn_alignbit(a[2], a[1], 16), o2 = __builtin_amdgcn_alignbit(a[3], a[2], 16),
              o3 = __builtin_amdgcn_alignbit(n[0], a[3], 16), o4 = __builtin_amdgcn_alignbit(n[1], n[0], 16);
    u32x4 c1 = {o0, o1, o2, o3}, c2 = {a[1], a[2], a[3], n[0]}, c3 = {o1, o2, o3, o4};
    *(LAS u32x4*)(tb + pos * 16) = a;
    *(LAS u32x4*)(tb + (K1_TCOPY + pos * 16)) = c1;
    *(LAS u32x4*)(tb + (2 * K1_TCOPY + pos * 16)) = c2;
    *(LAS u32x4*)(tb + (3 * K1_TCOPY + pos * 16)) = c3;
  };
  if (hf_ == 0 && l == 0) wv[0] = z4;
#pragma unroll
  for (int it = 0; it < 2; ++it) put(hf_ * 128 + it * 64 + l + 1, wv[it], wn[it]);
  if (hf_ == 1 && l < 2) { u32x2 z2 = {0u, 0u}; put(l ? 257 : 0, l ? wl : z4, z2); }
  ST(1);
  wait_vm<8>();
  LDS_BARRIER();
  ST(2);
  const int a_ = l & 15, q_ = l >> 4;
  const int cpy = (1 + a_) & 3;
  const ldsp taddr = LP + (K1_TOFF + ch * K1_TCH + cpy * K1_TCOPY + 2 * (1 + a_ + 8 * q_ - cpy) + hf_ * 256);
  const ldsp uaddr = LP + (ch * K1_UCH + ((q_ + 1) & 1) * K1_UPLANE + (16 + a_ - (q_ >> 1) - 8 * hf_ - 6) * 16);
  f32x4 acc[8];
  u32x4 dg;
  {
    const int j = 15 - a_ - 8 * q_;
    const u32 dval = hf_ ? 0u : dv1;
#pragma unroll
    for (int e = 0; e < 4; ++e) dg[e] = (j == 2 * e) ? dval : ((j == 2 * e + 1) ? (dval << 16) : 0u);
  }
  u32x4 U[2][8], T[8];
  auto ldU = [&](int e, int j) { return *(const LAS u32x4*)(uaddr + ((3 - e) * 32 + 256 * j)); };
  auto ldT = [&](int e, int m) {
    const u32x2 lo = *(volatile const LAS u32x2*)(taddr + (64 * e + 512 * m)), hi = *(volatile const LAS u32x2*)(taddr + (8 + 64 * e + 512 * m));
    u32x4 t = {lo[0], lo[1], hi[0], hi[1]};
    return t;
  };
#pragma unroll
  for (int j = 0; j < 8; ++j) U[0][j] = ldU(0, j);
  T[0] = ldT(0, 0); T[1] = ldT(0, 1);
  SCHED0();
  const f32x4 zero4 = {0.f, 0.f, 0.f, 0.f};
#pragma unroll
  for (int n = 0; n < 8; ++n) acc[n] = mfma16(dg, U[0][n], zero4);
  SCHED0();
  static_for<0, 32>([&](auto ic) {
    constexpr int e = ic.value >> 3, m = ic.value & 7;
    constexpr int nx = ic.value + 2;
    if constexpr (nx < 32) T[nx & 7] = ldT(nx >> 3, nx & 7);
    if constexpr (e < 3) U[(e + 1) & 1][m] = ldU(e + 1, m);
#pragma unroll
    for (int n = m; n < 8; ++n) acc[n] = mfma16(T[m], U[e & 1][n - m], acc[n]);
    SCHED0();
  });
  ST(3);
  {
    const ldsp yb = LP + (K1_YOFF + (hf_ * 4 + ch) * K1_YBUF + ((4 * a_ + q_) ^ ((a_ >> 1) & 3)) * 16);
#pragma unroll
    for (int n = 0; n < 8; ++n) *(LAS f32x4*)(yb + n * 1024) = acc[n];
  }
  LDS_BARRIER();
  ST(4);
  {
    const int sl = tid >> 2;
    const ldsp yb = LP + (K1_YOFF + (sl ^ ((sl >> 3) & 3)) * 16 + (tid & 3) * 4);
    float y[4][8];
#pragma unroll
    for (int i = 0; i < 4; ++i)
#pragma unroll
      for (int c = 0; c < 8; ++c) y[i][c] = *(const LAS float*)(yb + (i * 2048 + c * K1_YBUF));
    u16* hp = hf + ((((size_t)(tid >> 4) * 18 + (g >> 3)) * 64 + ((g >> 1) & 3) * 16 + (tid & 15)) * 8 + (g & 1) * 4);
#pragma unroll
    for (int i = 0; i < 4; ++i) {
      float hv[4];
#pragma unroll
      for (int c = 0; c < 4; ++c) {
        const float x = (c & 1) ? bfhi(xr[i][c >> 1]) : bflo(xr[i][c >> 1]);
        hv[c] = x + (x - st[i][0]) * st[i][1] * (y[i][c] + y[i][4 + c]);
      }
      u32x2 o = {pack2(hv[0], hv[1]), pack2(hv[2], hv[3])};
      *(u32x2*)(hp + (size_t)i * 32 * 18 * 512) = o;
    }
  }
  ST(5); ST_DRAIN(); ST(6);
  ST_FLUSH(dbg, b * 8 + w);
}

constexpr int G1_QE = 147456;
constexpr int G1_CS = G1_QE + 8192;
constexpr int G1_PF = G1_CS + 2048;
constexpr int G1_CP = G1_PF + 1024;
constexpr int G1_LDS = G1_CP + 5120;
#define G1_D 4
constexpr int g1_q_issue(int half, int r) { return half == 0 ? 8 + 6 * r : (r < 3 ? 11 + 6 * r : -1); }
constexpr int g1_b_issue(int half, int j) { return (j < 14 ? 29 + 3 * j : 73 + 3 * (j - 14)) + half; }
constexpr int G1_E0 = 8;
constexpr int g1_epi_start(int i) { return G1_E0 + 26 * i; }
constexpr bool g1_stores(int i) { return i == 1 || i == 3 || i == 4 || i == 6; }
constexpr int g1_ops_after(int half, int t0, int t1) {
  int n = 0;
  for (int r = 0; r < 4; ++r) { const int t = g1_q_issue(half, r); if (t > t0 && t <= t1) ++n; }
  for (int j = 0; j < 18; ++j) { const int t = g1_b_issue(half, j); if (t > t0 && t <= t1) ++n; }
  for (int i = 0; i < 8; ++i) if (g1_stores(i)) for (int k = 3; k <= 4; ++k) { const int t = g1_epi_start(i) + 6 * k; if (t > t0 && t <= t1) ++n; }
  return n;
}
constexpr int g1_wait_q(int r, int tw) { return cmin(g1_ops_after(0, g1_q_issue(0, r), tw - 1), g1_ops_after(1, g1_q_issue(1, r < 3 ? r : 2), tw - 1)); }
constexpr int g1_wait_b(int j, int tw) { return cmin(g1_ops_after(0, g1_b_issue(0, j), tw - 1), g1_ops_after(1, g1_b_issue(1, j), tw - 1)); }
constexpr float GC = -2.3022081983f;
struct Acc { f32x4 a[2][2]; };

__global__ __launch_bounds__(512, 2) void g1_gemm(const u16* __restrict__ hf, const u16* __restrict__ wg, const u16* __restrict__ wu,
                                                  const float* __restrict__ cs, u16* __restrict__ hh, char* __restrict__ st2,
                                                  const u16* __restrict__ wd, u32* dbg) {
  extern __shared__ __attribute__((aligned(128))) char lds[];
  ST_DECL; ST(0);
  const int tid = threadIdx.x, l = tid & 63;
  const int w = __builtin_amdgcn_readfirstlane(tid >> 6);
  const int b = blockIdx.x;
  const int fg = (b & 3) * 8 + ((b >> 3) & 7), tg = ((b >> 2) & 1) * 4 + (b >> 6);
  const int tt = tg * 8 + w;
  const int rotg = fg < 6 ? 3 * fg : 0;
  auto rot = [&](int g) { const int x = g + rotg; return x >= 18 ? x - 18 : x; };
  const ldsp LP = (ldsp)lds;
  const u32 L0 = (u32)(uintptr_t)LP;
  constexpr size_t RS = DF_ * 2;
  const char* wmat = (const char*)((w & 1) ? wu : wg);
  const int wblk = w & 1, whr = (w >> 1) & 1, whalf = w >> 2;
  const u32 ab_voff = (u32)((16 * whalf + 8 * whr + (l >> 3)) * RS + ((((l & 7) >> 1) ^ (((l >> 4) & 1) | (whr << 1))) * 32) + (l & 1) * 16);
  const char* a_src = wmat + fg * 256;
  const u32 ab_dst = L0 + whalf * 4096 + wblk * 2048 + whr * 1024;
  const char* q_src = wmat + 8192 + fg * 32;
  auto mk_qvoff = [&](u32 l2) { return (u32)(((l2 >> 1) ^ ((l2 >> 2) & 4)) * RS + (l2 & 1) * 16); };
  u32x4 h[2][18];
  const char* hsrc = (const char*)hf + (size_t)tt * 36864;
  const u32 lo16 = l * 16;
  auto issue_dma = [&](auto ic) { constexpr int g = ic.value; dma16(a_src + (u32)rot(g) * (u32)(32 * RS), ab_voff, ab_dst + g * 8192); };
  auto issue_h = [&](auto ic, auto tc) { constexpr int g = ic.value, tl = tc.value; gload16<0>(h[tl][g], lo16, hsrc + rot(g) * 1024 + tl * 18432); };
  static_for<0, G1_D>([&](auto ic) { issue_dma(ic); issue_h(ic, IC<0>{}); issue_h(ic, IC<1>{}); });
  ST(1);
  const int rq = l >> 4, rr = (l >> 2) & 3, rp = l & 3;
  const u32 rb0 = L0 + (rq >> 1) * 4096 + (rq & 1) * 1024 + rr * 128 + rp * 8 + (((rr >> 1) | ((rq & 1) << 1)) * 32);
  auto abase = [&](int c, int off) { return opaque((ldsp)(uintptr_t)((opq(rb0) ^ (u32)(c * 32)) + (u32)off)); };
  Acc acc[4];
  const char* stb = st2 + (size_t)tt * 2048;
  u32x4 sg4; u32x2 sg2;
  {
    float s1[2] = {0.f, 0.f}, s2[2] = {0.f, 0.f};
    ldsp pb[3];
    u32x4 pf[2][3];
    auto fetch1 = [&](auto ic) {
      constexpr int s = ic.value;
      if constexpr ((s & 15) == 0) {
#pragma unroll
        for (int c = 0; c < 3; ++c) pb[c] = abase(c, s * 4096);
      }
      constexpr int o = ((s & 15) >> 1) * 8192 + (s & 1) * 2048;
#pragma unroll
      for (int c = 0; c < 3; ++c) pf[s & 1][c] = tr_frag(pb[c] + o, pb[c] + (o + 512));
    };
    wait_vm2<3 * (G1_D - 1)>(h[0][0], h[1][0]);
    BARRIER();
    fetch1(IC<0>{});
    static_for<0, 36>([&](auto ic) {
      constexpr int s = ic.value, g = s >> 1, mt = s & 1;
      constexpr int SQ = 2 * (18 - G1_D);
      auto mm = [&](int c) {
#pragma unroll
        for (int tl = 0; tl < 2; ++tl) mma<g == 0>(acc[c].a[mt][tl], pf[s & 1][c], h[tl][g]);
      };
      if constexpr (mt == 1 && s < 35) {
        constexpr int last = cmin(g + G1_D, 18);
        constexpr int extra = (g + G1_D < 18) ? 2 : 3;
        wait_vm2<3 * (last - (g + 2)) + extra>(h[0][g + 1], h[1][g + 1]);
        BARRIER();
      }
      if constexpr (s < 35) fetch1(IC<s + 1>{});
      SCHED0();
      mm(0);
      if constexpr (g + G1_D < 18) { if constexpr (mt == 0) issue_dma(IC<g + G1_D>{}); else issue_h(IC<g + G1_D>{}, IC<1>{}); }
      else if constexpr (s == SQ) {
        const int ii = min(tid, 287), qd = ii / 72, r = ii - qd * 72;
        dma16_if<0>((w + 3) >> 3, (const char*)cs, (u32)(qd * 2304 + (r < 64 ? fg * 64 + r : 2048 + fg * 8 + (r - 64))) * 16u, L0 + G1_CP + w * 1024);
        dma16(q_src + (u32)rot(w >> 1) * (u32)(32 * RS), mk_qvoff(l), L0 + G1_QE + w * 1024);
      }
      if constexpr (s < 12) stat_acc(fg, h[(s >> 1) & 1][s >> 2][(s & 1) * 2], s1[(s >> 1) & 1], s2[(s >> 1) & 1]);
      SCHED0();
      mm(1);
      if constexpr (g + G1_D < 18 && mt == 0) issue_h(IC<g + G1_D>{}, IC<0>{});
      if constexpr (s == SQ) { const u32 so = ((rq >> 1) * 16 + (l & 15)) * 64 + (rq & 1) * 24; ld_sc1_x4(sg4, so, stb); ld_sc1_x2<16>(sg2, so, stb); }
      if constexpr (s < 12) stat_acc(fg, h[(s >> 1) & 1][s >> 2][(s & 1) * 2 + 1], s1[(s >> 1) & 1], s2[(s >> 1) & 1]);
      SCHED0();
      mm(2);
      if constexpr (s == 12) {
        const float a0 = swap32_sum(swap16_sum(s1[0])), c0 = swap32_sum(swap16_sum(s2[0]));
        const float a1 = swap32_sum(swap16_sum(s1[1])), c1 = swap32_sum(swap16_sum(s2[1]));
        u32x2 gr = {__float_as_uint((l & 16) ? a1 : a0), __float_as_uint((l & 16) ? c1 : c0)};
        st_sc1_x2_if(fg, stb + fg * 8, (u32)((l & 31) * 64), gr);
      }
      SCHED0();
      if constexpr (s % 6 == 5) { ST(2 + s / 6); }
    });
  }
  STR(28);
  u32x4 wf[3];
  ldsp fb = nullptr, fb2 = nullptr;
  auto fetch = [&](auto ic) {
    constexpr int i = ic.value, p = i / 36, s = i % 36;
    if constexpr (p != 1) {
      constexpr int c = (p == 0) ? 3 : p - 2;
      constexpr int pos = (p == 0) ? s : (s + 8) % 36;
      if constexpr (s == 0 || (pos & 15) == 0) fb = abase(c, (pos & ~15) * 4096);
      constexpr int o = ((pos & 15) >> 1) * 8192 + (pos & 1) * 2048;
      wf[i % 3] = tr_frag(fb + o, fb + (o + 512));
    } else {
      if constexpr (s == 0 || s == 8) {
        const u32 l2 = opq(tid);
        const int qo = (l2 >> 4) & 1;
        const int qb = (8 * ((l2 >> 4) & 3) + ((l2 >> 2) & 3)) * 32 + (l2 & 3) * 8 + (s == 0 ? G1_QE : 0);
        fb = opaque(LP + (qb + qo * 128)); fb2 = opaque(LP + (qb + (1 - qo) * 128));
      }
      constexpr int o = (s < 8 ? s : s - 8) * 1024;
      wf[i % 3] = tr_frag(fb + o, fb2 + o);
    }
  };
  fetch(IC<0>{}); fetch(IC<1>{});
  SCHED0();
  wait0_x6(sg4, sg2);
#pragma unroll 1
  for (int it = 0; it < (1 << 16); ++it) {
    const bool bad = (sg4[0] & sg4[1]) == 0xffffffffu || (sg4[2] & sg4[3]) == 0xffffffffu || (sg2[0] & sg2[1]) == 0xffffffffu;
    if (!__any(bad)) break;
    __builtin_amdgcn_s_sleep(2);
    const u32 so = ((rq >> 1) * 16 + (l & 15)) * 64 + (rq & 1) * 24;
    ld_sc1_x4(sg4, so, stb); ld_sc1_x2<16>(sg2, so, stb);
    wait0_x6(sg4, sg2);
  }
  float nmu[2], k1[2], k2[2], kd[2];
  {
    const float t1 = swap16_sum(__uint_as_float(sg4[0]) + __uint_as_float(sg4[2]) + __uint_as_float(sg2[0]));
    const float t2 = swap16_sum(__uint_as_float(sg4[1]) + __uint_as_float(sg4[3]) + __uint_as_float(sg2[1]));
    const float mu = hide(t1 * (1.f / D_));
    const float var = fmaxf(t2 * (1.f / D_) - mu * mu, 0.f) + 1e-5f;
    const auto m2 = __builtin_amdgcn_permlane32_swap(__float_as_uint(-mu), __float_as_uint(-mu), false, false);
    const auto v2 = __builtin_amdgcn_permlane32_swap(__float_as_uint(var), __float_as_uint(var), false, false);
#pragma unroll
    for (int tl = 0; tl < 2; ++tl) {
      kd[tl] = __uint_as_float(v2[tl]);
      const float r = __builtin_amdgcn_rsqf(kd[tl]);
      nmu[tl] = __uint_as_float(m2[tl]);
      k1[tl] = hide(GC * r);
      k2[tl] = hide(k1[tl] * 0.044715f * r * r);
    }
  }
  char* hdst = (char*)hh + ((size_t)tt * 288 + (fg >> 1) * 9 + (fg & 1) * 4) * 1024;
  const ldsp csb = opaque(LP + (G1_CS + rq * 32));
  f32x4 cc;
  auto ldcs = [&](int lc, int hfx) { cc = *(const LAS f32x4*)(csb + (lc * 128 + hfx * 16)); };
  u32x4 eo[2];
  u32x2 eq[2];
  float m0, m1, tu0, tu1, y0, y1;
  auto emit = [&](int lc, int i, u32 pk) {
    const int tl = i & 1, hx = i >> 1;
    if (lc == 8) {
      eq[tl][hx] = pk;
      if (hx) gstore8(hdst + (size_t)tl * 147456 + (8 - (fg & 1) * 4) * 1024 + (fg & 1) * 8, lo16, eq[tl]);
    } else {
      eo[tl][(lc & 1) * 2 + hx] = pk;
      if ((lc & 1) && hx) gstore16(hdst + (size_t)tl * 147456 + (lc >> 1) * 1024, lo16, eo[tl]);
    }
  };
  auto epi = [&](const Acc& a, int lc, int st, int nlc) {
    const int i = st / 6, sg = st % 6, tl = i & 1, e = (i >> 1) * 2;
    if (sg == 0) {
      if (i > 0) { u32 pk = pack2(tu0, tu1 * y1); pin(pk); emit(lc, i - 1, pk); }
      if (i < 4) { m0 = fmaf(nmu[tl], cc[0], a.a[0][tl][e]); m1 = fmaf(nmu[tl], cc[1], a.a[0][tl][e + 1]); pinf(m0); pinf(m1); }
    } else if (sg == 1) {
      tu0 = fmaf(nmu[tl], cc[2], a.a[1][tl][e]); tu1 = fmaf(nmu[tl], cc[3], a.a[1][tl][e + 1]);
      y0 = hide(m0 * m0); y1 = m1 * m1;
      pinf(tu0); pinf(tu1); pinf(y0); pinf(y1);
      if (i == 1) ldcs(lc, 1);
      if (i == 3) ldcs(nlc, 0);
    } else if (sg == 2) {
      y0 = hide(fmaf(y0, k2[tl], k1[tl])); y1 = fmaf(y1, k2[tl], k1[tl]); tu0 = hide(tu0 * m0); tu1 = tu1 * m1;
      pinf(y0); pinf(y1); pinf(tu0); pinf(tu1);
    } else if (sg == 3) {
      y0 = hide(y0 * m0); y1 = y1 * m1; y0 = __builtin_amdgcn_exp2f(y0);
      pinf(y0); pinf(y1);
    } else if (sg == 4) {
      y1 = __builtin_amdgcn_exp2f(y1); y0 = __builtin_amdgcn_rcpf(fmaf(y0, kd[tl], kd[tl]));
      pinf(y0); pinf(y1);
    } else {
      y1 = __builtin_amdgcn_rcpf(fmaf(y1, kd[tl], kd[tl])); tu0 = tu0 * y0;
      pinf(y1); pinf(tu0);
    }
  };
  u32 q_voff = 0;
  static_for<0, 216>([&](auto ic) {
    constexpr int t = ic.value, p = t / 36, s = t % 36;
    constexpr int slot = (p + 3) & 3;
    if constexpr (t + 2 < 216) fetch(IC<t + 2>{});
#pragma unroll
    for (int tl = 0; tl < 2; ++tl) mma<(s < 2)>(acc[slot].a[s & 1][tl], wf[t % 3], h[tl][s >> 1]);
    if constexpr (t == 2) {
      const u32 pi = min(opq(tid), 71u);
      const ldsp cp = LP + (G1_CP + pi * 16);
      const f32x4 r = (*(const LAS f32x4*)cp + *(const LAS f32x4*)(cp + 1152)) + (*(const LAS f32x4*)(cp + 2304) + *(const LAS f32x4*)(cp + 3456));
      *(LAS f32x4*)(LP + (G1_CS + pi * 16)) = r;
    }
    if constexpr (t == G1_E0 - 1) ldcs(0, 0);
    if constexpr (t >= G1_E0) {
      constexpr int ci = (t - G1_E0) / 26, st = (t - G1_E0) % 26;
      if constexpr (ci < 8 && st <= 24) {
        constexpr int lc = ci < 4 ? ci : (ci == 4 ? 8 : ci - 1);
        constexpr int nlc = ci < 3 ? ci + 1 : (ci == 3 ? 8 : ci);
        epi(acc[ci & 3], lc, st, nlc);
      }
    }
    if constexpr (t == 1 || t == 28 || t == 72) { SCHED0(); BARRIER(); }
    if constexpr (t == 6) { SCHED0(); LDS_BARRIER(); }
    if constexpr (t == 7) { SCHED0(); BARRIER(); }
    if constexpr (t == 41) { wait_vm<g1_wait_q(1, 41)>(); BARRIER(); }
    if constexpr (t == 57) { wait_vm<g1_wait_q(3, 57)>(); BARRIER(); }
    if constexpr (t == 69) { wait_vm<g1_wait_b(9, 69)>(); BARRIER(); }
    if constexpr (t == 89) { wait_vm<g1_wait_b(14, 89)>(); BARRIER(); }
    if constexpr (t == 99) { wait_vm<g1_wait_b(17, 99)>(); BARRIER(); }
    if constexpr (t == 7) q_voff = mk_qvoff(opq(tid) & 63);
    static_for<0, 4>([&](auto rc) {
      constexpr int r = rc.value;
      if constexpr (g1_q_issue(0, r) == t) dma16_if<0>(whalf, q_src + (u32)rot(4 + 4 * r + (w >> 1)) * (u32)(32 * RS), q_voff, L0 + r * 8192 + w * 1024);
      if constexpr (g1_q_issue(1, r) == t) dma16_if<1>(whalf, q_src + (u32)rot(4 + 4 * r + (w >> 1)) * (u32)(32 * RS), q_voff, L0 + r * 8192 + w * 1024);
    });
    static_for<0, 18>([&](auto jc) {
      constexpr int j = jc.value;
      if constexpr (g1_b_issue(0, j) == t) dma16_if<0>(whalf, a_src + 128 + (u32)rot(j) * (u32)(32 * RS), ab_voff, ab_dst + ((j + 4) % 18) * 8192);
      if constexpr (g1_b_issue(1, j) == t) dma16_if<1>(whalf, a_src + 128 + (u32)rot(j) * (u32)(32 * RS), ab_voff, ab_dst + ((j + 4) % 18) * 8192);
    });
    if constexpr (t >= 104 && t < 104 + 6 * 3 && (t - 104) % 6 == 0) {
      const int piece = min(((b >> 2) * 3 + (t - 104) / 6) * 8 + w, 1295);
      const int byte = piece * 1024;
      const char* src = (const char*)wd + (byte < 1024 * 1152 ? (b & 3) * 1024 * 1152 + byte : 4096 * 1152 + (b & 3) * 128 * 1152 + (byte - 1024 * 1152));
      dma16(src, lo16, L0 + G1_PF);
    }
    SCHED0();
    if constexpr (t % 12 == 11) { ST(8 + t / 12); }
    if constexpr (t == 107) { STR(29); }
  });
  MFMA_DRAIN(); SCHED0();
  {
    const f32x4 c2 = *(const LAS f32x4*)(csb + (7 * 128 + 16));
#pragma unroll
    for (int i = 0; i < 4; ++i) {
      const int tl = i & 1, e = (i >> 1) * 2;
      const f32x4 c = i < 2 ? cc : c2;
      float v[2];
#pragma unroll
      for (int j = 0; j < 2; ++j) {
        const float m = fmaf(nmu[tl], c[j], acc[0].a[0][tl][e + j]), tu = fmaf(nmu[tl], c[2 + j], acc[0].a[1][tl][e + j]);
        const float ex = __builtin_amdgcn_exp2f(m * fmaf(m * m, k2[tl], k1[tl]));
        v[j] = (m * tu) * __builtin_amdgcn_rcpf(fmaf(ex, kd[tl], kd[tl]));
      }
      emit(7, i, pack2(v[0], v[1]));
    }
  }
  ST(26); ST_DRAIN(); ST(27);
  ST_FLUSH(dbg, b * 8 + w);
}

constexpr int STG = 18432, NST = 8;
#define G2_D 3
constexpr int HB = G2_D + 1;
constexpr int G2_LDS = STG * NST;

__global__ __launch_bounds__(512, 2) void g2_gemm(const u16* __restrict__ hf, const u16* __restrict__ hh, const u16* __restrict__ wd,
                                                  char* __restrict__ part, u32* __restrict__ flags, u16* __restrict__ out, u32* dbg) {
  extern __shared__ __attribute__((aligned(128))) char lds[];
  ST_DECL; ST(0);
  const int tid = threadIdx.x, l = tid & 63;
  const int w = __builtin_amdgcn_readfirstlane(tid >> 6);
  const int b = blockIdx.x;
  const int r5 = b >> 3;
  const int ks = b & 3, tg = ((b >> 2) & 1) * 8 + (r5 & 7), dt = r5 >> 3;
  const int tq = w & 3, kh = w >> 2;
  const ldsp LP = (ldsp)lds;
  const u32 L0 = (u32)(uintptr_t)LP;
  constexpr u32 RS = D_ * 2;
  const u32 ab_voff = (u32)((8 * tq + (l >> 3)) * RS + ((((l & 7) >> 1) ^ ((l >> 4) & 3)) * 32) + (l & 1) * 16);
  const char* a_src = (const char*)wd + dt * 256;
  const char* q_src = (const char*)wd + 1024 + dt * 32;
  const u32 q_voff = (u32)((l >> 1) * RS + (l & 1) * 16);
  const u32 ab_dst = L0 + kh * 9216 + tq * 2048;
  const int m0 = ks * 4 + kh * 2;
  auto row0 = [&](int s) { const int q = s / 9, i = s - 9 * q; return i < 8 ? (m0 + q) * 256 + i * 32 : 4096 + (m0 + q) * 32; };
  u32x4 hb[HB][2];
  const char* hsrc0 = (const char*)hh + ((size_t)(tg * 8 + tq * 2 + kh) * 144 + m0 * 9) * 1024;
  const char* hsrc1 = (const char*)hh + ((size_t)(tg * 8 + tq * 2 + (kh ^ 1)) * 144 + m0 * 9) * 1024;
  const u32 lo16 = l * 16;
  auto issue = [&](auto nc, auto kc) {
    constexpr int n = nc.value, k = kc.value;
    if constexpr (k == 0) {
      dma16_if<(n & 3)>(tq, q_src + (u32)row0(n) * RS, q_voff, L0 + (n % NST) * STG + kh * 9216 + 8192);
      dma16(a_src + (u32)row0(n) * RS, ab_voff, ab_dst + (n % NST) * STG);
    }
    if constexpr (k == 1) gload16<(n & 3) * 1024>(hb[n % HB][0], lo16, hsrc0 + (n >> 2) * 4096);
    if constexpr (k == 2) gload16<(n & 3) * 1024>(hb[n % HB][1], lo16, hsrc1 + (n >> 2) * 4096);
    if constexpr (k == 3) dma16(a_src + (u32)row0(n) * RS + 128, ab_voff, ab_dst + (n % NST) * STG + 1024);
  };
  static_for<0, 4 * G2_D>([&](auto ic) { issue(IC<ic.value / 4>{}, IC<ic.value % 4>{}); });
  ST(1);
  const int rq = l >> 4, rr = (l >> 2) & 3, rp = l & 3;
  const u32 rb0 = L0 + kh * 9216 + (rq >> 1) * 2048 + (4 * (rq & 1) + rr) * 128 + ((2 * (rq & 1) + (rr >> 1)) * 32) + rp * 8;
  const u32 rbq = L0 + kh * 9216 + 8192 + (4 * rq + rr) * 32 + rp * 8;
  f32x4 acc[9][2];
  ldsp pb[5];
  u32x4 wf[4];
  auto fetch = [&](auto ic) {
    constexpr int i = ic.value, s = i / 9, c = i % 9, stg = s % NST;
    if constexpr (c == 0 && (stg % 3 == 0)) {
#pragma unroll
      for (int u = 0; u < 4; ++u) pb[u] = opaque((ldsp)(uintptr_t)((opq(rb0) ^ (u32)(u * 32)) + (u32)(stg * STG)));
      pb[4] = opaque((ldsp)(uintptr_t)(opq(rbq) + (u32)(stg * STG)));
    }
    constexpr int o = (stg % 3) * STG;
    if constexpr (c < 8) wf[i & 3] = tr_frag(pb[c & 3] + (o + (c >> 2) * 1024), pb[c & 3] + (o + (c >> 2) * 1024 + 4096));
    else wf[i & 3] = tr_frag(pb[4] + o, pb[4] + (o + 512));
  };
  const int ft = tg * 8 + ks * 2 + kh;
  const int dcol = dt * 128 + tq * 32 + 4 * rq;
  const int qcol = 512 + dt * 16 + 4 * rq;
  u32x2 r0, r1, r2;
  wait_vm2<4 * (G2_D - 1)>(hb[0][0], hb[0][1]);
  BARRIER();
  fetch(IC<0>{}); fetch(IC<1>{}); fetch(IC<2>{});
  static_for<0, 162>([&](auto ic) {
    constexpr int i = ic.value, s = i / 9, c = i % 9;
    if constexpr (c == 6 && s < 17) {
      wait_vm2<4 * (cmin(s + G2_D, 17) - (s + 1))>(hb[(s + 1) % HB][0], hb[(s + 1) % HB][1]);
      BARRIER();
    }
    if constexpr (i + 3 < 162) fetch(IC<i + 3>{});
#pragma unroll
    for (int tl = 0; tl < 2; ++tl) mma<s == 0>(acc[c][tl], wf[i & 3], hb[s % HB][tl]);
    if constexpr (s + G2_D < 18) {
      if constexpr (c == 0) issue(IC<s + G2_D>{}, IC<0>{});
      if constexpr (c == 2) issue(IC<s + G2_D>{}, IC<1>{});
      if constexpr (c == 3) issue(IC<s + G2_D>{}, IC<2>{});
      if constexpr (c == 5) issue(IC<s + G2_D>{}, IC<3>{});
    }
    if constexpr (s == 16 && c == 7) {
      const char* hb_ = (const char*)hf + (size_t)ft * 18432;
      auto po = [&](int d) { return (u32)(((d >> 5) * 64 + ((d >> 3) & 3) * 16 + (l & 15)) * 16 + (d & 4) * 2); };
      gload8(r0, po(dcol), hb_); gload8(r1, po(dcol + 16), hb_); gload8(r2, po(qcol), hb_);
    }
    SCHED0();
    if constexpr (c == 8 && s % 3 == 2) { ST(2 + s / 3); }
  });
  MFMA_DRAIN(); SCHED0();
  STR(28);
  {
    const ldsp xw = LP + (2 * STG + w * 9216 + l * 16);
#pragma unroll
    for (int c = 0; c < 9; ++c) *(LAS f32x4*)(xw + c * 1024) = acc[c][1];
  }
  LDS_BARRIER();
  f32x4 sm[9];
  {
    const ldsp xr = LP + (2 * STG + (w ^ 4) * 9216 + l * 16);
#pragma unroll
    for (int c = 0; c < 9; ++c) sm[c] = acc[c][0] + *(const LAS f32x4*)(xr + c * 1024);
  }
  ST(8);
  if (tq != ks) {
    char* pdst = part + ((((size_t)(tg * 4 + dt) * 4 + tq) * 4 + ks) * 2 + kh) * 4608;
#pragma unroll
    for (int pc = 0; pc < 4; ++pc) {
      u32x4 v = {pack2(sm[2 * pc][0], sm[2 * pc][1]), pack2(sm[2 * pc][2], sm[2 * pc][3]), pack2(sm[2 * pc + 1][0], sm[2 * pc + 1][1]), pack2(sm[2 * pc + 1][2], sm[2 * pc + 1][3])};
      st_sc1_x4(pdst + pc * 1024, lo16, v);
    }
    u32x2 v2 = {pack2(sm[8][0], sm[8][1]), pack2(sm[8][2], sm[8][3])};
    st_sc1_x2(pdst + 4096, l * 8, v2);
  } else {
    const ldsp ow = LP + (kh * 9216 + l * 16);
#pragma unroll
    for (int c = 0; c < 9; ++c) *(LAS f32x4*)(ow + c * 1024) = sm[c];
  }
  asm volatile("s_waitcnt vmcnt(0)" : "+v"(r0), "+v"(r1), "+v"(r2)::"memory");
  LDS_BARRIER();
  ST(9); STR(29);
  u32* fl = flags + (tg * 4 + dt) * 4 * 16;
  if (tid == 0) __hip_atomic_store((GAS u32*)(fl + ks * 16), 1u, __ATOMIC_RELAXED, __HIP_MEMORY_SCOPE_AGENT);
  const ldsp orp = LP + (kh * 9216 + l * 16);
  f32x4 o0 = *(const LAS f32x4*)(orp + tq * 2048), o1 = *(const LAS f32x4*)(orp + (tq * 2048 + 1024)), o2 = *(const LAS f32x4*)(orp + 8192);
  if (w == 0) {
#pragma unroll 1
    for (int it = 0; it < (1 << 18); ++it) {
      const u32 f = __hip_atomic_load((GAS u32*)(fl + (l & 3) * 16), __ATOMIC_RELAXED, __HIP_MEMORY_SCOPE_AGENT);
      if (__all(f == 1u)) break;
      __builtin_amdgcn_s_sleep(1);
    }
  }
  BARRIER();
  ST(10);
  {
    const char* psrc = part + (((size_t)(tg * 4 + dt) * 4 + ks) * 4 * 2 + kh) * 4608;
    u32x4 v[3]; u32x2 vq[3];
#pragma unroll
    for (int j = 0; j < 3; ++j) {
      const int src = (ks + 1 + j) & 3;
      ld_sc1_x4(v[j], lo16 + tq * 1024, psrc + src * 9216);
      ld_sc1_x2<0>(vq[j], l * 8 + 4096, psrc + src * 9216);
    }
    asm volatile("s_waitcnt vmcnt(0)" : "+v"(v[0]), "+v"(v[1]), "+v"(v[2]), "+v"(vq[0]), "+v"(vq[1]), "+v"(vq[2])::"memory");
    o0[0] += bflo(r0[0]); o0[1] += bfhi(r0[0]); o0[2] += bflo(r0[1]); o0[3] += bfhi(r0[1]);
    o1[0] += bflo(r1[0]); o1[1] += bfhi(r1[0]); o1[2] += bflo(r1[1]); o1[3] += bfhi(r1[1]);
    o2[0] += bflo(r2[0]); o2[1] += bfhi(r2[0]); o2[2] += bflo(r2[1]); o2[3] += bfhi(r2[1]);
#pragma unroll
    for (int j = 0; j < 3; ++j) {
      o0[0] += bflo(v[j][0]); o0[1] += bfhi(v[j][0]); o0[2] += bflo(v[j][1]); o0[3] += bfhi(v[j][1]);
      o1[0] += bflo(v[j][2]); o1[1] += bfhi(v[j][2]); o1[2] += bflo(v[j][3]); o1[3] += bfhi(v[j][3]);
      o2[0] += bflo(vq[j][0]); o2[1] += bfhi(vq[j][0]); o2[2] += bflo(vq[j][1]); o2[3] += bfhi(vq[j][1]);
    }
    u16* op = out + (size_t)(ft * 16 + (l & 15)) * D_;
    u32x2 oa = {pack2(o0[0], o0[1]), pack2(o0[2], o0[3])}, ob = {pack2(o1[0], o1[1]), pack2(o1[2], o1[3])}, oc = {pack2(o2[0], o2[1]), pack2(o2[2], o2[3])};
    *(u32x2*)(op + dcol) = oa; *(u32x2*)(op + dcol + 16) = ob;
    if (tq == 0) *(u32x2*)(op + qcol) = oc;
  }
  ST(11); ST_DRAIN(); ST(12);
  ST_FLUSH(dbg, b * 8 + w);
}

extern "C" void kernel_launch(void* const* d_in, const int* in_sizes, int n_in, void* d_out, int out_size, void* d_ws, size_t ws_size,
                              hipStream_t stream) {
  const u16* q = (const u16*)d_in[0]; const u16* k = (const u16*)d_in[1]; const u16* v = (const u16*)d_in[2];
  const u16* shk = (const u16*)d_in[3]; const u16* ssm = (const u16*)d_in[4]; const u16* ssmD = (const u16*)d_in[5];
  const u16* wg = (const u16*)d_in[7]; const u16* wu = (const u16*)d_in[8]; const u16* wd = (const u16*)d_in[9];
  char* ws = (char*)d_ws;
  u16* kvt = (u16*)(ws + OFF_KVT); u16* xp = (u16*)(ws + OFF_XP); u16* hf = (u16*)(ws + OFF_HF);
  f32x2* st1 = (f32x2*)(ws + OFF_ST1); float* cs = (float*)(ws + OFF_CS); u16* hh = (u16*)(ws + OFF_HH);
  (void)hipFuncSetAttribute((const void*)k1_conv, hipFuncAttributeMaxDynamicSharedMemorySize, K1_LDS);
  (void)hipFuncSetAttribute((const void*)g1_gemm, hipFuncAttributeMaxDynamicSharedMemorySize, G1_LDS);
  (void)hipFuncSetAttribute((const void*)g2_gemm, hipFuncAttributeMaxDynamicSharedMemorySize, G2_LDS);
  k0_prep<<<K0_GRID, 256, 0, stream>>>(q, k, v, shk, kvt, xp, st1, (u32x4*)(ws + OFF_FLG), ssm, (u32*)(ws + (202u << 20)));
  k1_conv<<<K1_GRID, 512, K1_LDS, stream>>>(ssm, ssmD, kvt, xp, st1, hf, wg, wu, cs, (u32*)(ws + (203u << 20)));
  g1_gemm<<<256, 512, G1_LDS, stream>>>(hf, wg, wu, cs, hh, ws + OFF_ST2, wd, (u32*)(ws + (200u << 20)));
  g2_gemm<<<256, 512, G2_LDS, stream>>>(hf, hh, wd, ws + OFF_PART, (u32*)(ws + OFF_FLG), (u16*)d_out, (u32*)(ws + (201u << 20)));
}
```

```cpp
#include <hip/hip_runtime.h>
#include <hip/hip_bf16.h>
#include <stdint.h>
#define STAG 0

typedef unsigned short u16;
typedef unsigned int u32;
typedef __attribute__((ext_vector_type(2))) unsigned u32x2;
typedef __attribute__((ext_vector_type(4))) unsigned u32x4;
typedef __attribute__((ext_vector_type(2))) float f32x2;
typedef __attribute__((ext_vector_type(4))) float f32x4;
typedef __attribute__((ext_vector_type(8))) __bf16 bf16x8;
typedef __attribute__((ext_vector_type(2))) __bf16 bf16x2;
typedef __attribute__((ext_vector_type(4))) short s16x4;
#define GAS __attribute__((address_space(1)))
#define LAS __attribute__((address_space(3)))
#define DI __device__ __forceinline__
typedef LAS char* ldsp;

constexpr int S_ = 2048, D_ = 576, DF_ = 4608;
constexpr size_t OFF_KVT = 0;
constexpr size_t OFF_XP  = OFF_KVT + (size_t)D_ * S_ * 2;
constexpr size_t OFF_HF  = OFF_XP + (size_t)D_ * S_ * 2;
constexpr size_t OFF_ST1 = OFF_HF + (size_t)D_ * S_ * 2;
constexpr size_t OFF_CS  = OFF_ST1 + 2048 * 8;
constexpr size_t OFF_FLG = OFF_CS + 4 * 2 * 4608 * 4;
constexpr size_t OFF_ST2 = OFF_FLG + 32768;
constexpr size_t OFF_HH  = OFF_ST2 + 131072;
constexpr size_t OFF_PART = OFF_HH + (size_t)S_ * DF_ * 2;

DI float bflo(u32 w) { return __uint_as_float(w << 16); }
DI float bfhi(u32 w) { return __uint_as_float(w & 0xffff0000u); }
DI u32 pack2(float a, float b) { bf16x2 p; p[0] = (__bf16)a; p[1] = (__bf16)b; return __builtin_bit_cast(u32, p); }

template <int I> struct IC { static constexpr int value = I; };
template <int B, int E, class F> DI void static_for(F&& f) { if constexpr (B < E) { f(IC<B>{}); static_for<B + 1, E>(f); } }
template <int N> DI void wait_vm() { asm volatile("s_waitcnt vmcnt(%0)" ::"n"(N) : "memory"); }
#define BARRIER() do { asm volatile("" ::: "memory"); __builtin_amdgcn_s_barrier(); asm volatile("" ::: "memory"); } while (0)
#define LDS_BARRIER() do { asm volatile("s_waitcnt lgkmcnt(0)" ::: "memory"); __builtin_amdgcn_s_barrier(); asm volatile("" ::: "memory"); } while (0)
#define SCHED0() __builtin_amdgcn_sched_barrier(0)
constexpr int cmin(int a, int b) { return a < b ? a : b; }

DI u32x4 tr_frag(ldsp a0, ldsp a1) {
  const s16x4 lo = __builtin_amdgcn_ds_read_tr16_b64_v4i16((LAS s16x4*)a0);
  const s16x4 hi = __builtin_amdgcn_ds_read_tr16_b64_v4i16((LAS s16x4*)a1);
  return __builtin_bit_cast(u32x4, __builtin_shufflevector(lo, hi, 0, 1, 2, 3, 4, 5, 6, 7));
}
DI f32x4 mfma16(const u32x4& a, const u32x4& b, const f32x4& c) {
  return __builtin_amdgcn_mfma_f32_16x16x32_bf16(__builtin_bit_cast(bf16x8, a), __builtin_bit_cast(bf16x8, b), c, 0, 0, 0);
}
template <bool FIRST> DI void mma(f32x4& c, const u32x4& a, const u32x4& b) {
  if constexpr (FIRST) asm volatile("v_mfma_f32_16x16x32_bf16 %0, %1, %2, 0" : "=&v"(c) : "v"(a), "v"(b));
  else asm volatile("v_mfma_f32_16x16x32_bf16 %0, %1, %2, %0" : "+v"(c) : "v"(a), "v"(b));
}
#define MFMA_DRAIN() asm volatile("s_nop 15" ::: "memory")
template <int OFF> DI void gload16_sc1(u32x4& dst, u32 voff, const char* sbase) {
  asm volatile("global_load_dwordx4 %0, %1, %2 offset:%3 sc1" : "=v"(dst) : "v"(voff), "s"(sbase), "n"(OFF) : "memory");
}
DI void poll_issue(u32& f, u32 voff, const char* sb) { asm volatile("global_load_dword %0, %1, %2 sc1" : "=v"(f) : "v"(voff), "s"(sb) : "memory"); }
template <int N, int PH> DI void poll_check(u32& f, u32 voff, const char* sb) {
  u32 cnt = 1u << 18;
  asm volatile("s_waitcnt vmcnt(%4)\n"
               "1:\n\tv_cmp_gt_u32_e32 vcc, %5, %0\n\ts_nop 4\n\ts_cbranch_vccz 2f\n\t"
               "s_sleep 1\n\tglobal_load_dword %0, %2, %3 sc1\n\ts_waitcnt vmcnt(0)\n\t"
               "s_sub_u32 %1, %1, 1\n\ts_cbranch_scc0 1b\n"
               "2:" : "+v"(f), "+s"(cnt) : "v"(voff), "s"(sb), "n"(N), "n"(PH) : "memory", "vcc", "scc");
}
DI void flag_store(int w, u32 zero_off, u32 val, const char* sb) {
  asm volatile("s_cmp_lg_u32 %3, 0\n\ts_cbranch_scc1 1f\n\tglobal_store_dword %0, %1, %2 sc1\n1:" ::"v"(zero_off), "v"(val), "s"(sb), "s"(w) : "memory", "scc");
}
template <int OFF> DI void gload16(u32x4& dst, u32 voff, const char* sbase) {
  asm volatile("global_load_dwordx4 %0, %1, %2 offset:%3" : "=v"(dst) : "v"(voff), "s"(sbase), "n"(OFF) : "memory");
}
DI void gload8(u32x2& dst, u32 voff, const char* sbase) { asm volatile("global_load_dwordx2 %0, %1, %2" : "=v"(dst) : "v"(voff), "s"(sbase) : "memory"); }
template <int N, int VAL> DI void waitbar_if(int sel, u32x4& a, u32x4& b) {
  asm volatile("s_cmp_lg_u32 %2, %4\n\ts_cbranch_scc1 1f\n\ts_waitcnt vmcnt(%3)\n\ts_barrier\n1:" : "+v"(a), "+v"(b) : "s"(sel), "n"(N), "n"(VAL) : "memory", "scc");
}
template <int N> DI void wait_vm2(u32x4& a, u32x4& b) { asm volatile("s_waitcnt vmcnt(%2)" : "+v"(a), "+v"(b) : "n"(N) : "memory"); }
DI void dma16(const char* sbase, u32 voff, u32 lds_dst) {
  asm volatile("s_mov_b32 m0, %2\n\ts_nop 0\n\tglobal_load_lds_dwordx4 %0, %1" ::"v"(voff), "s"(sbase), "s"(lds_dst) : "memory");
}
template <int VAL> DI void dma16_if(int sel, const char* sbase, u32 voff, u32 lds_dst) {
  asm volatile("s_cmp_eq_u32 %3, %4\n\ts_mov_b32 m0, %2\n\ts_cselect_b64 exec, -1, 0\n\tglobal_load_lds_dwordx4 %0, %1\n\ts_mov_b64 exec, -1" ::"v"(voff), "s"(sbase), "s"(lds_dst), "s"(sel), "n"(VAL) : "memory", "scc");
}
DI ldsp opaque(ldsp p) { asm volatile("" : "+v"(p)); return p; }
DI u32 opq(u32 p) { asm volatile("" : "+v"(p)); return p; }
DI void pin(u32& p) { asm volatile("" : "+v"(p)); }
DI void pinf(float& p) { asm volatile("" : "+v"(p)); }
DI float hide(float p) { asm("" : "+v"(p)); return p; }
DI float swap16_sum(float x) { const auto r = __builtin_amdgcn_permlane16_swap(__float_as_uint(x), __float_as_uint(x), false, false); return __uint_as_float(r[0]) + __uint_as_float(r[1]); }
DI float swap32_sum(float x) { const auto r = __builtin_amdgcn_permlane32_swap(__float_as_uint(x), __float_as_uint(x), false, false); return __uint_as_float(r[0]) + __uint_as_float(r[1]); }
#define STAT1(R) "v_lshlrev_b32_e32 %0, 16, " R "\n\tv_and_b32_e32 %1, %8, " R "\n\tv_add_f32_e32 %2, %2, %0\n\tv_fmac_f32_e32 %3, %0, %0\n\tv_add_f32_e32 %2, %2, %1\n\tv_fmac_f32_e32 %3, %1, %1\n\t"
DI void stat_acc(int fg, const u32x4& v, float& s1, float& s2) {
  float x, y;
  asm volatile("s_cmp_gt_u32 %9, 5\n\ts_cbranch_scc1 1f\n\t" STAT1("%4") STAT1("%5") STAT1("%6") STAT1("%7") "\n1:"
               : "=&v"(x), "=&v"(y), "+v"(s1), "+v"(s2) : "v"(v[0]), "v"(v[1]), "v"(v[2]), "v"(v[3]), "s"(0xffff0000u), "s"(fg) : "scc");
}
DI void ld_sc1_x4(u32x4& d, u32 voff, const char* sb) { asm volatile("global_load_dwordx4 %0, %1, %2 sc1" : "=v"(d) : "v"(voff), "s"(sb) : "memory"); }
template <int OFF> DI void ld_sc1_x2(u32x2& d, u32 voff, const char* sb) { asm volatile("global_load_dwordx2 %0, %1, %2 offset:%3 sc1" : "=v"(d) : "v"(voff), "s"(sb), "n"(OFF) : "memory"); }
DI void wait0_x6(u32x4& a, u32x2& b) { asm volatile("s_waitcnt vmcnt(0)" : "+v"(a), "+v"(b)::"memory"); }
DI void st_sc1_x4(char* sb, u32 voff, const u32x4& v) { asm volatile("global_store_dwordx4 %0, %1, %2 sc1\n\ts_nop 1" ::"v"(voff), "v"(v), "s"(sb) : "memory"); }
DI void gstore16(char* sb, u32 voff, const u32x4& v) { asm volatile("global_store_dwordx4 %0, %1, %2\n\ts_nop 1" ::"v"(voff), "v"(v), "s"(sb) : "memory"); }
DI void gstore8(char* sb, u32 voff, const u32x2& v) { asm volatile("global_store_dwordx2 %0, %1, %2" ::"v"(voff), "v"(v), "s"(sb) : "memory"); }
DI void st_sc1_x2_if(int skip, const char* sb, u32 voff, const u32x2& v) {
  asm volatile("s_cmp_gt_u32 %3, 5\n\ts_cbranch_scc1 1f\n\tglobal_store_dwordx2 %0, %1, %2 sc1\n1:" ::"v"(voff), "v"(v), "s"(sb), "s"(skip) : "memory", "scc");
}
DI void st_sc1_x2(char* sb, u32 voff, const u32x2& v) { asm volatile("global_store_dwordx2 %0, %1, %2 sc1" ::"v"(voff), "v"(v), "s"(sb) : "memory"); }

#define STR(i)
#define ST(i)
#define ST_DECL
#define ST_FLUSH(dbg, wv)
#define ST_DRAIN()

DI void colsum_slice(const u16* __restrict__ wg, const u16* __restrict__ wu, float* __restrict__ cs, int sl, int l) {
  const int mat = sl & 1, qr = (sl >> 1) & 3, cb = sl >> 3;
  const u16* p = (mat ? wu : wg) + (size_t)(qr * 144 + (l >> 3)) * DF_ + cb * 64 + (l & 7) * 8;
  u32x4 x[18];
#pragma unroll
  for (int i = 0; i < 18; ++i) x[i] = *(const u32x4*)(p + (size_t)i * 8 * DF_);
  float a[8];
#pragma unroll
  for (int e = 0; e < 8; ++e) a[e] = 0.f;
#pragma unroll
  for (int i = 0; i < 18; ++i)
#pragma unroll
    for (int e = 0; e < 4; ++e) { a[2 * e] += bflo(x[i][e]); a[2 * e + 1] += bfhi(x[i][e]); }
#pragma unroll
  for (int e = 0; e < 8; ++e) { a[e] += __shfl_xor(a[e], 8); a[e] = swap32_sum(swap16_sum(a[e])); }
  if (l < 8) {
    float* d = cs + qr * 9216 + (cb * 32 + l * 4) * 4 + mat * 2;
#pragma unroll
    for (int e = 0; e < 4; ++e) { f32x2 o = {a[2 * e], a[2 * e + 1]}; *(f32x2*)(d + e * 4) = o; }
  }
}

#define K0_SL 0
constexpr int K0_GRID = 288 + 64 + K0_SL / 4;
__global__ __launch_bounds__(256) void k0_prep(const u16* __restrict__ q, const u16* __restrict__ k, const u16* __restrict__ v,
                                               const u16* __restrict__ shk, const u16* __restrict__ ssm, const u16* __restrict__ wg,
                                               const u16* __restrict__ wu, char* __restrict__ ws) {
  ST_DECL; ST(0);
  asm volatile("" ::"s"(q), "s"(k), "s"(v), "s"(shk), "s"(ssm), "s"(wg), "s"(wu), "s"(ws));
  u16* __restrict__ kvt = (u16*)(ws + OFF_KVT);
  u16* __restrict__ xp = (u16*)(ws + OFF_XP);
  const int b = blockIdx.x, tid = threadIdx.x, w = tid >> 6, l = tid & 63;
  if (b < 288) {
    const int tb = b / 9, cb = b - tb * 9;
    const int s0 = tb * 64 + w * 16 + (l >> 4) * 4;
    const int c0 = cb * 64 + (l & 15) * 4;
    u32x2 kr[7], vr[4], qr[4];
#pragma unroll
    for (int i = 0; i < 7; ++i) {
      const int s = s0 - 3 + i;
      kr[i] = *(const u32x2*)(k + (size_t)(s < 0 ? 0 : s) * D_ + c0);
    }
#pragma unroll
    for (int i = 0; i < 4; ++i) vr[i] = *(const u32x2*)(v + (size_t)(s0 + i) * D_ + c0);
    const u32x4 w0 = *(const u32x4*)(shk + c0 * 4), w1 = *(const u32x4*)(shk + c0 * 4 + 8);
#pragma unroll
    for (int i = 0; i < 4; ++i) qr[i] = *(const u32x2*)(q + (size_t)(s0 + i) * D_ + c0);
    u32x4 pf0, pf1;
    gload16<0>(pf0, (u32)(b * 8192 + tid * 16), (const char*)ssm);
    gload16<0>(pf1, (u32)(b * 8192 + 4096 + tid * 16), (const char*)ssm);
    if (s0 == 0) { kr[0] = (u32x2){0u, 0u}; kr[1] = kr[0]; kr[2] = kr[0]; }
#pragma unroll
    for (int c = 0; c < 4; ++c) {
      const u32 wa = (c < 2) ? w0[2 * (c & 1)] : w1[2 * (c & 1)], wb = (c < 2) ? w0[2 * (c & 1) + 1] : w1[2 * (c & 1) + 1];
      const float f0 = bflo(wa), f1 = bfhi(wa), f2 = bflo(wb), f3 = bfhi(wb);
      float kk[7], kvv[4];
#pragma unroll
      for (int i = 0; i < 7; ++i) kk[i] = (c & 1) ? bfhi(kr[i][c >> 1]) : bflo(kr[i][c >> 1]);
#pragma unroll
      for (int i = 0; i < 4; ++i) {
        const float ks = kk[i + 3] * f0 + kk[i + 2] * f1 + kk[i + 1] * f2 + kk[i] * f3;
        const float vv = (c & 1) ? bfhi(vr[i][c >> 1]) : bflo(vr[i][c >> 1]);
        kvv[i] = ks * vv;
      }
      u32x2 o = {pack2(kvv[3], kvv[2]), pack2(kvv[1], kvv[0])};
      u16* dst = kvt + (size_t)(c0 + c) * S_ + (s0 >> 3) * 8 + ((s0 & 4) ? 0 : 4);
      *(u32x2*)dst = o;
    }
    u32x4 xa = {qr[0][0], qr[0][1], qr[1][0], qr[1][1]}, xb = {qr[2][0], qr[2][1], qr[3][0], qr[3][1]};
    u16* xdst = xp + ((size_t)(c0 >> 2) * S_ + s0) * 4;
    *(u32x4*)xdst = xa; *(u32x4*)(xdst + 8) = xb;
    if (b < 40) {
      const u32 f = b < 8 ? 0u : 0xffffffffu;
      u32x4 z = {f, f, f, f};
      ((u32x4*)(ws + OFF_FLG))[b * 256 + tid] = z;
    }
    asm volatile("s_waitcnt vmcnt(0)" : "+v"(pf0), "+v"(pf1)::"memory");
  } else if (b < 352) {
    const int row = (b - 288) * 32 + w * 8 + (l >> 3);
    const u16* p = q + (size_t)row * D_ + (l & 7) * 8;
    u32x4 x[9];
#pragma unroll
    for (int i = 0; i < 9; ++i) x[i] = *(const u32x4*)(p + i * 64);
    float sum = 0.f;
#pragma unroll
    for (int i = 0; i < 9; ++i)
#pragma unroll
      for (int j = 0; j < 4; ++j) sum += bflo(x[i][j]) + bfhi(x[i][j]);
    sum += __shfl_xor(sum, 1); sum += __shfl_xor(sum, 2); sum += __shfl_xor(sum, 4);
    const float mu = sum * (1.f / D_);
    float sq = 0.f;
#pragma unroll
    for (int i = 0; i < 9; ++i)
#pragma unroll
      for (int j = 0; j < 4; ++j) { const float a = bflo(x[i][j]) - mu, c = bfhi(x[i][j]) - mu; sq += a * a + c * c; }
    sq += __shfl_xor(sq, 1); sq += __shfl_xor(sq, 2); sq += __shfl_xor(sq, 4);
    if ((l & 7) == 0) { f32x2 o = {mu, rsqrtf(sq * (1.f / D_) + 1e-5f)}; ((f32x2*)(ws + OFF_ST1))[row] = o; }
  } else {
    colsum_slice(wg, wu, (float*)(ws + OFF_CS), (b - 352) * 4 + w, l);
  }
  ST(1); ST_DRAIN(); ST(2);
  ST_FLUSH((u32*)(ws + (202u << 20)), b * 4 + w);
}

constexpr int K1_UPLANE = 2304, K1_UCH = 4608, K1_TOFF = 4 * K1_UCH, K1_TCOPY = 4160, K1_TCH = 4 * K1_TCOPY;
constexpr int K1_YOFF = K1_TOFF + 4 * K1_TCH, K1_YBUF = 8192;
constexpr int K1_LDS = K1_YOFF + 8 * K1_YBUF;
constexpr int K1_GRID = 256, K1_SIDE = K1_GRID - 144;

__global__ __launch_bounds__(512) void k1_conv(const u16* __restrict__ ssm, const u16* __restrict__ ssmD, const u16* __restrict__ wg,
                                               const u16* __restrict__ wu, char* __restrict__ ws) {
  extern __shared__ __attribute__((aligned(128))) char lds[];
  ST_DECL; ST(0);
  asm volatile("" ::"s"(ssm), "s"(ssmD), "s"(wg), "s"(wu), "s"(ws));
  const u16* __restrict__ kvt = (const u16*)(ws + OFF_KVT);
  const u16* __restrict__ xp = (const u16*)(ws + OFF_XP);
  const f32x2* __restrict__ st1 = (const f32x2*)(ws + OFF_ST1);
  u16* __restrict__ hf = (u16*)(ws + OFF_HF);
  const int tid = threadIdx.x, l = tid & 63;
  const int w = __builtin_amdgcn_readfirstlane(tid >> 6);
  const int b = blockIdx.x;
  if (b >= 144) {
    const int sl = K0_SL + (b - 144) + K1_SIDE * w;
    if (sl < 576) colsum_slice(wg, wu, (float*)(ws + OFF_CS), sl, l);
    ST(6);
    ST_FLUSH((u32*)(ws + (203u << 20)), b * 8 + w);
    return;
  }
  const int g = b, ch = w & 3, hf_ = w >> 2;
  const int gch = g * 4 + ch;
  const ldsp LP = (ldsp)lds;
  const u32 L0 = (u32)(uintptr_t)LP;
  {
    const char* src = (const char*)(kvt + (size_t)gch * S_);
    const u32 vo = (2 * l + hf_) * 16;
    const u32 dst = L0 + ch * K1_UCH + hf_ * K1_UPLANE + 256;
    dma16(src, vo, dst);
    dma16(src + 2048, vo, dst + 1024);
  }
  const u16* krow = ssm + (size_t)gch * S_;
  u32x4 wv[2]; u32x2 wn[2];
#pragma unroll
  for (int it = 0; it < 2; ++it) {
    const int idx = hf_ * 128 + it * 64 + l;
    wv[it] = *(const u32x4*)(krow + (idx > 0 ? idx - 1 : 0) * 8);
    wn[it] = *(const u32x2*)(krow + idx * 8);
  }
  const u32x4 wl = *(const u32x4*)(krow + opq(255 * 8));
  const u32 dv1 = ssmD[opq(gch)];
  SCHED0();
  u32x2 xr[4]; f32x2 st[4];
#pragma unroll
  for (int i = 0; i < 4; ++i) { xr[i] = *(const u32x2*)(xp + ((size_t)g * S_ + tid + 512 * i) * 4); st[i] = st1[tid + 512 * i]; }
  SCHED0();
  const u32x4 z4 = {0u, 0u, 0u, 0u};
  if (l < 16) *(LAS u32x4*)(LP + (ch * K1_UCH + hf_ * K1_UPLANE + l * 16)) = z4;
  const ldsp tb = LP + (K1_TOFF + ch * K1_TCH);
  auto put = [&](int pos  , u32x4 a, u32x2 n) {
    const u32 o0 = __builtin_amdgcn_alignbit(a[1], a[0], 16), o1 = __builtin_amdgcn_alignbit(a[2], a[1], 16), o2 = __builtin_amdgcn_alignbit(a[3], a[2], 16),
              o3 = __builtin_amdgcn_alignbit(n[0], a[3], 16), o4 = __builtin_amdgcn_alignbit(n[1], n[0], 16);
    u32x4 c1 = {o0, o1, o2, o3}, c2 = {a[1], a[2], a[3], n[0]}, c3 = {o1, o2, o3, o4};
    *(LAS u32x4*)(tb + pos * 16) = a;
    *(LAS u32x4*)(tb + (K1_TCOPY + pos * 16)) = c1;
    *(LAS u32x4*)(tb + (2 * K1_TCOPY + pos * 16)) = c2;
    *(LAS u32x4*)(tb + (3 * K1_TCOPY + pos * 16)) = c3;
  };
  if (hf_ == 0 && l == 0) wv[0] = z4;
#pragma unroll
  for (int it = 0; it < 2; ++it) put(hf_ * 128 + it * 64 + l + 1, wv[it], wn[it]);
  if (hf_ == 1 && l < 2) { u32x2 z2 = {0u, 0u}; put(l ? 257 : 0, l ? wl : z4, z2); }
  ST(1);
  wait_vm<8>();
  LDS_BARRIER();
  ST(2);
  const int a_ = l & 15, q_ = l >> 4;
  const int cpy = (1 + a_) & 3;
  const ldsp taddr = LP + (K1_TOFF + ch * K1_TCH + cpy * K1_TCOPY + 2 * (1 + a_ + 8 * q_ - cpy) + hf_ * 256);
  const ldsp uaddr = LP + (ch * K1_UCH + ((q_ + 1) & 1) * K1_UPLANE + (16 + a_ - (q_ >> 1) - 8 * hf_ - 6) * 16);
  f32x4 acc[8];
  u32x4 dg;
  {
    const int j = 15 - a_ - 8 * q_;
    const u32 dval = hf_ ? 0u : dv1;
#pragma unroll
    for (int e = 0; e < 4; ++e) dg[e] = (j == 2 * e) ? dval : ((j == 2 * e + 1) ? (dval << 16) : 0u);
  }
  u32x4 U[2][8], T[8];
  auto ldU = [&](int e, int j) { return *(const LAS u32x4*)(uaddr + ((3 - e) * 32 + 256 * j)); };
  auto ldT = [&](int e, int m) {
    const u32x2 lo = *(volatile const LAS u32x2*)(taddr + (64 * e + 512 * m)), hi = *(volatile const LAS u32x2*)(taddr + (8 + 64 * e + 512 * m));
    u32x4 t = {lo[0], lo[1], hi[0], hi[1]};
    return t;
  };
#pragma unroll
  for (int j = 0; j < 8; ++j) U[0][j] = ldU(0, j);
  T[0] = ldT(0, 0); T[1] = ldT(0, 1);
  SCHED0();
  const f32x4 zero4 = {0.f, 0.f, 0.f, 0.f};
#pragma unroll
  for (int n = 0; n < 8; ++n) acc[n] = mfma16(dg, U[0][n], zero4);
  SCHED0();
  static_for<0, 32>([&](auto ic) {
    constexpr int e = ic.value >> 3, m = ic.value & 7;
    constexpr int nx = ic.value + 2;
    if constexpr (nx < 32) T[nx & 7] = ldT(nx >> 3, nx & 7);
    if constexpr (e < 3) U[(e + 1) & 1][m] = ldU(e + 1, m);
#pragma unroll
    for (int n = m; n < 8; ++n) acc[n] = mfma16(T[m], U[e & 1][n - m], acc[n]);
    SCHED0();
  });
  ST(3);
  {
    const ldsp yb = LP + (K1_YOFF + (hf_ * 4 + ch) * K1_YBUF + ((4 * a_ + q_) ^ ((a_ >> 1) & 3)) * 16);
#pragma unroll
    for (int n = 0; n < 8; ++n) *(LAS f32x4*)(yb + n * 1024) = acc[n];
  }
  LDS_BARRIER();
  ST(4);
  {
    const int sl = tid >> 2;
    const ldsp yb = LP + (K1_YOFF + (sl ^ ((sl >> 3) & 3)) * 16 + (tid & 3) * 4);
    float y[4][8];
#pragma unroll
    for (int i = 0; i < 4; ++i)
#pragma unroll
      for (int c = 0; c < 8; ++c) y[i][c] = *(const LAS float*)(yb + (i * 2048 + c * K1_YBUF));
    u16* hp = hf + ((((size_t)(tid >> 4) * 18 + (g >> 3)) * 64 + ((g >> 1) & 3) * 16 + (tid & 15)) * 8 + (g & 1) * 4);
#pragma unroll
    for (int i = 0; i < 4; ++i) {
      float hv[4];
#pragma unroll
      for (int c = 0; c < 4; ++c) {
        const float x = (c & 1) ? bfhi(xr[i][c >> 1]) : bflo(xr[i][c >> 1]);
        hv[c] = x + (x - st[i][0]) * st[i][1] * (y[i][c] + y[i][4 + c]);
      }
      u32x2 o = {pack2(hv[0], hv[1]), pack2(hv[2], hv[3])};
      *(u32x2*)(hp + (size_t)i * 32 * 18 * 512) = o;
    }
  }
  ST(5); ST_DRAIN(); ST(6);
  ST_FLUSH((u32*)(ws + (203u << 20)), b * 8 + w);
}

constexpr int G1_QE = 147456;
constexpr int G1_QL = 14 * 8192;
constexpr int G1_CS = G1_QE + 8192;
constexpr int G1_CP = G1_CS + 2048;
constexpr int FF_LDS = G1_CP + 5120;
constexpr int PF_ISS0 = 86;
constexpr int STG = 18432, NST = 8;
#define G1_DV 4
constexpr int G1_D = G1_DV, HD = 5, HB = HD + 1;
constexpr float GC = -2.3022081983f;
constexpr int U2 = 216;
constexpr int g1_q_issue(int half, int r) { return 8 + 6 * r; }
constexpr int g1_b_issue(int half, int j) { return (j < 14 ? 29 + 3 * j : 73 + 3 * (j - 14)); }
constexpr int G1_E0 = 8;
constexpr int epi_start(int i) { return i < 8 ? G1_E0 + 26 * i : U2 + 2; }
constexpr bool epi_stores(int i) { return i == 1 || i == 3 || i == 4 || i == 6 || i == 8; }
constexpr int w_issue(int n, int k) {
  return n < 4 ? 191 + 4 * n + 2 * k : n < 6 ? 209 + 4 * (n - 4) + 2 * k : n == 6 ? U2 + 7 + k : U2 + 9 * (n - 6) + 4 * k;
}
constexpr int h_issue(int n, int k) { return n < 5 ? 192 + 4 * n + 2 * k : U2 + 9 * (n - (n < 10 ? HD : 3)) + 2 + 3 * k; }
constexpr int dq_issue(int r) { return r == 0 ? 183 : r < 4 ? U2 + 9 * (4 * r - 3) + 7 : U2 + 9 * 12 + 7; }
constexpr int poll_iss(int p) { return p == 0 ? 181 : p == 1 ? U2 + 9 * 5 + 8 : U2 + 9 * 9 + 8; }
constexpr int poll_chk(int p) { return p == 0 ? 191 : p == 1 ? U2 + 9 * 7 + 1 : U2 + 9 * 11 + 1; }
constexpr int RES_ISS = U2 + 9 * 16 + 7;
constexpr int cnt_ops(int half, int k0, int k1) {
  int n = 0;
  auto in = [&](int key) { return key > k0 && key < k1 ? 1 : 0; };
  for (int r = 0; r < 4; ++r) n += in(4 * g1_q_issue(half, r) + 2);
  for (int j = 0; j < 18; ++j) n += in(4 * g1_b_issue(half, j) + 2);
  for (int i = 0; i < 9; ++i) if (epi_stores(i)) n += in(4 * (epi_start(i) + 18)) + in(4 * (epi_start(i) + 24));
  for (int s = 0; s < 18; ++s) for (int k = 0; k < 2; ++k) n += in(4 * w_issue(s, k) + 2) + in(4 * h_issue(s, k) + 2);
  for (int p = 0; p < 3; ++p) n += in(4 * poll_iss(p) + 2);
  n += 3 * in(4 * RES_ISS + 2);
  for (int r = 0; r < 5; ++r) n += in(4 * (PF_ISS0 + 4 * r) + 2);
  for (int r = 0; r < 5; ++r) n += in(4 * dq_issue(r) + 2);
  return n;
}
constexpr int wait_n(int k0a, int k0b, int k1) { return cmin(cnt_ops(0, k0a, k1), cnt_ops(1, k0b, k1)); }
constexpr int wait_q(int r, int tw) { return wait_n(4 * g1_q_issue(0, r) + 2, 4 * g1_q_issue(1, r) + 2, 4 * tw + 1); }
constexpr int wait_b(int j, int tw) { return wait_n(4 * g1_b_issue(0, j) + 2, 4 * g1_b_issue(1, j) + 2, 4 * tw + 1); }
constexpr int wait_k(int k0, int k1) { return wait_n(k0, k0, k1); }
constexpr int cmax(int a, int b) { return a > b ? a : b; }
static_assert(h_issue(1, 1) > epi_start(6) + 24 && h_issue(0, 0) > poll_chk(0) && h_issue(10, 0) > poll_chk(1) && h_issue(14, 0) > poll_chk(2), "schedule");
constexpr int kk_of(int s) { return s < 8 ? 9 * ((s >> 1) & 1) + 4 * (s & 1) + (s >> 2) : s < 10 ? 9 * (s - 8) + 8 : 9 * (((s - 10) >> 1) & 1) + 4 * (s & 1) + 2 + ((s - 10) >> 2); }
struct Acc { f32x4 a[2][2]; };

__global__ __launch_bounds__(512, 2) void ffn_fused(const u16* __restrict__ wg, const u16* __restrict__ wu, const u16* __restrict__ wd,
                                                    char* __restrict__ ws, u16* __restrict__ out) {
  extern __shared__ __attribute__((aligned(128))) char lds[];
  ST_DECL; ST(0);
  asm volatile("" ::"s"(wg), "s"(wu), "s"(wd), "s"(ws), "s"(out));
  const int tid = threadIdx.x, l = tid & 63;
  const int w = __builtin_amdgcn_readfirstlane(tid >> 6);
  const int b = blockIdx.x;
  const int fg = (b & 3) * 8 + ((b >> 3) & 7), tg = ((b >> 2) & 1) * 4 + (b >> 6);
  const int tt = tg * 8 + w;
  const int rotg = fg < 6 ? 3 * fg : 0;
  auto rot = [&](int g) { const int x = g + rotg; return x >= 18 ? x - 18 : x; };
  const ldsp LP = (ldsp)lds;
  const u32 L0 = (u32)(uintptr_t)LP;
  constexpr size_t RS = DF_ * 2;
  const char* wmat = (const char*)((w & 1) ? wu : wg);
  const int wblk = w & 1, whr = (w >> 1) & 1, whalf = w >> 2;
  const u32 ab_voff = (u32)((16 * whalf + 8 * whr + (l >> 3)) * RS + ((((l & 7) >> 1) ^ (((l >> 4) & 1) | (whr << 1))) * 32) + (l & 1) * 16);
  const char* a_src = wmat + fg * 256;
  const u32 ab_dst = L0 + whalf * 4096 + wblk * 2048 + whr * 1024;
  const char* q_src = wmat + 8192 + fg * 32;
  auto mk_qvoff = [&](u32 l2) { return (u32)(((l2 >> 1) ^ ((l2 >> 2) & 4)) * RS + (l2 & 1) * 16); };
  u32x4 h[2][18];
  const char* hsrc = ws + OFF_HF + (size_t)tt * 36864;
  const u32 lo16 = l * 16;
  auto issue_dma = [&](auto ic) { constexpr int g = ic.value; dma16(a_src + (u32)rot(g) * (u32)(32 * RS), ab_voff, ab_dst + g * 8192); };
  auto issue_h = [&](auto ic, auto tc) { constexpr int g = ic.value, tl = tc.value; gload16<0>(h[tl][g], lo16, hsrc + rot(g) * 1024 + tl * 18432); };
  static_for<0, G1_D>([&](auto ic) { issue_dma(ic); issue_h(ic, IC<0>{}); issue_h(ic, IC<1>{}); });
  ST(1);
  const int rq = l >> 4, rr = (l >> 2) & 3, rp = l & 3;
  const u32 rb0 = L0 + (rq >> 1) * 4096 + (rq & 1) * 1024 + rr * 128 + rp * 8 + (((rr >> 1) | ((rq & 1) << 1)) * 32);
  auto abase = [&](int c, int off) { return opaque((ldsp)(uintptr_t)((opq(rb0) ^ (u32)(c * 32)) + (u32)off)); };
  Acc acc[4];
  const char* stb = ws + OFF_ST2 + (size_t)tt * 2048;
  u32x4 sg4; u32x2 sg2;
  {
    float s1[2] = {0.f, 0.f}, s2[2] = {0.f, 0.f};
    ldsp pb[3];
    u32x4 pf[2][3];
    auto fetch1 = [&](auto ic) {
      constexpr int s = ic.value;
      if constexpr ((s & 15) == 0) {
#pragma unroll
        for (int c = 0; c < 3; ++c) pb[c] = abase(c, s * 4096);
      }
      constexpr int o = ((s & 15) >> 1) * 8192 + (s & 1) * 2048;
#pragma unroll
      for (int c = 0; c < 3; ++c) pf[s & 1][c] = tr_frag(pb[c] + o, pb[c] + (o + 512));
    };
    wait_vm2<3 * (G1_D - 1)>(h[0][0], h[1][0]);
    BARRIER();
    fetch1(IC<0>{});
    static_for<0, 36>([&](auto ic) {
      constexpr int s = ic.value, g = s >> 1, mt = s & 1;
      constexpr int SQ = 2 * (18 - G1_D);
      auto mm = [&](int c) {
#pragma unroll
        for (int tl = 0; tl < 2; ++tl) mma<g == 0>(acc[c].a[mt][tl], pf[s & 1][c], h[tl][g]);
      };
      if constexpr (mt == 1 && s < 35) {
        constexpr int last = cmin(g + G1_D, 18);
        constexpr int extra = (g + G1_D < 18) ? 2 : 3;
        wait_vm2<3 * (last - (g + 2)) + extra>(h[0][g + 1], h[1][g + 1]);
        BARRIER();
      }
      if constexpr (s < 35) fetch1(IC<s + 1>{});
      SCHED0();
      mm(0);
      if constexpr (g + G1_D < 18) { if constexpr (mt == 0) issue_dma(IC<g + G1_D>{}); else issue_h(IC<g + G1_D>{}, IC<1>{}); }
      else if constexpr (s == SQ) {
        const int ii = min(tid, 287), qd = ii / 72, r = ii - qd * 72;
        if (w < 5) dma16(ws + OFF_CS, (u32)(qd * 2304 + (r < 64 ? fg * 64 + r : 2048 + fg * 8 + (r - 64))) * 16u, L0 + G1_CP + w * 1024);
        dma16(q_src + (u32)rot(w >> 1) * (u32)(32 * RS), mk_qvoff(l), L0 + G1_QE + w * 1024);
      }
      SCHED0();
      mm(1);
      if constexpr (g + G1_D < 18 && mt == 0) issue_h(IC<g + G1_D>{}, IC<0>{});
      if constexpr (s == SQ) { const u32 so = ((rq >> 1) * 16 + (l & 15)) * 64 + (rq & 1) * 24; ld_sc1_x4(sg4, so, stb); ld_sc1_x2<16>(sg2, so, stb); }
      if constexpr (s < 6) stat_acc(fg, h[s & 1][s >> 1], s1[s & 1], s2[s & 1]);
      SCHED0();
      mm(2);
      if constexpr (s == 6) {
        const float a0 = swap32_sum(swap16_sum(s1[0])), c0 = swap32_sum(swap16_sum(s2[0]));
        const float a1 = swap32_sum(swap16_sum(s1[1])), c1 = swap32_sum(swap16_sum(s2[1]));
        u32x2 gr = {__float_as_uint((l & 16) ? a1 : a0), __float_as_uint((l & 16) ? c1 : c0)};
        st_sc1_x2_if(fg, stb + fg * 8, (u32)((l & 31) * 64), gr);
      }
      SCHED0();
      if constexpr (s % 12 == 11) { ST(2 + s / 12); }
    });
  }
  STR(28);
  u32x4 wf[4];
  ldsp fb = nullptr, fb2 = nullptr;
  auto fetch = [&](auto ic) {
    constexpr int i = ic.value, p = i / 36, s = i % 36;
    if constexpr (p != 1) {
      constexpr int c = (p == 0) ? 3 : p - 2;
      constexpr int pos = (p == 0) ? (s + 28) % 36 : s;
      if constexpr (s == 0 || (pos & 15) == 0) fb = abase(c, (pos & ~15) * 4096);
      constexpr int o = ((pos & 15) >> 1) * 8192 + (pos & 1) * 2048;
      wf[i % 3] = tr_frag(fb + o, fb + (o + 512));
    } else {
      if constexpr (s == 0 || s == 8) {
        const u32 l2 = opq(tid);
        const int qo = (l2 >> 4) & 1;
        const int qb = (8 * ((l2 >> 4) & 3) + ((l2 >> 2) & 3)) * 32 + (l2 & 3) * 8 + (s == 0 ? G1_QE : G1_QL);
        fb = opaque(LP + (qb + qo * 128)); fb2 = opaque(LP + (qb + (1 - qo) * 128));
      }
      constexpr int o = (s < 8 ? s : s - 8) * 1024;
      wf[i % 3] = tr_frag(fb + o, fb2 + o);
    }
  };
  fetch(IC<0>{}); fetch(IC<1>{});
  SCHED0();
  wait0_x6(sg4, sg2);
#pragma unroll 1
  for (int it = 0; it < (1 << 16); ++it) {
    const bool bad = (sg4[0] & sg4[1]) == 0xffffffffu || (sg4[2] & sg4[3]) == 0xffffffffu || (sg2[0] & sg2[1]) == 0xffffffffu;
    if (!__any(bad)) break;
    __builtin_amdgcn_s_sleep(2);
    const u32 so = ((rq >> 1) * 16 + (l & 15)) * 64 + (rq & 1) * 24;
    ld_sc1_x4(sg4, so, stb); ld_sc1_x2<16>(sg2, so, stb);
    wait0_x6(sg4, sg2);
  }
  float nmu[2], k1[2], k2[2], kd[2];
  {
    const float t1 = swap16_sum(__uint_as_float(sg4[0]) + __uint_as_float(sg4[2]) + __uint_as_float(sg2[0]));
    const float t2 = swap16_sum(__uint_as_float(sg4[1]) + __uint_as_float(sg4[3]) + __uint_as_float(sg2[1]));
    const float mu = hide(t1 * (1.f / D_));
    const float var = fmaxf(t2 * (1.f / D_) - mu * mu, 0.f) + 1e-5f;
    const auto m2 = __builtin_amdgcn_permlane32_swap(__float_as_uint(-mu), __float_as_uint(-mu), false, false);
    const auto v2 = __builtin_amdgcn_permlane32_swap(__float_as_uint(var), __float_as_uint(var), false, false);
#pragma unroll
    for (int tl = 0; tl < 2; ++tl) {
      kd[tl] = __uint_as_float(v2[tl]);
      const float r = __builtin_amdgcn_rsqf(kd[tl]);
      nmu[tl] = __uint_as_float(m2[tl]);
      k1[tl] = hide(GC * r);
      k2[tl] = hide(k1[tl] * 0.044715f * r * r);
    }
  }
  char* hdst = ws + OFF_HH + ((size_t)tt * 288 + (fg >> 1) * 9 + (fg & 1) * 4) * 1024;
  const ldsp csb = opaque(LP + (G1_CS + rq * 32));
  f32x4 cc;
  auto ldcs = [&](int lc, int hfx) { cc = *(const LAS f32x4*)(csb + (lc * 128 + hfx * 16)); };
  u32x4 eo[2];
  u32x2 eq[2];
  float m0, m1, tu0, tu1, y0, y1;
  auto emit = [&](int lc, int i, u32 pk) {
    const int tl = i & 1, hx = i >> 1;
    if (lc == 8) {
      eq[tl][hx] = pk;
      if (hx) st_sc1_x2(hdst + (size_t)tl * 147456 + (8 - (fg & 1) * 4) * 1024 + (fg & 1) * 8, lo16, eq[tl]);
    } else {
      eo[tl][(lc & 1) * 2 + hx] = pk;
      if ((lc & 1) && hx) st_sc1_x4(hdst + (size_t)tl * 147456 + (lc >> 1) * 1024, lo16, eo[tl]);
    }
  };
  auto epi = [&](const Acc& a, int lc, int st, int nlc) {
    const int i = st / 6, sg = st % 6, tl = i & 1, e = (i >> 1) * 2;
    if (sg == 0) {
      if (i > 0) { u32 pk = pack2(tu0, tu1 * y1); pin(pk); emit(lc, i - 1, pk); }
      if (i < 4) { m0 = fmaf(nmu[tl], cc[0], a.a[0][tl][e]); m1 = fmaf(nmu[tl], cc[1], a.a[0][tl][e + 1]); pinf(m0); pinf(m1); }
    } else if (sg == 1) {
      tu0 = fmaf(nmu[tl], cc[2], a.a[1][tl][e]); tu1 = fmaf(nmu[tl], cc[3], a.a[1][tl][e + 1]);
      y0 = hide(m0 * m0); y1 = m1 * m1;
      pinf(tu0); pinf(tu1); pinf(y0); pinf(y1);
      if (i == 1) ldcs(lc, 1);
      if (i == 3 && nlc >= 0) ldcs(nlc, 0);
    } else if (sg == 2) {
      y0 = hide(fmaf(y0, k2[tl], k1[tl])); y1 = fmaf(y1, k2[tl], k1[tl]); tu0 = hide(tu0 * m0); tu1 = tu1 * m1;
      pinf(y0); pinf(y1); pinf(tu0); pinf(tu1);
    } else if (sg == 3) {
      y0 = hide(y0 * m0); y1 = y1 * m1; y0 = __builtin_amdgcn_exp2f(y0);
      pinf(y0); pinf(y1);
    } else if (sg == 4) {
      y1 = __builtin_amdgcn_exp2f(y1); y0 = __builtin_amdgcn_rcpf(fmaf(y0, kd[tl], kd[tl]));
      pinf(y0); pinf(y1);
    } else {
      y1 = __builtin_amdgcn_rcpf(fmaf(y1, kd[tl], kd[tl])); tu0 = tu0 * y0;
      pinf(y1); pinf(tu0);
    }
  };
  const int r5 = b >> 3;
  const int ks = b & 3, tg2 = ((b >> 2) & 1) * 8 + (r5 & 7), dt = r5 >> 3;
  const int tq = w & 3, kh = w >> 2;
  constexpr u32 RD = D_ * 2;
  const char* d_src = (const char*)wd + dt * 256;
  const char* dq_src = (const char*)wd + 1024 + dt * 32;
  const u32 d_dst = L0 + kh * 9216 + tq * 2048;
  const int mb = ks * 4 + kh * 2;
  auto row0 = [&](int kk) { const int q = kk / 9, i = kk - 9 * q; return i < 8 ? (mb + q) * 256 + i * 32 : 4096 + (mb + q) * 32; };
  u32x4 hb[HB][2];
  const char* hsrc0 = ws + OFF_HH + ((size_t)(tg2 * 8 + tq * 2 + kh) * 144 + mb * 9) * 1024;
  const char* hsrc1 = ws + OFF_HH + ((size_t)(tg2 * 8 + tq * 2 + (kh ^ 1)) * 144 + mb * 9) * 1024;
  const char* myflag = ws + OFF_FLG + 16384 + b * 64;
  const char* pflags = ws + OFF_FLG + 16384 + ((b & 7) + kh * 32 + ((r5 & 7) >> 1) * 64) * 64;
  u32 d_voff = 0, dq_voff = 0, p_voff = 0, pv = 0, q_voff = 0;
  f32x4 ac2[9][2];
  ldsp pb[5];
  u32 rb2 = 0, rbq = 0;
  auto fetch2 = [&](auto ic) {
    constexpr int i = ic.value, s = i / 9, c = i % 9, stg = s % NST;
    if constexpr (c == 0 && (stg % 3 == 0)) {
#pragma unroll
      for (int u = 0; u < 4; ++u) pb[u] = opaque((ldsp)(uintptr_t)((opq(rb2) ^ (u32)(u * 32)) + (u32)(stg * STG)));
      pb[4] = opaque((ldsp)(uintptr_t)(opq(rbq) + (u32)(stg * STG)));
    }
    constexpr int o = (stg % 3) * STG;
    if constexpr (c < 8) wf[i & 3] = tr_frag(pb[c & 3] + (o + (c >> 2) * 1024), pb[c & 3] + (o + (c >> 2) * 1024 + 4096));
    else if constexpr (s < 4) { const ldsp qe = (ldsp)(uintptr_t)(opq(rbq) + (u32)(G1_QE - 8192) - kh * (9216 - 1024)); wf[i & 3] = tr_frag(qe + s * 2048, qe + (s * 2048 + 512)); }
    else wf[i & 3] = tr_frag(pb[4] + o, pb[4] + (o + 512));
  };
  auto issue2 = [&](auto uc) {
    constexpr int u = uc.value;
    static_for<0, 18>([&](auto nc) {
      constexpr int n = nc.value, kk = kk_of(n);
      if constexpr (w_issue(n, 0) == u) dma16(d_src + (u32)row0(kk) * RD, d_voff, d_dst + (n % NST) * STG);
      if constexpr (w_issue(n, 1) == u) dma16(d_src + (u32)row0(kk) * RD + 128, d_voff, d_dst + (n % NST) * STG + 1024);
      if constexpr (h_issue(n, 0) == u) gload16_sc1<(kk & 3) * 1024>(hb[n % HB][0], lo16, hsrc0 + (kk >> 2) * 4096);
      if constexpr (h_issue(n, 1) == u) gload16_sc1<(kk & 3) * 1024>(hb[n % HB][1], lo16, hsrc1 + (kk >> 2) * 4096);
    });
    static_for<0, 5>([&](auto rc) {
      constexpr int r = rc.value;
      if constexpr (dq_issue(r) == u) {
        const int tsel = r == 4 ? (tq & 1) : tq;
        const int ra = row0(kk_of(4 * r)), rb = row0(kk_of(4 * r + 1)), rc_ = row0(kk_of(r == 4 ? 16 : 4 * r + 2)), rd = row0(kk_of(r == 4 ? 17 : 4 * r + 3));
        const int rsel = (tsel & 2) ? ((tsel & 1) ? rd : rc_) : ((tsel & 1) ? rb : ra);
        dma16(dq_src + (u32)rsel * RD, dq_voff, r == 0 ? L0 + G1_QE + (tq * 2 + kh) * 1024 : L0 + (((4 * r) % NST) + tsel) * STG + kh * 9216 + 8192);
      }
    });
    static_for<0, 3>([&](auto pc) {
      constexpr int p = pc.value;
      if constexpr (poll_iss(p) == u) poll_issue(pv, p_voff, pflags);
      if constexpr (poll_chk(p) == u) poll_check<wait_k(4 * poll_iss(p) + 2, 4 * u + 2), p + 1>(pv, p_voff, pflags);
    });
  };
  static_for<0, 216>([&](auto ic) {
    constexpr int t = ic.value, p = t / 36, s = t % 36;
    constexpr int slot = (p + 3) & 3;
    constexpr int hg = (p == 0) ? ((s >> 1) + 14) % 18 : (s >> 1);
    if constexpr (t + 2 < 216) fetch(IC<t + 2>{});
#pragma unroll
    for (int tl = 0; tl < 2; ++tl) mma<(s < 2)>(acc[slot].a[s & 1][tl], wf[t % 3], h[tl][hg]);
    if constexpr (t == 2) {
      const u32 pi = min(opq(tid), 71u);
      const ldsp cp = LP + (G1_CP + pi * 16);
      const f32x4 r = (*(const LAS f32x4*)cp + *(const LAS f32x4*)(cp + 1152)) + (*(const LAS f32x4*)(cp + 2304) + *(const LAS f32x4*)(cp + 3456));
      *(LAS f32x4*)(LP + (G1_CS + pi * 16)) = r;
    }
    if constexpr (t == G1_E0 - 1) ldcs(0, 0);
    if constexpr (t >= G1_E0) {
      constexpr int ci = (t - G1_E0) / 26, st = (t - G1_E0) % 26;
      if constexpr (ci < 8 && st <= 24) {
        constexpr int lc = ci < 4 ? ci : (ci == 4 ? 8 : ci - 1);
        constexpr int nlc = ci < 3 ? ci + 1 : (ci == 3 ? 8 : ci);
        epi(acc[ci & 3], lc, st, nlc);
      }
    }
    if constexpr (t == 1 || t == 28 || t == 72 || t == 190) { SCHED0(); BARRIER(); }
    if constexpr (t == 6) { SCHED0(); LDS_BARRIER(); }
    if constexpr (t == 7) { SCHED0(); BARRIER(); }
    if constexpr (t == 41) { wait_vm<wait_q(1, 41)>(); BARRIER(); }
    if constexpr (t == 57) { wait_vm<wait_q(3, 57)>(); BARRIER(); }
    if constexpr (t == 69) { wait_vm<wait_b(9, 69)>(); BARRIER(); }
    if constexpr (t == 89) { wait_vm<wait_b(14, 89)>(); BARRIER(); }
    if constexpr (t == 99) { wait_vm<wait_b(17, 99)>(); BARRIER(); }
    if constexpr (t == 162) { wait_vm<wait_k(4 * (epi_start(4) + 24), 4 * t + 1)>(); BARRIER(); flag_store(w, opq(0u), opq(1u), myflag); }
    if constexpr (t == 198) { SCHED0(); BARRIER(); }
    if constexpr (t == 208) { SCHED0(); wait_vm<wait_k(4 * w_issue(1, 1) + 2, 4 * t + 1)>(); BARRIER(); }
    if constexpr (t == 7) q_voff = mk_qvoff(opq(tid) & 63);
    if constexpr (t == 180) {
      const u32 l2 = opq(tid) & 63;
      d_voff = (u32)((8 * tq + (l2 >> 3)) * RD + ((((l2 & 7) >> 1) ^ ((l2 >> 4) & 3)) * 32) + (l2 & 1) * 16);
      dq_voff = (u32)((l2 >> 1) * RD + (l2 & 1) * 16);
      p_voff = (l2 & 3) * 512;
      pin(d_voff); pin(dq_voff); pin(p_voff);
    }
    static_for<0, 4>([&](auto rc) {
      constexpr int r = rc.value;
      if constexpr (g1_q_issue(0, r) == t) dma16(q_src + (u32)rot(4 + 4 * r + (w >> 1)) * (u32)(32 * RS), q_voff, L0 + G1_QL + r * 8192 + w * 1024);
    });
    static_for<0, 18>([&](auto jc) {
      constexpr int j = jc.value;
      if constexpr (g1_b_issue(0, j) == t) dma16(a_src + 128 + (u32)rot(j) * (u32)(32 * RS), ab_voff, ab_dst + j * 8192);
    });
    if constexpr (t >= PF_ISS0 && t < PF_ISS0 + 20 && (t - PF_ISS0) % 4 == 0) {
      const int piece = ((b >> 3) * 5 + (t - PF_ISS0) / 4) * 8 + w;
      const int byte = piece * 1024;
      const char* src = (const char*)wd + (byte < 1024 * 1152 ? (b & 3) * 1024 * 1152 + byte : 4096 * 1152 + (b & 3) * 128 * 1152 + (byte - 1024 * 1152));
      dma16(src, lo16, L0 + G1_QE + w * 1024);
    }
    if constexpr (t >= 180) issue2(ic);
    SCHED0();
    if constexpr (t % 24 == 23) { ST(5 + t / 24); }
  });
  STR(29);
  {
    const u32 l2 = opq(tid) & 63;
    const u32 q2 = l2 >> 4, r2_ = (l2 >> 2) & 3, p2 = l2 & 3;
    rb2 = L0 + kh * 9216 + (q2 >> 1) * 2048 + (4 * (q2 & 1) + r2_) * 128 + ((2 * (q2 & 1) + (r2_ >> 1)) * 32) + p2 * 8;
    rbq = L0 + kh * 9216 + 8192 + (4 * q2 + r2_) * 32 + p2 * 8;
  }
  const int ft = tg2 * 8 + ks * 2 + kh;
  const int dcol = dt * 128 + tq * 32 + 4 * rq;
  const int qcol = 512 + dt * 16 + 4 * rq;
  u32x2 r0, r1, r2;
  fetch2(IC<0>{}); fetch2(IC<1>{}); fetch2(IC<2>{});
  wait_vm2<wait_k(4 * h_issue(0, 1) + 2, 4 * U2 - 1)>(hb[0][0], hb[0][1]);
  static_for<0, 162>([&](auto ic) {
    constexpr int i = ic.value, s = i / 9, c = i % 9, u = U2 + i;
    if constexpr (c == 6 && s < 17) {
      constexpr int tgt = cmax(cmax(cmax(4 * h_issue(s + 1, 1) + 2, 4 * w_issue(s + 1, 1) + 2), 4 * dq_issue((s + 1) >> 2) + 2), s == 6 ? 4 * (epi_start(8) + 24) : 0);
      wait_vm2<wait_k(tgt, 4 * u - 1)>(hb[(s + 1) % HB][0], hb[(s + 1) % HB][1]);
      BARRIER();
      if constexpr (s == 0) flag_store(w, opq(0u), opq(2u), myflag);
      if constexpr (s == 6) flag_store(w, opq(0u), opq(3u), myflag);
    }
    if constexpr (i + 3 < 162) fetch2(IC<i + 3>{});
#pragma unroll
    for (int tl = 0; tl < 2; ++tl) mma<s == 0>(ac2[c][tl], wf[i & 3], hb[s % HB][tl]);
    if constexpr (i >= 2 && i <= 26) epi(acc[0], 7, i - 2, -1);
    issue2(IC<u>{});
    if constexpr (u == RES_ISS) {
      const char* hb_ = ws + OFF_HF + (size_t)ft * 18432;
      auto po = [&](int d) { return (u32)(((d >> 5) * 64 + ((d >> 3) & 3) * 16 + (l & 15)) * 16 + (d & 4) * 2); };
      gload8(r0, po(dcol), hb_); gload8(r1, po(dcol + 16), hb_); gload8(r2, po(qcol), hb_);
    }
    SCHED0();
    if constexpr (c == 8 && s % 3 == 2) { ST(14 + s / 3); }
  });
  MFMA_DRAIN(); SCHED0();
  {
    const ldsp xw = LP + (2 * STG + w * 9216 + l * 16);
#pragma unroll
    for (int c = 0; c < 9; ++c) *(LAS f32x4*)(xw + c * 1024) = ac2[c][1];
  }
  LDS_BARRIER();
  f32x4 sm[9];
  {
    const ldsp xr = LP + (2 * STG + (w ^ 4) * 9216 + l * 16);
#pragma unroll
    for (int c = 0; c < 9; ++c) sm[c] = ac2[c][0] + *(const LAS f32x4*)(xr + c * 1024);
  }
  ST(20);
  char* part = ws + OFF_PART;
  if (tq != ks) {
    char* pdst = part + ((((size_t)(tg2 * 4 + dt) * 4 + tq) * 4 + ks) * 2 + kh) * 4608;
#pragma unroll
    for (int pc = 0; pc < 4; ++pc) {
      u32x4 v = {pack2(sm[2 * pc][0], sm[2 * pc][1]), pack2(sm[2 * pc][2], sm[2 * pc][3]), pack2(sm[2 * pc + 1][0], sm[2 * pc + 1][1]), pack2(sm[2 * pc + 1][2], sm[2 * pc + 1][3])};
      st_sc1_x4(pdst + pc * 1024, lo16, v);
    }
    u32x2 v2 = {pack2(sm[8][0], sm[8][1]), pack2(sm[8][2], sm[8][3])};
    st_sc1_x2(pdst + 4096, l * 8, v2);
  } else {
    const ldsp ow = LP + (kh * 9216 + l * 16);
#pragma unroll
    for (int c = 0; c < 9; ++c) *(LAS f32x4*)(ow + c * 1024) = sm[c];
  }
  asm volatile("s_waitcnt vmcnt(0)" : "+v"(r0), "+v"(r1), "+v"(r2)::"memory");
  LDS_BARRIER();
  ST(21);
  const char* fl = ws + OFF_FLG + (tg2 * 4 + dt) * 256;
  flag_store(w, opq(0u), opq(1u), fl + ks * 64);
  const ldsp orp = LP + (kh * 9216 + l * 16);
  f32x4 o0 = *(const LAS f32x4*)(orp + tq * 2048), o1 = *(const LAS f32x4*)(orp + (tq * 2048 + 1024)), o2 = *(const LAS f32x4*)(orp + 8192);
  o0[0] += bflo(r0[0]); o0[1] += bfhi(r0[0]); o0[2] += bflo(r0[1]); o0[3] += bfhi(r0[1]);
  o1[0] += bflo(r1[0]); o1[1] += bfhi(r1[0]); o1[2] += bflo(r1[1]); o1[3] += bfhi(r1[1]);
  o2[0] += bflo(r2[0]); o2[1] += bfhi(r2[0]); o2[2] += bflo(r2[1]); o2[3] += bfhi(r2[1]);
  {
    const u32 fo = (l & 3) * 64;
    poll_issue(pv, fo, fl);
    poll_check<0, 1>(pv, fo, fl);
  }
  ST(22);
  {
    const char* psrc = part + (((size_t)(tg2 * 4 + dt) * 4 + ks) * 4 * 2 + kh) * 4608;
    u32x4 v[3]; u32x2 vq[3];
#pragma unroll
    for (int j = 0; j < 3; ++j) {
      const int src = (ks + 1 + j) & 3;
      ld_sc1_x4(v[j], lo16 + tq * 1024, psrc + src * 9216);
      ld_sc1_x2<0>(vq[j], l * 8 + 4096, psrc + src * 9216);
    }
    asm volatile("s_waitcnt vmcnt(0)" : "+v"(v[0]), "+v"(v[1]), "+v"(v[2]), "+v"(vq[0]), "+v"(vq[1]), "+v"(vq[2])::"memory");
#pragma unroll
    for (int j = 0; j < 3; ++j) {
      o0[0] += bflo(v[j][0]); o0[1] += bfhi(v[j][0]); o0[2] += bflo(v[j][1]); o0[3] += bfhi(v[j][1]);
      o1[0] += bflo(v[j][2]); o1[1] += bfhi(v[j][2]); o1[2] += bflo(v[j][3]); o1[3] += bfhi(v[j][3]);
      o2[0] += bflo(vq[j][0]); o2[1] += bfhi(vq[j][0]); o2[2] += bflo(vq[j][1]); o2[3] += bfhi(vq[j][1]);
    }
    u16* op = out + (size_t)(ft * 16 + (l & 15)) * D_;
    u32x2 oa = {pack2(o0[0], o0[1]), pack2(o0[2], o0[3])}, ob = {pack2(o1[0], o1[1]), pack2(o1[2], o1[3])}, oc = {pack2(o2[0], o2[1]), pack2(o2[2], o2[3])};
    *(u32x2*)(op + dcol) = oa; *(u32x2*)(op + dcol + 16) = ob;
    if (tq == 0) *(u32x2*)(op + qcol) = oc;
  }
  ST(23); ST_DRAIN(); ST(24);
  ST_FLUSH((u32*)(ws + (200u << 20)), b * 8 + w);
}

extern "C" void kernel_launch(void* const* d_in, const int* in_sizes, int n_in, void* d_out, int out_size, void* d_ws, size_t ws_size,
                              hipStream_t stream) {
  const u16* q = (const u16*)d_in[0]; const u16* k = (const u16*)d_in[1]; const u16* v = (const u16*)d_in[2];
  const u16* shk = (const u16*)d_in[3]; const u16* ssm = (const u16*)d_in[4]; const u16* ssmD = (const u16*)d_in[5];
  const u16* wg = (const u16*)d_in[7]; const u16* wu = (const u16*)d_in[8]; const u16* wd = (const u16*)d_in[9];
  char* ws = (char*)d_ws;
  (void)hipFuncSetAttribute((const void*)k1_conv, hipFuncAttributeMaxDynamicSharedMemorySize, K1_LDS);
  (void)hipFuncSetAttribute((const void*)ffn_fused, hipFuncAttributeMaxDynamicSharedMemorySize, FF_LDS);
  k0_prep<<<K0_GRID, 256, 0, stream>>>(q, k, v, shk, ssm, wg, wu, ws);
  k1_conv<<<K1_GRID, 512, K1_LDS, stream>>>(ssm, ssmD, wg, wu, ws);
  ffn_fused<<<256, 512, FF_LDS, stream>>>(wg, wu, wd, ws, (u16*)d_out);
}
```

```cpp
#include <hip/hip_runtime.h>
#include <hip/hip_bf16.h>
#include <stdint.h>
#define STAG 0

typedef unsigned short u16;
typedef unsigned int u32;
typedef __attribute__((ext_vector_type(2))) unsigned u32x2;
typedef __attribute__((ext_vector_type(4))) unsigned u32x4;
typedef __attribute__((ext_vector_type(2))) float f32x2;
typedef __attribute__((ext_vector_type(4))) float f32x4;
typedef __attribute__((ext_vector_type(8))) __bf16 bf16x8;
typedef __attribute__((ext_vector_type(2))) __bf16 bf16x2;
typedef __attribute__((ext_vector_type(4))) short s16x4;
#define GAS __attribute__((address_space(1)))
#define LAS __attribute__((address_space(3)))
#define DI __device__ __forceinline__
typedef LAS char* ldsp;

constexpr int S_ = 2048, D_ = 576, DF_ = 4608;
constexpr size_t OFF_KVT = 0;
constexpr size_t OFF_XP  = OFF_KVT + (size_t)D_ * S_ * 2;
constexpr size_t OFF_HF  = OFF_XP + (size_t)D_ * S_ * 2;
constexpr size_t OFF_ST1 = OFF_HF + (size_t)D_ * S_ * 2;
constexpr size_t OFF_CS  = OFF_ST1 + 2048 * 8;
constexpr size_t OFF_FLG = OFF_CS + 4 * 2 * 4608 * 4;
constexpr size_t OFF_ST2 = OFF_FLG + 32768;
constexpr size_t OFF_HH  = OFF_ST2 + 131072;
constexpr size_t OFF_PART = OFF_HH + (size_t)S_ * DF_ * 2;

DI float bflo(u32 w) { return __uint_as_float(w << 16); }
DI float bfhi(u32 w) { return __uint_as_float(w & 0xffff0000u); }
DI u32 pack2(float a, float b) { bf16x2 p; p[0] = (__bf16)a; p[1] = (__bf16)b; return __builtin_bit_cast(u32, p); }

template <int I> struct IC { static constexpr int value = I; };
template <int B, int E, class F> DI void static_for(F&& f) { if constexpr (B < E) { f(IC<B>{}); static_for<B + 1, E>(f); } }
template <int N> DI void wait_vm() { asm volatile("s_waitcnt vmcnt(%0)" ::"n"(N) : "memory"); }
#define BARRIER() do { asm volatile("" ::: "memory"); __builtin_amdgcn_s_barrier(); asm volatile("" ::: "memory"); } while (0)
#define LDS_BARRIER() do { asm volatile("s_waitcnt lgkmcnt(0)" ::: "memory"); __builtin_amdgcn_s_barrier(); asm volatile("" ::: "memory"); } while (0)
#define SCHED0() __builtin_amdgcn_sched_barrier(0)
constexpr int cmin(int a, int b) { return a < b ? a : b; }

DI u32x4 tr_frag(ldsp a0, ldsp a1) {
  const s16x4 lo = __builtin_amdgcn_ds_read_tr16_b64_v4i16((LAS s16x4*)a0);
  const s16x4 hi = __builtin_amdgcn_ds_read_tr16_b64_v4i16((LAS s16x4*)a1);
  return __builtin_bit_cast(u32x4, __builtin_shufflevector(lo, hi, 0, 1, 2, 3, 4, 5, 6, 7));
}
DI f32x4 mfma16(const u32x4& a, const u32x4& b, const f32x4& c) {
  return __builtin_amdgcn_mfma_f32_16x16x32_bf16(__builtin_bit_cast(bf16x8, a), __builtin_bit_cast(bf16x8, b), c, 0, 0, 0);
}
template <bool FIRST> DI void mma(f32x4& c, const u32x4& a, const u32x4& b) {
  if constexpr (FIRST) asm volatile("v_mfma_f32_16x16x32_bf16 %0, %1, %2, 0" : "=&v"(c) : "v"(a), "v"(b));
  else asm volatile("v_mfma_f32_16x16x32_bf16 %0, %1, %2, %0" : "+v"(c) : "v"(a), "v"(b));
}
#define MFMA_DRAIN() asm volatile("s_nop 15" ::: "memory")
template <int OFF> DI void gload16_sc1(u32x4& dst, u32 voff, const char* sbase) {
  asm volatile("global_load_dwordx4 %0, %1, %2 offset:%3 sc1" : "=v"(dst) : "v"(voff), "s"(sbase), "n"(OFF) : "memory");
}
DI void poll_issue(u32& f, u32 voff, const char* sb) { asm volatile("global_load_dword %0, %1, %2 sc1" : "=v"(f) : "v"(voff), "s"(sb) : "memory"); }
template <int N, int PH> DI void poll_check(u32& f, u32 voff, const char* sb) {
  u32 cnt = 1u << 18;
  asm volatile("s_waitcnt vmcnt(%4)\n"
               "1:\n\tv_cmp_gt_u32_e32 vcc, %5, %0\n\ts_nop 4\n\ts_cbranch_vccz 2f\n\t"
               "s_sleep 1\n\tglobal_load_dword %0, %2, %3 sc1\n\ts_waitcnt vmcnt(0)\n\t"
               "s_sub_u32 %1, %1, 1\n\ts_cbranch_scc0 1b\n"
               "2:" : "+v"(f), "+s"(cnt) : "v"(voff), "s"(sb), "n"(N), "n"(PH) : "memory", "vcc", "scc");
}
DI void flag_store(int w, u32 zero_off, u32 val, const char* sb) {
  asm volatile("s_cmp_lg_u32 %3, 0\n\ts_cbranch_scc1 1f\n\tglobal_store_dword %0, %1, %2 sc1\n1:" ::"v"(zero_off), "v"(val), "s"(sb), "s"(w) : "memory", "scc");
}
template <int OFF> DI void gload16(u32x4& dst, u32 voff, const char* sbase) {
  asm volatile("global_load_dwordx4 %0, %1, %2 offset:%3" : "=v"(dst) : "v"(voff), "s"(sbase), "n"(OFF) : "memory");
}
DI void gload8(u32x2& dst, u32 voff, const char* sbase) { asm volatile("global_load_dwordx2 %0, %1, %2" : "=v"(dst) : "v"(voff), "s"(sbase) : "memory"); }
template <int N, int VAL> DI void waitbar_if(int sel, u32x4& a, u32x4& b) {
  asm volatile("s_cmp_lg_u32 %2, %4\n\ts_cbranch_scc1 1f\n\ts_waitcnt vmcnt(%3)\n\ts_barrier\n1:" : "+v"(a), "+v"(b) : "s"(sel), "n"(N), "n"(VAL) : "memory", "scc");
}
template <int N> DI void wait_vm2(u32x4& a, u32x4& b) { asm volatile("s_waitcnt vmcnt(%2)" : "+v"(a), "+v"(b) : "n"(N) : "memory"); }
DI void dma16(const char* sbase, u32 voff, u32 lds_dst) {
  asm volatile("s_mov_b32 m0, %2\n\ts_nop 0\n\tglobal_load_lds_dwordx4 %0, %1" ::"v"(voff), "s"(sbase), "s"(lds_dst) : "memory");
}
template <int VAL> DI void dma16_if(int sel, const char* sbase, u32 voff, u32 lds_dst) {
  asm volatile("s_cmp_eq_u32 %3, %4\n\ts_mov_b32 m0, %2\n\ts_cselect_b64 exec, -1, 0\n\tglobal_load_lds_dwordx4 %0, %1\n\ts_mov_b64 exec, -1" ::"v"(voff), "s"(sbase), "s"(lds_dst), "s"(sel), "n"(VAL) : "memory", "scc");
}
DI ldsp opaque(ldsp p) { asm volatile("" : "+v"(p)); return p; }
DI u32 opq(u32 p) { asm volatile("" : "+v"(p)); return p; }
DI void pin(u32& p) { asm volatile("" : "+v"(p)); }
DI void pinf(float& p) { asm volatile("" : "+v"(p)); }
DI float hide(float p) { asm("" : "+v"(p)); return p; }
DI float swap16_sum(float x) { const auto r = __builtin_amdgcn_permlane16_swap(__float_as_uint(x), __float_as_uint(x), false, false); return __uint_as_float(r[0]) + __uint_as_float(r[1]); }
DI float swap32_sum(float x) { const auto r = __builtin_amdgcn_permlane32_swap(__float_as_uint(x), __float_as_uint(x), false, false); return __uint_as_float(r[0]) + __uint_as_float(r[1]); }
#define STAT1(R) "v_lshlrev_b32_e32 %0, 16, " R "\n\tv_and_b32_e32 %1, %8, " R "\n\tv_add_f32_e32 %2, %2, %0\n\tv_fmac_f32_e32 %3, %0, %0\n\tv_add_f32_e32 %2, %2, %1\n\tv_fmac_f32_e32 %3, %1, %1\n\t"
DI void stat_acc(int fg, const u32x4& v, float& s1, float& s2) {
  float x, y;
  asm volatile("s_cmp_gt_u32 %9, 5\n\ts_cbranch_scc1 1f\n\t" STAT1("%4") STAT1("%5") STAT1("%6") STAT1("%7") "\n1:"
               : "=&v"(x), "=&v"(y), "+v"(s1), "+v"(s2) : "v"(v[0]), "v"(v[1]), "v"(v[2]), "v"(v[3]), "s"(0xffff0000u), "s"(fg) : "scc");
}
DI void ld_sc1_x4(u32x4& d, u32 voff, const char* sb) { asm volatile("global_load_dwordx4 %0, %1, %2 sc1" : "=v"(d) : "v"(voff), "s"(sb) : "memory"); }
template <int OFF> DI void ld_sc1_x2(u32x2& d, u32 voff, const char* sb) { asm volatile("global_load_dwordx2 %0, %1, %2 offset:%3 sc1" : "=v"(d) : "v"(voff), "s"(sb), "n"(OFF) : "memory"); }
DI void wait0_x6(u32x4& a, u32x2& b) { asm volatile("s_waitcnt vmcnt(0)" : "+v"(a), "+v"(b)::"memory"); }
DI void st_sc1_x4(char* sb, u32 voff, const u32x4& v) { asm volatile("global_store_dwordx4 %0, %1, %2 sc1\n\ts_nop 1" ::"v"(voff), "v"(v), "s"(sb) : "memory"); }
DI void gstore16(char* sb, u32 voff, const u32x4& v) { asm volatile("global_store_dwordx4 %0, %1, %2\n\ts_nop 1" ::"v"(voff), "v"(v), "s"(sb) : "memory"); }
DI void gstore8(char* sb, u32 voff, const u32x2& v) { asm volatile("global_store_dwordx2 %0, %1, %2" ::"v"(voff), "v"(v), "s"(sb) : "memory"); }
DI void st_sc1_x2_if(int skip, const char* sb, u32 voff, const u32x2& v) {
  asm volatile("s_cmp_gt_u32 %3, 5\n\ts_cbranch_scc1 1f\n\tglobal_store_dwordx2 %0, %1, %2 sc1\n1:" ::"v"(voff), "v"(v), "s"(sb), "s"(skip) : "memory", "scc");
}
DI void st_sc1_x2(char* sb, u32 voff, const u32x2& v) { asm volatile("global_store_dwordx2 %0, %1, %2 sc1" ::"v"(voff), "v"(v), "s"(sb) : "memory"); }

#define STR(i)
#define ST(i)
#define ST_DECL
#define ST_FLUSH(dbg, wv)
#define ST_DRAIN()

DI void colsum_slice(const u16* __restrict__ wg, const u16* __restrict__ wu, float* __restrict__ cs, int sl, int l) {
  const int mat = sl & 1, qr = (sl >> 1) & 3, cb = sl >> 3;
  const u16* p = (mat ? wu : wg) + (size_t)(qr * 144 + (l >> 3)) * DF_ + cb * 64 + (l & 7) * 8;
  u32x4 x[18];
#pragma unroll
  for (int i = 0; i < 18; ++i) x[i] = *(const u32x4*)(p + (size_t)i * 8 * DF_);
  float a[8];
#pragma unroll
  for (int e = 0; e < 8; ++e) a[e] = 0.f;
#pragma unroll
  for (int i = 0; i < 18; ++i)
#pragma unroll
    for (int e = 0; e < 4; ++e) { a[2 * e] += bflo(x[i][e]); a[2 * e + 1] += bfhi(x[i][e]); }
#pragma unroll
  for (int e = 0; e < 8; ++e) { a[e] += __shfl_xor(a[e], 8); a[e] = swap32_sum(swap16_sum(a[e])); }
  if (l < 8) {
    float* d = cs + qr * 9216 + (cb * 32 + l * 4) * 4 + mat * 2;
#pragma unroll
    for (int e = 0; e < 4; ++e) { f32x2 o = {a[2 * e], a[2 * e + 1]}; *(f32x2*)(d + e * 4) = o; }
  }
}

#define K0_SL 0
constexpr int K0_GRID = 288 + 64 + K0_SL / 4;
extern "C" __global__ __launch_bounds__(256) void k0_prep(const u16* __restrict__ q, const u16* __restrict__ k, const u16* __restrict__ v,
                                               const u16* __restrict__ shk, const u16* __restrict__ ssm, const u16* __restrict__ wg,
                                               const u16* __restrict__ wu, char* __restrict__ ws) {
  ST_DECL; ST(0);
  asm volatile("" ::"s"(q), "s"(k), "s"(v), "s"(shk), "s"(ssm), "s"(wg), "s"(wu), "s"(ws));
  u16* __restrict__ kvt = (u16*)(ws + OFF_KVT);
  u16* __restrict__ xp = (u16*)(ws + OFF_XP);
  const int b = blockIdx.x, tid = threadIdx.x, w = tid >> 6, l = tid & 63;
  if (b < 288) {
    const int tb = b / 9, cb = b - tb * 9;
    const int s0 = tb * 64 + w * 16 + (l >> 4) * 4;
    const int c0 = cb * 64 + (l & 15) * 4;
    u32x2 kr[7], vr[4], qr[4];
#pragma unroll
    for (int i = 0; i < 7; ++i) {
      const int s = s0 - 3 + i;
      kr[i] = *(const u32x2*)(k + (size_t)(s < 0 ? 0 : s) * D_ + c0);
    }
#pragma unroll
    for (int i = 0; i < 4; ++i) vr[i] = *(const u32x2*)(v + (size_t)(s0 + i) * D_ + c0);
    const u32x4 w0 = *(const u32x4*)(shk + c0 * 4), w1 = *(const u32x4*)(shk + c0 * 4 + 8);
#pragma unroll
    for (int i = 0; i < 4; ++i) qr[i] = *(const u32x2*)(q + (size_t)(s0 + i) * D_ + c0);
    u32x4 pf0, pf1;
    gload16<0>(pf0, (u32)(b * 8192 + tid * 16), (const char*)ssm);
    gload16<0>(pf1, (u32)(b * 8192 + 4096 + tid * 16), (const char*)ssm);
    if (s0 == 0) { kr[0] = (u32x2){0u, 0u}; kr[1] = kr[0]; kr[2] = kr[0]; }
#pragma unroll
    for (int c = 0; c < 4; ++c) {
      const u32 wa = (c < 2) ? w0[2 * (c & 1)] : w1[2 * (c & 1)], wb = (c < 2) ? w0[2 * (c & 1) + 1] : w1[2 * (c & 1) + 1];
      const float f0 = bflo(wa), f1 = bfhi(wa), f2 = bflo(wb), f3 = bfhi(wb);
      float kk[7], kvv[4];
#pragma unroll
      for (int i = 0; i < 7; ++i) kk[i] = (c & 1) ? bfhi(kr[i][c >> 1]) : bflo(kr[i][c >> 1]);
#pragma unroll
      for (int i = 0; i < 4; ++i) {
        const float ks = kk[i + 3] * f0 + kk[i + 2] * f1 + kk[i + 1] * f2 + kk[i] * f3;
        const float vv = (c & 1) ? bfhi(vr[i][c >> 1]) : bflo(vr[i][c >> 1]);
        kvv[i] = ks * vv;
      }
      u32x2 o = {pack2(kvv[3], kvv[2]), pack2(kvv[1], kvv[0])};
      u16* dst = kvt + (size_t)(c0 + c) * S_ + (s0 >> 3) * 8 + ((s0 & 4) ? 0 : 4);
      *(u32x2*)dst = o;
    }
    u32x4 xa = {qr[0][0], qr[0][1], qr[1][0], qr[1][1]}, xb = {qr[2][0], qr[2][1], qr[3][0], qr[3][1]};
    u16* xdst = xp + ((size_t)(c0 >> 2) * S_ + s0) * 4;
    *(u32x4*)xdst = xa; *(u32x4*)(xdst + 8) = xb;
    if (b < 40) {
      const u32 f = b < 8 ? 0u : 0xffffffffu;
      u32x4 z = {f, f, f, f};
      ((u32x4*)(ws + OFF_FLG))[b * 256 + tid] = z;
    }
    asm volatile("s_waitcnt vmcnt(0)" : "+v"(pf0), "+v"(pf1)::"memory");
  } else if (b < 352) {
    if (b < 296) {
      unsigned long long c0, c1;
      asm volatile("s_getpc_b64 s[40:41]\n\ts_add_u32 s40, s40, k1_conv@rel32@lo+4\n\ts_addc_u32 s41, s41, k1_conv@rel32@hi+12\n\ts_mov_b64 %0, s[40:41]\n\t"
                   "s_getpc_b64 s[40:41]\n\ts_add_u32 s40, s40, .Lffn_code_end@rel32@lo+4\n\ts_addc_u32 s41, s41, .Lffn_code_end@rel32@hi+12\n\ts_mov_b64 %1, s[40:41]"
                   : "=s"(c0), "=s"(c1)::"s40", "s41", "scc");
      const u32 n = (u32)(c1 - c0);
      u32 acc = 0;
#pragma unroll 4
      for (u32 o = tid * 64; o < n; o += 256 * 64) acc ^= *(const GAS u32*)(c0 + o);
      asm volatile("" ::"v"(acc));
    }
    const int row = (b - 288) * 32 + w * 8 + (l >> 3);
    const u16* p = q + (size_t)row * D_ + (l & 7) * 8;
    u32x4 x[9];
#pragma unroll
    for (int i = 0; i < 9; ++i) x[i] = *(const u32x4*)(p + i * 64);
    float sum = 0.f;
#pragma unroll
    for (int i = 0; i < 9; ++i)
#pragma unroll
      for (int j = 0; j < 4; ++j) sum += bflo(x[i][j]) + bfhi(x[i][j]);
    sum += __shfl_xor(sum, 1); sum += __shfl_xor(sum, 2); sum += __shfl_xor(sum, 4);
    const float mu = sum * (1.f / D_);
    float sq = 0.f;
#pragma unroll
    for (int i = 0; i < 9; ++i)
#pragma unroll
      for (int j = 0; j < 4; ++j) { const float a = bflo(x[i][j]) - mu, c = bfhi(x[i][j]) - mu; sq += a * a + c * c; }
    sq += __shfl_xor(sq, 1); sq += __shfl_xor(sq, 2); sq += __shfl_xor(sq, 4);
    if ((l & 7) == 0) { f32x2 o = {mu, rsqrtf(sq * (1.f / D_) + 1e-5f)}; ((f32x2*)(ws + OFF_ST1))[row] = o; }
  } else {
    colsum_slice(wg, wu, (float*)(ws + OFF_CS), (b - 352) * 4 + w, l);
  }
  ST(1); ST_DRAIN(); ST(2);
  ST_FLUSH((u32*)(ws + (202u << 20)), b * 4 + w);
}

constexpr int K1_UPLANE = 2304, K1_UCH = 4608, K1_TOFF = 4 * K1_UCH, K1_TCOPY = 4160, K1_TCH = 4 * K1_TCOPY;
constexpr int K1_YOFF = K1_TOFF + 4 * K1_TCH, K1_YBUF = 8192;
constexpr int K1_LDS = K1_YOFF + 8 * K1_YBUF;
constexpr int K1_GRID = 256, K1_SIDE = K1_GRID - 144;

extern "C" __global__ __launch_bounds__(512) void k1_conv(const u16* __restrict__ ssm, const u16* __restrict__ ssmD, const u16* __restrict__ wg,
                                               const u16* __restrict__ wu, char* __restrict__ ws) {
  extern __shared__ __attribute__((aligned(128))) char lds[];
  ST_DECL; ST(0);
  asm volatile("" ::"s"(ssm), "s"(ssmD), "s"(wg), "s"(wu), "s"(ws));
  const u16* __restrict__ kvt = (const u16*)(ws + OFF_KVT);
  const u16* __restrict__ xp = (const u16*)(ws + OFF_XP);
  const f32x2* __restrict__ st1 = (const f32x2*)(ws + OFF_ST1);
  u16* __restrict__ hf = (u16*)(ws + OFF_HF);
  const int tid = threadIdx.x, l = tid & 63;
  const int w = __builtin_amdgcn_readfirstlane(tid >> 6);
  const int b = blockIdx.x;
  if (b >= 144) {
    const int sl = K0_SL + (b - 144) + K1_SIDE * w;
    if (sl < 576) colsum_slice(wg, wu, (float*)(ws + OFF_CS), sl, l);
    ST(6);
    ST_FLUSH((u32*)(ws + (203u << 20)), b * 8 + w);
    return;
  }
  const int g = b, ch = w & 3, hf_ = w >> 2;
  const int gch = g * 4 + ch;
  const ldsp LP = (ldsp)lds;
  const u32 L0 = (u32)(uintptr_t)LP;
  {
    const char* src = (const char*)(kvt + (size_t)gch * S_);
    const u32 vo = (2 * l + hf_) * 16;
    const u32 dst = L0 + ch * K1_UCH + hf_ * K1_UPLANE + 256;
    dma16(src, vo, dst);
    dma16(src + 2048, vo, dst + 1024);
  }
  const u16* krow = ssm + (size_t)gch * S_;
  u32x4 wv[2]; u32x2 wn[2];
#pragma unroll
  for (int it = 0; it < 2; ++it) {
    const int idx = hf_ * 128 + it * 64 + l;
    wv[it] = *(const u32x4*)(krow + (idx > 0 ? idx - 1 : 0) * 8);
    wn[it] = *(const u32x2*)(krow + idx * 8);
  }
  const u32x4 wl = *(const u32x4*)(krow + opq(255 * 8));
  const u32 dv1 = ssmD[opq(gch)];
  SCHED0();
  u32x2 xr[4]; f32x2 st[4];
#pragma unroll
  for (int i = 0; i < 4; ++i) { xr[i] = *(const u32x2*)(xp + ((size_t)g * S_ + tid + 512 * i) * 4); st[i] = st1[tid + 512 * i]; }
  SCHED0();
  const u32x4 z4 = {0u, 0u, 0u, 0u};
  if (l < 16) *(LAS u32x4*)(LP + (ch * K1_UCH + hf_ * K1_UPLANE + l * 16)) = z4;
  const ldsp tb = LP + (K1_TOFF + ch * K1_TCH);
  auto put = [&](int pos  , u32x4 a, u32x2 n) {
    const u32 o0 = __builtin_amdgcn_alignbit(a[1], a[0], 16), o1 = __builtin_amdgcn_alignbit(a[2], a[1], 16), o2 = __builtin_amdgcn_alignbit(a[3], a[2], 16),
              o3 = __builtin_amdgcn_alignbit(n[0], a[3], 16), o4 = __builtin_amdgcn_alignbit(n[1], n[0], 16);
    u32x4 c1 = {o0, o1, o2, o3}, c2 = {a[1], a[2], a[3], n[0]}, c3 = {o1, o2, o3, o4};
    *(LAS u32x4*)(tb + pos * 16) = a;
    *(LAS u32x4*)(tb + (K1_TCOPY + pos * 16)) = c1;
    *(LAS u32x4*)(tb + (2 * K1_TCOPY + pos * 16)) = c2;
    *(LAS u32x4*)(tb + (3 * K1_TCOPY + pos * 16)) = c3;
  };
  if (hf_ == 0 && l == 0) wv[0] = z4;
#pragma unroll
  for (int it = 0; it < 2; ++it) put(hf_ * 128 + it * 64 + l + 1, wv[it], wn[it]);
  if (hf_ == 1 && l < 2) { u32x2 z2 = {0u, 0u}; put(l ? 257 : 0, l ? wl : z4, z2); }
  ST(1);
  wait_vm<8>();
  LDS_BARRIER();
  ST(2);
  const int a_ = l & 15, q_ = l >> 4;
  const int cpy = (1 + a_) & 3;
  const ldsp taddr = LP + (K1_TOFF + ch * K1_TCH + cpy * K1_TCOPY + 2 * (1 + a_ + 8 * q_ - cpy) + hf_ * 256);
  const ldsp uaddr = LP + (ch * K1_UCH + ((q_ + 1) & 1) * K1_UPLANE + (16 + a_ - (q_ >> 1) - 8 * hf_ - 6) * 16);
  f32x4 acc[8];
  u32x4 dg;
  {
    const int j = 15 - a_ - 8 * q_;
    const u32 dval = hf_ ? 0u : dv1;
#pragma unroll
    for (int e = 0; e < 4; ++e) dg[e] = (j == 2 * e) ? dval : ((j == 2 * e + 1) ? (dval << 16) : 0u);
  }
  u32x4 U[2][8], T[8];
  auto ldU = [&](int e, int j) { return *(const LAS u32x4*)(uaddr + ((3 - e) * 32 + 256 * j)); };
  auto ldT = [&](int e, int m) {
    const u32x2 lo = *(volatile const LAS u32x2*)(taddr + (64 * e + 512 * m)), hi = *(volatile const LAS u32x2*)(taddr + (8 + 64 * e + 512 * m));
    u32x4 t = {lo[0], lo[1], hi[0], hi[1]};
    return t;
  };
#pragma unroll
  for (int j = 0; j < 8; ++j) U[0][j] = ldU(0, j);
  T[0] = ldT(0, 0); T[1] = ldT(0, 1);
  SCHED0();
  const f32x4 zero4 = {0.f, 0.f, 0.f, 0.f};
#pragma unroll
  for (int n = 0; n < 8; ++n) acc[n] = mfma16(dg, U[0][n], zero4);
  SCHED0();
  static_for<0, 32>([&](auto ic) {
    constexpr int e = ic.value >> 3, m = ic.value & 7;
    constexpr int nx = ic.value + 2;
    if constexpr (nx < 32) T[nx & 7] = ldT(nx >> 3, nx & 7);
    if constexpr (e < 3) U[(e + 1) & 1][m] = ldU(e + 1, m);
#pragma unroll
    for (int n = m; n < 8; ++n) acc[n] = mfma16(T[m], U[e & 1][n - m], acc[n]);
    SCHED0();
  });
  ST(3);
  {
    const ldsp yb = LP + (K1_YOFF + (hf_ * 4 + ch) * K1_YBUF + ((4 * a_ + q_) ^ ((a_ >> 1) & 3)) * 16);
#pragma unroll
    for (int n = 0; n < 8; ++n) *(LAS f32x4*)(yb + n * 1024) = acc[n];
  }
  LDS_BARRIER();
  ST(4);
  {
    const int sl = tid >> 2;
    const ldsp yb = LP + (K1_YOFF + (sl ^ ((sl >> 3) & 3)) * 16 + (tid & 3) * 4);
    float y[4][8];
#pragma unroll
    for (int i = 0; i < 4; ++i)
#pragma unroll
      for (int c = 0; c < 8; ++c) y[i][c] = *(const LAS float*)(yb + (i * 2048 + c * K1_YBUF));
    u16* hp = hf + ((((size_t)(tid >> 4) * 18 + (g >> 3)) * 64 + ((g >> 1) & 3) * 16 + (tid & 15)) * 8 + (g & 1) * 4);
#pragma unroll
    for (int i = 0; i < 4; ++i) {
      float hv[4];
#pragma unroll
      for (int c = 0; c < 4; ++c) {
        const float x = (c & 1) ? bfhi(xr[i][c >> 1]) : bflo(xr[i][c >> 1]);
        hv[c] = x + (x - st[i][0]) * st[i][1] * (y[i][c] + y[i][4 + c]);
      }
      u32x2 o = {pack2(hv[0], hv[1]), pack2(hv[2], hv[3])};
      *(u32x2*)(hp + (size_t)i * 32 * 18 * 512) = o;
    }
  }
  ST(5); ST_DRAIN(); ST(6);
  ST_FLUSH((u32*)(ws + (203u << 20)), b * 8 + w);
}

constexpr int G1_QE = 147456;
constexpr int G1_QL = 14 * 8192;
constexpr int G1_CS = G1_QE + 8192;
constexpr int G1_CP = G1_CS + 2048;
constexpr int FF_LDS = G1_CP + 5120;
constexpr int PF_ISS0 = 86;
constexpr int STG = 18432, NST = 8;
#define G1_DV 4
constexpr int G1_D = G1_DV, HD = 5, HB = HD + 1;
constexpr float GC = -2.3022081983f;
constexpr int U2 = 216;
constexpr int g1_q_issue(int half, int r) { return 8 + 6 * r; }
constexpr int g1_b_issue(int half, int j) { return (j < 14 ? 29 + 3 * j : 73 + 3 * (j - 14)); }
constexpr int G1_E0 = 8;
constexpr int epi_start(int i) { return i < 8 ? G1_E0 + 26 * i : U2 + 2; }
constexpr bool epi_stores(int i) { return i == 1 || i == 3 || i == 4 || i == 6 || i == 8; }
constexpr int w_issue(int n, int k) {
  return n < 4 ? 191 + 4 * n + 2 * k : n < 6 ? 209 + 4 * (n - 4) + 2 * k : n == 6 ? U2 + 7 + k : U2 + 9 * (n - 6) + 4 * k;
}
constexpr int h_issue(int n, int k) { return n < 5 ? 192 + 4 * n + 2 * k : U2 + 9 * (n - (n < 10 ? HD : 3)) + 2 + 3 * k; }
constexpr int dq_issue(int r) { return r == 0 ? 183 : r < 4 ? U2 + 9 * (4 * r - 3) + 7 : U2 + 9 * 12 + 7; }
constexpr int poll_iss(int p) { return p == 0 ? 181 : p == 1 ? U2 + 9 * 5 + 8 : U2 + 9 * 9 + 8; }
constexpr int poll_chk(int p) { return p == 0 ? 191 : p == 1 ? U2 + 9 * 7 + 1 : U2 + 9 * 11 + 1; }
constexpr int RES_ISS = U2 + 9 * 16 + 7;
constexpr int cnt_ops(int half, int k0, int k1) {
  int n = 0;
  auto in = [&](int key) { return key > k0 && key < k1 ? 1 : 0; };
  for (int r = 0; r < 4; ++r) n += in(4 * g1_q_issue(half, r) + 2);
  for (int j = 0; j < 18; ++j) n += in(4 * g1_b_issue(half, j) + 2);
  for (int i = 0; i < 9; ++i) if (epi_stores(i)) n += in(4 * (epi_start(i) + 18)) + in(4 * (epi_start(i) + 24));
  for (int s = 0; s < 18; ++s) for (int k = 0; k < 2; ++k) n += in(4 * w_issue(s, k) + 2) + in(4 * h_issue(s, k) + 2);
  for (int p = 0; p < 3; ++p) n += in(4 * poll_iss(p) + 2);
  n += 3 * in(4 * RES_ISS + 2);
  for (int r = 0; r < 5; ++r) n += in(4 * (PF_ISS0 + 4 * r) + 2);
  for (int r = 0; r < 5; ++r) n += in(4 * dq_issue(r) + 2);
  return n;
}
constexpr int wait_n(int k0a, int k0b, int k1) { return cmin(cnt_ops(0, k0a, k1), cnt_ops(1, k0b, k1)); }
constexpr int wait_q(int r, int tw) { return wait_n(4 * g1_q_issue(0, r) + 2, 4 * g1_q_issue(1, r) + 2, 4 * tw + 1); }
constexpr int wait_b(int j, int tw) { return wait_n(4 * g1_b_issue(0, j) + 2, 4 * g1_b_issue(1, j) + 2, 4 * tw + 1); }
constexpr int wait_k(int k0, int k1) { return wait_n(k0, k0, k1); }
constexpr int cmax(int a, int b) { return a > b ? a : b; }
static_assert(h_issue(1, 1) > epi_start(6) + 24 && h_issue(0, 0) > poll_chk(0) && h_issue(10, 0) > poll_chk(1) && h_issue(14, 0) > poll_chk(2), "schedule");
constexpr int kk_of(int s) { return s < 8 ? 9 * ((s >> 1) & 1) + 4 * (s & 1) + (s >> 2) : s < 10 ? 9 * (s - 8) + 8 : 9 * (((s - 10) >> 1) & 1) + 4 * (s & 1) + 2 + ((s - 10) >> 2); }
struct Acc { f32x4 a[2][2]; };

extern "C" __global__ __launch_bounds__(512, 2) void ffn_fused(const u16* __restrict__ wg, const u16* __restrict__ wu, const u16* __restrict__ wd,
                                                    char* __restrict__ ws, u16* __restrict__ out) {
  extern __shared__ __attribute__((aligned(128))) char lds[];
  ST_DECL; ST(0);
  asm volatile("" ::"s"(wg), "s"(wu), "s"(wd), "s"(ws), "s"(out));
  const int tid = threadIdx.x, l = tid & 63;
  const int w = __builtin_amdgcn_readfirstlane(tid >> 6);
  const int b = blockIdx.x;
  const int fg = (b & 3) * 8 + ((b >> 3) & 7), tg = ((b >> 2) & 1) * 4 + (b >> 6);
  const int tt = tg * 8 + w;
  const int rotg = fg < 6 ? 3 * fg : 0;
  auto rot = [&](int g) { const int x = g + rotg; return x >= 18 ? x - 18 : x; };
  const ldsp LP = (ldsp)lds;
  const u32 L0 = (u32)(uintptr_t)LP;
  constexpr size_t RS = DF_ * 2;
  const char* wmat = (const char*)((w & 1) ? wu : wg);
  const int wblk = w & 1, whr = (w >> 1) & 1, whalf = w >> 2;
  const u32 ab_voff = (u32)((16 * whalf + 8 * whr + (l >> 3)) * RS + ((((l & 7) >> 1) ^ (((l >> 4) & 1) | (whr << 1))) * 32) + (l & 1) * 16);
  const char* a_src = wmat + fg * 256;
  const u32 ab_dst = L0 + whalf * 4096 + wblk * 2048 + whr * 1024;
  const char* q_src = wmat + 8192 + fg * 32;
  auto mk_qvoff = [&](u32 l2) { return (u32)(((l2 >> 1) ^ ((l2 >> 2) & 4)) * RS + (l2 & 1) * 16); };
  u32x4 h[2][18];
  const char* hsrc = ws + OFF_HF + (size_t)tt * 36864;
  const u32 lo16 = l * 16;
  auto issue_dma = [&](auto ic) { constexpr int g = ic.value; dma16(a_src + (u32)rot(g) * (u32)(32 * RS), ab_voff, ab_dst + g * 8192); };
  auto issue_h = [&](auto ic, auto tc) { constexpr int g = ic.value, tl = tc.value; gload16<0>(h[tl][g], lo16, hsrc + rot(g) * 1024 + tl * 18432); };
  static_for<0, G1_D>([&](auto ic) { issue_dma(ic); issue_h(ic, IC<0>{}); issue_h(ic, IC<1>{}); });
  ST(1);
  const int rq = l >> 4, rr = (l >> 2) & 3, rp = l & 3;
  const u32 rb0 = L0 + (rq >> 1) * 4096 + (rq & 1) * 1024 + rr * 128 + rp * 8 + (((rr >> 1) | ((rq & 1) << 1)) * 32);
  auto abase = [&](int c, int off) { return opaque((ldsp)(uintptr_t)((opq(rb0) ^ (u32)(c * 32)) + (u32)off)); };
  Acc acc[4];
  const char* stb = ws + OFF_ST2 + (size_t)tt * 2048;
  u32x4 sg4; u32x2 sg2;
  {
    float s1[2] = {0.f, 0.f}, s2[2] = {0.f, 0.f};
    ldsp pb[3];
    u32x4 pf[2][3];
    auto fetch1 = [&](auto ic) {
      constexpr int s = ic.value;
      if constexpr ((s & 15) == 0) {
#pragma unroll
        for (int c = 0; c < 3; ++c) pb[c] = abase(c, s * 4096);
      }
      constexpr int o = ((s & 15) >> 1) * 8192 + (s & 1) * 2048;
#pragma unroll
      for (int c = 0; c < 3; ++c) pf[s & 1][c] = tr_frag(pb[c] + o, pb[c] + (o + 512));
    };
    wait_vm2<3 * (G1_D - 1)>(h[0][0], h[1][0]);
    BARRIER();
    fetch1(IC<0>{});
    static_for<0, 36>([&](auto ic) {
      constexpr int s = ic.value, g = s >> 1, mt = s & 1;
      constexpr int SQ = 2 * (18 - G1_D);
      auto mm = [&](int c) {
#pragma unroll
        for (int tl = 0; tl < 2; ++tl) mma<g == 0>(acc[c].a[mt][tl], pf[s & 1][c], h[tl][g]);
      };
      if constexpr (mt == 1 && s < 35) {
        constexpr int last = cmin(g + G1_D, 18);
        constexpr int extra = (g + G1_D < 18) ? 2 : 3;
        wait_vm2<3 * (last - (g + 2)) + extra>(h[0][g + 1], h[1][g + 1]);
        BARRIER();
      }
      if constexpr (s < 35) fetch1(IC<s + 1>{});
      SCHED0();
      mm(0);
      if constexpr (g + G1_D < 18) { if constexpr (mt == 0) issue_dma(IC<g + G1_D>{}); else issue_h(IC<g + G1_D>{}, IC<1>{}); }
      else if constexpr (s == SQ) {
        const int ii = min(tid, 287), qd = ii / 72, r = ii - qd * 72;
        if (w < 5) dma16(ws + OFF_CS, (u32)(qd * 2304 + (r < 64 ? fg * 64 + r : 2048 + fg * 8 + (r - 64))) * 16u, L0 + G1_CP + w * 1024);
        dma16(q_src + (u32)rot(w >> 1) * (u32)(32 * RS), mk_qvoff(l), L0 + G1_QE + w * 1024);
      }
      SCHED0();
      mm(1);
      if constexpr (g + G1_D < 18 && mt == 0) issue_h(IC<g + G1_D>{}, IC<0>{});
      if constexpr (s == SQ) { const u32 so = ((rq >> 1) * 16 + (l & 15)) * 64 + (rq & 1) * 24; ld_sc1_x4(sg4, so, stb); ld_sc1_x2<16>(sg2, so, stb); }
      if constexpr (s < 6) stat_acc(fg, h[s & 1][s >> 1], s1[s & 1], s2[s & 1]);
      SCHED0();
      mm(2);
      if constexpr (s == 6) {
        const float a0 = swap32_sum(swap16_sum(s1[0])), c0 = swap32_sum(swap16_sum(s2[0]));
        const float a1 = swap32_sum(swap16_sum(s1[1])), c1 = swap32_sum(swap16_sum(s2[1]));
        u32x2 gr = {__float_as_uint((l & 16) ? a1 : a0), __float_as_uint((l & 16) ? c1 : c0)};
        st_sc1_x2_if(fg, stb + fg * 8, (u32)((l & 31) * 64), gr);
      }
      SCHED0();
      if constexpr (s % 12 == 11) { ST(2 + s / 12); }
    });
  }
  STR(28);
  u32x4 wf[4];
  ldsp fb = nullptr, fb2 = nullptr;
  auto fetch = [&](auto ic) {
    constexpr int i = ic.value, p = i / 36, s = i % 36;
    if constexpr (p != 1) {
      constexpr int c = (p == 0) ? 3 : p - 2;
      constexpr int pos = (p == 0) ? (s + 28) % 36 : s;
      if constexpr (s == 0 || (pos & 15) == 0) fb = abase(c, (pos & ~15) * 4096);
      constexpr int o = ((pos & 15) >> 1) * 8192 + (pos & 1) * 2048;
      wf[i % 3] = tr_frag(fb + o, fb + (o + 512));
    } else {
      if constexpr (s == 0 || s == 8) {
        const u32 l2 = opq(tid);
        const int qo = (l2 >> 4) & 1;
        const int qb = (8 * ((l2 >> 4) & 3) + ((l2 >> 2) & 3)) * 32 + (l2 & 3) * 8 + (s == 0 ? G1_QE : G1_QL);
        fb = opaque(LP + (qb + qo * 128)); fb2 = opaque(LP + (qb + (1 - qo) * 128));
      }
      constexpr int o = (s < 8 ? s : s - 8) * 1024;
      wf[i % 3] = tr_frag(fb + o, fb2 + o);
    }
  };
  fetch(IC<0>{}); fetch(IC<1>{});
  SCHED0();
  wait0_x6(sg4, sg2);
#pragma unroll 1
  for (int it = 0; it < (1 << 16); ++it) {
    const bool bad = (sg4[0] & sg4[1]) == 0xffffffffu || (sg4[2] & sg4[3]) == 0xffffffffu || (sg2[0] & sg2[1]) == 0xffffffffu;
    if (!__any(bad)) break;
    __builtin_amdgcn_s_sleep(2);
    const u32 so = ((rq >> 1) * 16 + (l & 15)) * 64 + (rq & 1) * 24;
    ld_sc1_x4(sg4, so, stb); ld_sc1_x2<16>(sg2, so, stb);
    wait0_x6(sg4, sg2);
  }
  float nmu[2], k1[2], k2[2], kd[2];
  {
    const float t1 = swap16_sum(__uint_as_float(sg4[0]) + __uint_as_float(sg4[2]) + __uint_as_float(sg2[0]));
    const float t2 = swap16_sum(__uint_as_float(sg4[1]) + __uint_as_float(sg4[3]) + __uint_as_float(sg2[1]));
    const float mu = hide(t1 * (1.f / D_));
    const float var = fmaxf(t2 * (1.f / D_) - mu * mu, 0.f) + 1e-5f;
    const auto m2 = __builtin_amdgcn_permlane32_swap(__float_as_uint(-mu), __float_as_uint(-mu), false, false);
    const auto v2 = __builtin_amdgcn_permlane32_swap(__float_as_uint(var), __float_as_uint(var), false, false);
#pragma unroll
    for (int tl = 0; tl < 2; ++tl) {
      kd[tl] = __uint_as_float(v2[tl]);
      const float r = __builtin_amdgcn_rsqf(kd[tl]);
      nmu[tl] = __uint_as_float(m2[tl]);
      k1[tl] = hide(GC * r);
      k2[tl] = hide(k1[tl] * 0.044715f * r * r);
    }
  }
  char* hdst = ws + OFF_HH + ((size_t)tt * 288 + (fg >> 1) * 9 + (fg & 1) * 4) * 1024;
  const ldsp csb = opaque(LP + (G1_CS + rq * 32));
  f32x4 cc;
  auto ldcs = [&](int lc, int hfx) { cc = *(const LAS f32x4*)(csb + (lc * 128 + hfx * 16)); };
  u32x4 eo[2];
  u32x2 eq[2];
  float m0, m1, tu0, tu1, y0, y1;
  auto emit = [&](int lc, int i, u32 pk) {
    const int tl = i & 1, hx = i >> 1;
    if (lc == 8) {
      eq[tl][hx] = pk;
      if (hx) st_sc1_x2(hdst + (size_t)tl * 147456 + (8 - (fg & 1) * 4) * 1024 + (fg & 1) * 8, lo16, eq[tl]);
    } else {
      eo[tl][(lc & 1) * 2 + hx] = pk;
      if ((lc & 1) && hx) st_sc1_x4(hdst + (size_t)tl * 147456 + (lc >> 1) * 1024, lo16, eo[tl]);
    }
  };
  auto epi = [&](const Acc& a, int lc, int st, int nlc) {
    const int i = st / 6, sg = st % 6, tl = i & 1, e = (i >> 1) * 2;
    if (sg == 0) {
      if (i > 0) { u32 pk = pack2(tu0, tu1 * y1); pin(pk); emit(lc, i - 1, pk); }
      if (i < 4) { m0 = fmaf(nmu[tl], cc[0], a.a[0][tl][e]); m1 = fmaf(nmu[tl], cc[1], a.a[0][tl][e + 1]); pinf(m0); pinf(m1); }
    } else if (sg == 1) {
      tu0 = fmaf(nmu[tl], cc[2], a.a[1][tl][e]); tu1 = fmaf(nmu[tl], cc[3], a.a[1][tl][e + 1]);
      y0 = hide(m0 * m0); y1 = m1 * m1;
      pinf(tu0); pinf(tu1); pinf(y0); pinf(y1);
      if (i == 1) ldcs(lc, 1);
      if (i == 3 && nlc >= 0) ldcs(nlc, 0);
    } else if (sg == 2) {
      y0 = hide(fmaf(y0, k2[tl], k1[tl])); y1 = fmaf(y1, k2[tl], k1[tl]); tu0 = hide(tu0 * m0); tu1 = tu1 * m1;
      pinf(y0); pinf(y1); pinf(tu0); pinf(tu1);
    } else if (sg == 3) {
      y0 = hide(y0 * m0); y1 = y1 * m1; y0 = __builtin_amdgcn_exp2f(y0);
      pinf(y0); pinf(y1);
    } else if (sg == 4) {
      y1 = __builtin_amdgcn_exp2f(y1); y0 = __builtin_amdgcn_rcpf(fmaf(y0, kd[tl], kd[tl]));
      pinf(y0); pinf(y1);
    } else {
      y1 = __builtin_amdgcn_rcpf(fmaf(y1, kd[tl], kd[tl])); tu0 = tu0 * y0;
      pinf(y1); pinf(tu0);
    }
  };
  const int r5 = b >> 3;
  const int ks = b & 3, tg2 = ((b >> 2) & 1) * 8 + (r5 & 7), dt = r5 >> 3;
  const int tq = w & 3, kh = w >> 2;
  constexpr u32 RD = D_ * 2;
  const char* d_src = (const char*)wd + dt * 256;
  const char* dq_src = (const char*)wd + 1024 + dt * 32;
  const u32 d_dst = L0 + kh * 9216 + tq * 2048;
  const int mb = ks * 4 + kh * 2;
  auto row0 = [&](int kk) { const int q = kk / 9, i = kk - 9 * q; return i < 8 ? (mb + q) * 256 + i * 32 : 4096 + (mb + q) * 32; };
  u32x4 hb[HB][2];
  const char* hsrc0 = ws + OFF_HH + ((size_t)(tg2 * 8 + tq * 2 + kh) * 144 + mb * 9) * 1024;
  const char* hsrc1 = ws + OFF_HH + ((size_t)(tg2 * 8 + tq * 2 + (kh ^ 1)) * 144 + mb * 9) * 1024;
  const char* myflag = ws + OFF_FLG + 16384 + b * 64;
  const char* pflags = ws + OFF_FLG + 16384 + ((b & 7) + kh * 32 + ((r5 & 7) >> 1) * 64) * 64;
  u32 d_voff = 0, dq_voff = 0, p_voff = 0, pv = 0, q_voff = 0;
  f32x4 ac2[9][2];
  ldsp pb[5];
  u32 rb2 = 0, rbq = 0;
  auto fetch2 = [&](auto ic) {
    constexpr int i = ic.value, s = i / 9, c = i % 9, stg = s % NST;
    if constexpr (c == 0 && (stg % 3 == 0)) {
#pragma unroll
      for (int u = 0; u < 4; ++u) pb[u] = opaque((ldsp)(uintptr_t)((opq(rb2) ^ (u32)(u * 32)) + (u32)(stg * STG)));
      pb[4] = opaque((ldsp)(uintptr_t)(opq(rbq) + (u32)(stg * STG)));
    }
    constexpr int o = (stg % 3) * STG;
    if constexpr (c < 8) wf[i & 3] = tr_frag(pb[c & 3] + (o + (c >> 2) * 1024), pb[c & 3] + (o + (c >> 2) * 1024 + 4096));
    else if constexpr (s < 4) { const ldsp qe = (ldsp)(uintptr_t)(opq(rbq) + (u32)(G1_QE - 8192) - kh * (9216 - 1024)); wf[i & 3] = tr_frag(qe + s * 2048, qe + (s * 2048 + 512)); }
    else wf[i & 3] = tr_frag(pb[4] + o, pb[4] + (o + 512));
  };
  auto issue2 = [&](auto uc) {
    constexpr int u = uc.value;
    static_for<0, 18>([&](auto nc) {
      constexpr int n = nc.value, kk = kk_of(n);
      if constexpr (w_issue(n, 0) == u) dma16(d_src + (u32)row0(kk) * RD, d_voff, d_dst + (n % NST) * STG);
      if constexpr (w_issue(n, 1) == u) dma16(d_src + (u32)row0(kk) * RD + 128, d_voff, d_dst + (n % NST) * STG + 1024);
      if constexpr (h_issue(n, 0) == u) gload16_sc1<(kk & 3) * 1024>(hb[n % HB][0], lo16, hsrc0 + (kk >> 2) * 4096);
      if constexpr (h_issue(n, 1) == u) gload16_sc1<(kk & 3) * 1024>(hb[n % HB][1], lo16, hsrc1 + (kk >> 2) * 4096);
    });
    static_for<0, 5>([&](auto rc) {
      constexpr int r = rc.value;
      if constexpr (dq_issue(r) == u) {
        const int tsel = r == 4 ? (tq & 1) : tq;
        const int ra = row0(kk_of(4 * r)), rb = row0(kk_of(4 * r + 1)), rc_ = row0(kk_of(r == 4 ? 16 : 4 * r + 2)), rd = row0(kk_of(r == 4 ? 17 : 4 * r + 3));
        const int rsel = (tsel & 2) ? ((tsel & 1) ? rd : rc_) : ((tsel & 1) ? rb : ra);
        dma16(dq_src + (u32)rsel * RD, dq_voff, r == 0 ? L0 + G1_QE + (tq * 2 + kh) * 1024 : L0 + (((4 * r) % NST) + tsel) * STG + kh * 9216 + 8192);
      }
    });
    static_for<0, 3>([&](auto pc) {
      constexpr int p = pc.value;
      if constexpr (poll_iss(p) == u) poll_issue(pv, p_voff, pflags);
      if constexpr (poll_chk(p) == u) poll_check<wait_k(4 * poll_iss(p) + 2, 4 * u + 2), p + 1>(pv, p_voff, pflags);
    });
  };
  static_for<0, 216>([&](auto ic) {
    constexpr int t = ic.value, p = t / 36, s = t % 36;
    constexpr int slot = (p + 3) & 3;
    constexpr int hg = (p == 0) ? ((s >> 1) + 14) % 18 : (s >> 1);
    if constexpr (t + 2 < 216) fetch(IC<t + 2>{});
#pragma unroll
    for (int tl = 0; tl < 2; ++tl) mma<(s < 2)>(acc[slot].a[s & 1][tl], wf[t % 3], h[tl][hg]);
    if constexpr (t == 2) {
      const u32 pi = min(opq(tid), 71u);
      const ldsp cp = LP + (G1_CP + pi * 16);
      const f32x4 r = (*(const LAS f32x4*)cp + *(const LAS f32x4*)(cp + 1152)) + (*(const LAS f32x4*)(cp + 2304) + *(const LAS f32x4*)(cp + 3456));
      *(LAS f32x4*)(LP + (G1_CS + pi * 16)) = r;
    }
    if constexpr (t == G1_E0 - 1) ldcs(0, 0);
    if constexpr (t >= G1_E0) {
      constexpr int ci = (t - G1_E0) / 26, st = (t - G1_E0) % 26;
      if constexpr (ci < 8 && st <= 24) {
        constexpr int lc = ci < 4 ? ci : (ci == 4 ? 8 : ci - 1);
        constexpr int nlc = ci < 3 ? ci + 1 : (ci == 3 ? 8 : ci);
        epi(acc[ci & 3], lc, st, nlc);
      }
    }
    if constexpr (t == 1 || t == 28 || t == 72 || t == 190) { SCHED0(); BARRIER(); }
    if constexpr (t == 6) { SCHED0(); LDS_BARRIER(); }
    if constexpr (t == 7) { SCHED0(); BARRIER(); }
    if constexpr (t == 41) { wait_vm<wait_q(1, 41)>(); BARRIER(); }
    if constexpr (t == 57) { wait_vm<wait_q(3, 57)>(); BARRIER(); }
    if constexpr (t == 69) { wait_vm<wait_b(9, 69)>(); BARRIER(); }
    if constexpr (t == 89) { wait_vm<wait_b(14, 89)>(); BARRIER(); }
    if constexpr (t == 99) { wait_vm<wait_b(17, 99)>(); BARRIER(); }
    if constexpr (t == 162) { wait_vm<wait_k(4 * (epi_start(4) + 24), 4 * t + 1)>(); BARRIER(); flag_store(w, opq(0u), opq(1u), myflag); }
    if constexpr (t == 198) { SCHED0(); BARRIER(); }
    if constexpr (t == 208) { SCHED0(); wait_vm<wait_k(4 * w_issue(1, 1) + 2, 4 * t + 1)>(); BARRIER(); }
    if constexpr (t == 7) q_voff = mk_qvoff(opq(tid) & 63);
    if constexpr (t == 180) {
      const u32 l2 = opq(tid) & 63;
      d_voff = (u32)((8 * tq + (l2 >> 3)) * RD + ((((l2 & 7) >> 1) ^ ((l2 >> 4) & 3)) * 32) + (l2 & 1) * 16);
      dq_voff = (u32)((l2 >> 1) * RD + (l2 & 1) * 16);
      p_voff = (l2 & 3) * 512;
      pin(d_voff); pin(dq_voff); pin(p_voff);
    }
    static_for<0, 4>([&](auto rc) {
      constexpr int r = rc.value;
      if constexpr (g1_q_issue(0, r) == t) dma16(q_src + (u32)rot(4 + 4 * r + (w >> 1)) * (u32)(32 * RS), q_voff, L0 + G1_QL + r * 8192 + w * 1024);
    });
    static_for<0, 18>([&](auto jc) {
      constexpr int j = jc.value;
      if constexpr (g1_b_issue(0, j) == t) dma16(a_src + 128 + (u32)rot(j) * (u32)(32 * RS), ab_voff, ab_dst + j * 8192);
    });
    if constexpr (t >= PF_ISS0 && t < PF_ISS0 + 20 && (t - PF_ISS0) % 4 == 0) {
      const int piece = ((b >> 3) * 5 + (t - PF_ISS0) / 4) * 8 + w;
      const int byte = piece * 1024;
      const char* src = (const char*)wd + (byte < 1024 * 1152 ? (b & 3) * 1024 * 1152 + byte : 4096 * 1152 + (b & 3) * 128 * 1152 + (byte - 1024 * 1152));
      dma16(src, lo16, L0 + G1_QE + w * 1024);
    }
    if constexpr (t >= 180) issue2(ic);
    SCHED0();
    if constexpr (t % 24 == 23) { ST(5 + t / 24); }
  });
  STR(29);
  {
    const u32 l2 = opq(tid) & 63;
    const u32 q2 = l2 >> 4, r2_ = (l2 >> 2) & 3, p2 = l2 & 3;
    rb2 = L0 + kh * 9216 + (q2 >> 1) * 2048 + (4 * (q2 & 1) + r2_) * 128 + ((2 * (q2 & 1) + (r2_ >> 1)) * 32) + p2 * 8;
    rbq = L0 + kh * 9216 + 8192 + (4 * q2 + r2_) * 32 + p2 * 8;
  }
  const int ft = tg2 * 8 + ks * 2 + kh;
  const int dcol = dt * 128 + tq * 32 + 4 * rq;
  const int qcol = 512 + dt * 16 + 4 * rq;
  u32x2 r0, r1, r2;
  fetch2(IC<0>{}); fetch2(IC<1>{}); fetch2(IC<2>{});
  wait_vm2<wait_k(4 * h_issue(0, 1) + 2, 4 * U2 - 1)>(hb[0][0], hb[0][1]);
  static_for<0, 162>([&](auto ic) {
    constexpr int i = ic.value, s = i / 9, c = i % 9, u = U2 + i;
    if constexpr (c == 6 && s < 17) {
      constexpr int tgt = cmax(cmax(cmax(4 * h_issue(s + 1, 1) + 2, 4 * w_issue(s + 1, 1) + 2), 4 * dq_issue((s + 1) >> 2) + 2), s == 6 ? 4 * (epi_start(8) + 24) : 0);
      wait_vm2<wait_k(tgt, 4 * u - 1)>(hb[(s + 1) % HB][0], hb[(s + 1) % HB][1]);
      BARRIER();
      if constexpr (s == 0) flag_store(w, opq(0u), opq(2u), myflag);
      if constexpr (s == 6) flag_store(w, opq(0u), opq(3u), myflag);
    }
    if constexpr (i + 3 < 162) fetch2(IC<i + 3>{});
#pragma unroll
    for (int tl = 0; tl < 2; ++tl) mma<s == 0>(ac2[c][tl], wf[i & 3], hb[s % HB][tl]);
    if constexpr (i >= 2 && i <= 26) epi(acc[0], 7, i - 2, -1);
    issue2(IC<u>{});
    if constexpr (u == RES_ISS) {
      const char* hb_ = ws + OFF_HF + (size_t)ft * 18432;
      auto po = [&](int d) { return (u32)(((d >> 5) * 64 + ((d >> 3) & 3) * 16 + (l & 15)) * 16 + (d & 4) * 2); };
      gload8(r0, po(dcol), hb_); gload8(r1, po(dcol + 16), hb_); gload8(r2, po(qcol), hb_);
    }
    SCHED0();
    if constexpr (c == 8 && s % 3 == 2) { ST(14 + s / 3); }
  });
  MFMA_DRAIN(); SCHED0();
  {
    const ldsp xw = LP + (2 * STG + w * 9216 + l * 16);
#pragma unroll
    for (int c = 0; c < 9; ++c) *(LAS f32x4*)(xw + c * 1024) = ac2[c][1];
  }
  LDS_BARRIER();
  f32x4 sm[9];
  {
    const ldsp xr = LP + (2 * STG + (w ^ 4) * 9216 + l * 16);
#pragma unroll
    for (int c = 0; c < 9; ++c) sm[c] = ac2[c][0] + *(const LAS f32x4*)(xr + c * 1024);
  }
  ST(20);
  char* part = ws + OFF_PART;
  if (tq != ks) {
    char* pdst = part + ((((size_t)(tg2 * 4 + dt) * 4 + tq) * 4 + ks) * 2 + kh) * 4608;
#pragma unroll
    for (int pc = 0; pc < 4; ++pc) {
      u32x4 v = {pack2(sm[2 * pc][0], sm[2 * pc][1]), pack2(sm[2 * pc][2], sm[2 * pc][3]), pack2(sm[2 * pc + 1][0], sm[2 * pc + 1][1]), pack2(sm[2 * pc + 1][2], sm[2 * pc + 1][3])};
      st_sc1_x4(pdst + pc * 1024, lo16, v);
    }
    u32x2 v2 = {pack2(sm[8][0], sm[8][1]), pack2(sm[8][2], sm[8][3])};
    st_sc1_x2(pdst + 4096, l * 8, v2);
  } else {
    const ldsp ow = LP + (kh * 9216 + l * 16);
#pragma unroll
    for (int c = 0; c < 9; ++c) *(LAS f32x4*)(ow + c * 1024) = sm[c];
  }
  asm volatile("s_waitcnt vmcnt(0)" : "+v"(r0), "+v"(r1), "+v"(r2)::"memory");
  LDS_BARRIER();
  ST(21);
  const char* fl = ws + OFF_FLG + (tg2 * 4 + dt) * 256;
  flag_store(w, opq(0u), opq(1u), fl + ks * 64);
  const ldsp orp = LP + (kh * 9216 + l * 16);
  f32x4 o0 = *(const LAS f32x4*)(orp + tq * 2048), o1 = *(const LAS f32x4*)(orp + (tq * 2048 + 1024)), o2 = *(const LAS f32x4*)(orp + 8192);
  o0[0] += bflo(r0[0]); o0[1] += bfhi(r0[0]); o0[2] += bflo(r0[1]); o0[3] += bfhi(r0[1]);
  o1[0] += bflo(r1[0]); o1[1] += bfhi(r1[0]); o1[2] += bflo(r1[1]); o1[3] += bfhi(r1[1]);
  o2[0] += bflo(r2[0]); o2[1] += bfhi(r2[0]); o2[2] += bflo(r2[1]); o2[3] += bfhi(r2[1]);
  {
    const u32 fo = (l & 3) * 64;
    poll_issue(pv, fo, fl);
    poll_check<0, 1>(pv, fo, fl);
  }
  ST(22);
  {
    const char* psrc = part + (((size_t)(tg2 * 4 + dt) * 4 + ks) * 4 * 2 + kh) * 4608;
    u32x4 v[3]; u32x2 vq[3];
#pragma unroll
    for (int j = 0; j < 3; ++j) {
      const int src = (ks + 1 + j) & 3;
      ld_sc1_x4(v[j], lo16 + tq * 1024, psrc + src * 9216);
      ld_sc1_x2<0>(vq[j], l * 8 + 4096, psrc + src * 9216);
    }
    asm volatile("s_waitcnt vmcnt(0)" : "+v"(v[0]), "+v"(v[1]), "+v"(v[2]), "+v"(vq[0]), "+v"(vq[1]), "+v"(vq[2])::"memory");
#pragma unroll
    for (int j = 0; j < 3; ++j) {
      o0[0] += bflo(v[j][0]); o0[1] += bfhi(v[j][0]); o0[2] += bflo(v[j][1]); o0[3] += bfhi(v[j][1]);
      o1[0] += bflo(v[j][2]); o1[1] += bfhi(v[j][2]); o1[2] += bflo(v[j][3]); o1[3] += bfhi(v[j][3]);
      o2[0] += bflo(vq[j][0]); o2[1] += bfhi(vq[j][0]); o2[2] += bflo(vq[j][1]); o2[3] += bfhi(vq[j][1]);
    }
    u16* op = out + (size_t)(ft * 16 + (l & 15)) * D_;
    u32x2 oa = {pack2(o0[0], o0[1]), pack2(o0[2], o0[3])}, ob = {pack2(o1[0], o1[1]), pack2(o1[2], o1[3])}, oc = {pack2(o2[0], o2[1]), pack2(o2[2], o2[3])};
    *(u32x2*)(op + dcol) = oa; *(u32x2*)(op + dcol + 16) = ob;
    if (tq == 0) *(u32x2*)(op + qcol) = oc;
  }
  asm volatile(".Lffn_code_end:");
  ST(23); ST_DRAIN(); ST(24);
  ST_FLUSH((u32*)(ws + (200u << 20)), b * 8 + w);
}

extern "C" void kernel_launch(void* const* d_in, const int* in_sizes, int n_in, void* d_out, int out_size, void* d_ws, size_t ws_size,
                              hipStream_t stream) {
  const u16* q = (const u16*)d_in[0]; const u16* k = (const u16*)d_in[1]; const u16* v = (const u16*)d_in[2];
  const u16* shk = (const u16*)d_in[3]; const u16* ssm = (const u16*)d_in[4]; const u16* ssmD = (const u16*)d_in[5];
  const u16* wg = (const u16*)d_in[7]; const u16* wu = (const u16*)d_in[8]; const u16* wd = (const u16*)d_in[9];
  char* ws = (char*)d_ws;
  (void)hipFuncSetAttribute((const void*)k1_conv, hipFuncAttributeMaxDynamicSharedMemorySize, K1_LDS);
  (void)hipFuncSetAttribute((const void*)ffn_fused, hipFuncAttributeMaxDynamicSharedMemorySize, FF_LDS);
  k0_prep<<<K0_GRID, 256, 0, stream>>>(q, k, v, shk, ssm, wg, wu, ws);
  k1_conv<<<K1_GRID, 512, K1_LDS, stream>>>(ssm, ssmD, wg, wu, ws);
  ffn_fused<<<256, 512, FF_LDS, stream>>>(wg, wu, wd, ws, (u16*)d_out);
}
```

```cpp
#include <hip/hip_runtime.h>
#include <hip/hip_bf16.h>
#include <stdint.h>
#include <utility>
#define SIDE_DELAY 4

typedef unsigned short u16;
typedef unsigned int u32;
typedef __attribute__((ext_vector_type(2))) unsigned u32x2;
typedef __attribute__((ext_vector_type(4))) unsigned u32x4;
typedef __attribute__((ext_vector_type(2))) float f32x2;
typedef __attribute__((ext_vector_type(4))) float f32x4;
typedef __attribute__((ext_vector_type(8))) __bf16 bf16x8;
typedef __attribute__((ext_vector_type(2))) __bf16 bf16x2;
typedef __attribute__((ext_vector_type(4))) short s16x4;
#define GAS __attribute__((address_space(1)))
#define LAS __attribute__((address_space(3)))
#define DI __device__ __forceinline__
typedef LAS char* ldsp;

constexpr int S_ = 2048, D_ = 576, DF_ = 4608;
constexpr size_t OFF_KVT = 0;
constexpr size_t OFF_XP  = OFF_KVT + (size_t)D_ * S_ * 2;
constexpr size_t OFF_HF  = OFF_XP + (size_t)D_ * S_ * 2;
constexpr size_t OFF_ST1 = OFF_HF + (size_t)D_ * S_ * 2;
constexpr size_t OFF_CS  = OFF_ST1 + 2048 * 8;
constexpr size_t OFF_FLG = OFF_CS + 4 * 2 * 4608 * 4;
constexpr size_t OFF_ST2 = OFF_FLG + 36864;
constexpr size_t OFF_HH  = OFF_ST2 + 131072;
constexpr size_t OFF_PART = OFF_HH + (size_t)S_ * DF_ * 2;

DI float bflo(u32 w) { return __uint_as_float(w << 16); }
DI float bfhi(u32 w) { return __uint_as_float(w & 0xffff0000u); }
DI u32 pack2(float a, float b) { bf16x2 p; p[0] = (__bf16)a; p[1] = (__bf16)b; return __builtin_bit_cast(u32, p); }

template <int I> struct IC { static constexpr int value = I; };
template <int... I> struct Seq {};
template <int B, class F, int... I> DI void static_for_impl(F&& f, std::integer_sequence<int, I...>) { (f(IC<B + I>{}), ...); }
template <int B, int E, class F> DI void static_for(F&& f) { static_for_impl<B>(f, std::make_integer_sequence<int, E - B>{}); }
template <int N> DI void wait_vm() { asm volatile("s_waitcnt vmcnt(%0)" ::"n"(N) : "memory"); }
#define BARRIER() do { asm volatile("" ::: "memory"); __builtin_amdgcn_s_barrier(); asm volatile("" ::: "memory"); } while (0)
#define LDS_BARRIER() do { asm volatile("s_waitcnt lgkmcnt(0)" ::: "memory"); __builtin_amdgcn_s_barrier(); asm volatile("" ::: "memory"); } while (0)
#define SCHED0() __builtin_amdgcn_sched_barrier(0)
constexpr int cmin(int a, int b) { return a < b ? a : b; }

DI u32x4 tr_frag(ldsp a0, ldsp a1) {
  const s16x4 lo = __builtin_amdgcn_ds_read_tr16_b64_v4i16((LAS s16x4*)a0);
  const s16x4 hi = __builtin_amdgcn_ds_read_tr16_b64_v4i16((LAS s16x4*)a1);
  return __builtin_bit_cast(u32x4, __builtin_shufflevector(lo, hi, 0, 1, 2, 3, 4, 5, 6, 7));
}
DI f32x4 mfma16(const u32x4& a, const u32x4& b, const f32x4& c) {
  return __builtin_amdgcn_mfma_f32_16x16x32_bf16(__builtin_bit_cast(bf16x8, a), __builtin_bit_cast(bf16x8, b), c, 0, 0, 0);
}
template <bool FIRST> DI void mma(f32x4& c, const u32x4& a, const u32x4& b) {
  if constexpr (FIRST) asm volatile("v_mfma_f32_16x16x32_bf16 %0, %1, %2, 0" : "=&v"(c) : "v"(a), "v"(b));
  else asm volatile("v_mfma_f32_16x16x32_bf16 %0, %1, %2, %0" : "+v"(c) : "v"(a), "v"(b));
}
#define MFMA_DRAIN() asm volatile("s_nop 15" ::: "memory")
template <int OFF> DI void gload16_sc1(u32x4& dst, u32 voff, const char* sbase) {
  asm volatile("global_load_dwordx4 %0, %1, %2 offset:%3 sc1" : "=v"(dst) : "v"(voff), "s"(sbase), "n"(OFF) : "memory");
}
DI void poll_issue(u32& f, u32 voff, const char* sb) { asm volatile("global_load_dword %0, %1, %2 sc1" : "=v"(f) : "v"(voff), "s"(sb) : "memory"); }
template <int N, int PH> DI void poll_check(u32& f, u32 voff, const char* sb) {
  u32 cnt = 1u << 18;
  asm volatile("s_waitcnt vmcnt(%4)\n"
               "1:\n\tv_cmp_gt_u32_e32 vcc, %5, %0\n\ts_nop 4\n\ts_cbranch_vccz 2f\n\t"
               "s_sleep 1\n\tglobal_load_dword %0, %2, %3 sc1\n\ts_waitcnt vmcnt(0)\n\t"
               "s_sub_u32 %1, %1, 1\n\ts_cbranch_scc0 1b\n"
               "2:" : "+v"(f), "+s"(cnt) : "v"(voff), "s"(sb), "n"(N), "n"(PH) : "memory", "vcc", "scc");
}
DI void poll4(u32 voff, const char* sb) {
  u32 cnt = 1u << 18;
  u32x4 f; u32 m;
  asm volatile("1:\n\tglobal_load_dwordx4 %0, %3, %4 sc1\n\ts_waitcnt vmcnt(0)\n\t"
               "v_min3_u32 %1, v100, v101, v102\n\tv_min_u32_e32 %1, %1, v103\n\tv_cmp_eq_u32_e32 vcc, 0, %1\n\ts_nop 4\n\ts_cbranch_vccz 2f\n\t"
               "s_sleep 4\n\ts_sub_u32 %2, %2, 1\n\ts_cbranch_scc0 1b\n"
               "2:" : "=&{v[100:103]}"(f), "=&v"(m), "+s"(cnt) : "v"(voff), "s"(sb) : "memory", "vcc", "scc");
}
DI void swap_halves(u32x4& x, u32x4& y) {
  u32 a = x[2], b = x[3], c = y[0], d = y[1];
  asm volatile("v_swap_b32 %0, %2\n\tv_swap_b32 %1, %3" : "+v"(a), "+v"(b), "+v"(c), "+v"(d));
  x[2] = a; x[3] = b; y[0] = c; y[1] = d;
}
DI void flag_store(int w, u32 zero_off, u32 val, const char* sb) {
  asm volatile("s_cmp_lg_u32 %3, 0\n\ts_cbranch_scc1 1f\n\tglobal_store_dword %0, %1, %2 sc1\n1:" ::"v"(zero_off), "v"(val), "s"(sb), "s"(w) : "memory", "scc");
}
template <int OFF> DI void gload16(u32x4& dst, u32 voff, const char* sbase) {
  asm volatile("global_load_dwordx4 %0, %1, %2 offset:%3" : "=v"(dst) : "v"(voff), "s"(sbase), "n"(OFF) : "memory");
}
DI void gload8(u32x2& dst, u32 voff, const char* sbase) { asm volatile("global_load_dwordx2 %0, %1, %2" : "=v"(dst) : "v"(voff), "s"(sbase) : "memory"); }
template <int N, int VAL> DI void waitbar_if(int sel, u32x4& a, u32x4& b) {
  asm volatile("s_cmp_lg_u32 %2, %4\n\ts_cbranch_scc1 1f\n\ts_waitcnt vmcnt(%3)\n\ts_barrier\n1:" : "+v"(a), "+v"(b) : "s"(sel), "n"(N), "n"(VAL) : "memory", "scc");
}
template <int N> DI void wait_vm2(u32x4& a, u32x4& b) { asm volatile("s_waitcnt vmcnt(%2)" : "+v"(a), "+v"(b) : "n"(N) : "memory"); }
DI void dma16(const char* sbase, u32 voff, u32 lds_dst) {
  asm volatile("s_mov_b32 m0, %2\n\ts_nop 0\n\tglobal_load_lds_dwordx4 %0, %1" ::"v"(voff), "s"(sbase), "s"(lds_dst) : "memory");
}
template <int VAL> DI void dma16_if(int sel, const char* sbase, u32 voff, u32 lds_dst) {
  asm volatile("s_cmp_eq_u32 %3, %4\n\ts_mov_b32 m0, %2\n\ts_cselect_b64 exec, -1, 0\n\tglobal_load_lds_dwordx4 %0, %1\n\ts_mov_b64 exec, -1" ::"v"(voff), "s"(sbase), "s"(lds_dst), "s"(sel), "n"(VAL) : "memory", "scc");
}
DI ldsp opaque(ldsp p) { asm volatile("" : "+v"(p)); return p; }
DI u32 opq(u32 p) { asm volatile("" : "+v"(p)); return p; }
DI void pin(u32& p) { asm volatile("" : "+v"(p)); }
DI void pinf(float& p) { asm volatile("" : "+v"(p)); }
DI float hide(float p) { asm("" : "+v"(p)); return p; }
DI float swap16_sum(float x) { const auto r = __builtin_amdgcn_permlane16_swap(__float_as_uint(x), __float_as_uint(x), false, false); return __uint_as_float(r[0]) + __uint_as_float(r[1]); }
DI float swap32_sum(float x) { const auto r = __builtin_amdgcn_permlane32_swap(__float_as_uint(x), __float_as_uint(x), false, false); return __uint_as_float(r[0]) + __uint_as_float(r[1]); }
#define STAT1(R) "v_lshlrev_b32_e32 %0, 16, " R "\n\tv_and_b32_e32 %1, %8, " R "\n\tv_add_f32_e32 %2, %2, %0\n\tv_fmac_f32_e32 %3, %0, %0\n\tv_add_f32_e32 %2, %2, %1\n\tv_fmac_f32_e32 %3, %1, %1\n\t"
DI void stat_acc(int fg, const u32x4& v, float& s1, float& s2) {
  float x, y;
  asm volatile("s_cmp_gt_u32 %9, 5\n\ts_cbranch_scc1 1f\n\t" STAT1("%4") STAT1("%5") STAT1("%6") STAT1("%7") "\n1:"
               : "=&v"(x), "=&v"(y), "+v"(s1), "+v"(s2) : "v"(v[0]), "v"(v[1]), "v"(v[2]), "v"(v[3]), "s"(0xffff0000u), "s"(fg) : "scc");
}
DI void ld_sc1_x4(u32x4& d, u32 voff, const char* sb) { asm volatile("global_load_dwordx4 %0, %1, %2 sc1" : "=v"(d) : "v"(voff), "s"(sb) : "memory"); }
template <int OFF> DI void ld_sc1_x2(u32x2& d, u32 voff, const char* sb) { asm volatile("global_load_dwordx2 %0, %1, %2 offset:%3 sc1" : "=v"(d) : "v"(voff), "s"(sb), "n"(OFF) : "memory"); }
DI void wait0_x6(u32x4& a, u32x2& b) { asm volatile("s_waitcnt vmcnt(0)" : "+v"(a), "+v"(b)::"memory"); }
DI void st_sc1_x4(char* sb, u32 voff, const u32x4& v) { asm volatile("global_store_dwordx4 %0, %1, %2 sc1\n\ts_nop 1" ::"v"(voff), "v"(v), "s"(sb) : "memory"); }
DI void gstore16(char* sb, u32 voff, const u32x4& v) { asm volatile("global_store_dwordx4 %0, %1, %2\n\ts_nop 1" ::"v"(voff), "v"(v), "s"(sb) : "memory"); }
DI void gstore8(char* sb, u32 voff, const u32x2& v) { asm volatile("global_store_dwordx2 %0, %1, %2" ::"v"(voff), "v"(v), "s"(sb) : "memory"); }
DI void st_sc1_x2_if(int skip, const char* sb, u32 voff, const u32x2& v) {
  asm volatile("s_cmp_gt_u32 %3, 5\n\ts_cbranch_scc1 1f\n\tglobal_store_dwordx2 %0, %1, %2 sc1\n1:" ::"v"(voff), "v"(v), "s"(sb), "s"(skip) : "memory", "scc");
}
DI void st_sc1_x2(char* sb, u32 voff, const u32x2& v) { asm volatile("global_store_dwordx2 %0, %1, %2 sc1" ::"v"(voff), "v"(v), "s"(sb) : "memory"); }

#define STR(i)
#define ST(i)
#define ST_DECL
#define ST_FLUSH(dbg, wv)
#define ST_DRAIN()

DI void colsum_slice(const u16* __restrict__ wg, const u16* __restrict__ wu, float* __restrict__ cs, int sl, int l) {
  const int mat = sl & 1, qr = (sl >> 1) & 3, cb = sl >> 3;
  const u16* p = (mat ? wu : wg) + (size_t)(qr * 144 + (l >> 3)) * DF_ + cb * 64 + (l & 7) * 8;
  u32x4 x[18];
#pragma unroll
  for (int i = 0; i < 18; ++i) x[i] = *(const u32x4*)(p + (size_t)i * 8 * DF_);
  float a[8];
#pragma unroll
  for (int e = 0; e < 8; ++e) a[e] = 0.f;
#pragma unroll
  for (int i = 0; i < 18; ++i)
#pragma unroll
    for (int e = 0; e < 4; ++e) { a[2 * e] += bflo(x[i][e]); a[2 * e + 1] += bfhi(x[i][e]); }
#pragma unroll
  for (int e = 0; e < 8; ++e) { a[e] += __shfl_xor(a[e], 8); a[e] = swap32_sum(swap16_sum(a[e])); }
  if (l < 8) {
    float* d = cs + qr * 9216 + (cb * 32 + l * 4) * 4 + mat * 2;
#pragma unroll
    for (int e = 0; e < 4; ++e) { f32x2 o = {a[2 * e], a[2 * e + 1]}; *(f32x2*)(d + e * 4) = o; }
  }
}

constexpr int K0_GRID = 288 + 64;
extern "C" __global__ __launch_bounds__(256) void k0_prep(const u16* __restrict__ q, const u16* __restrict__ k, const u16* __restrict__ v,
                                               const u16* __restrict__ shk, const u16* __restrict__ ssm, char* __restrict__ ws) {
  ST_DECL; ST(0);
  asm volatile("" ::"s"(q), "s"(k), "s"(v), "s"(shk), "s"(ssm), "s"(ws));
  u16* __restrict__ kvt = (u16*)(ws + OFF_KVT);
  u16* __restrict__ xp = (u16*)(ws + OFF_XP);
  const int b = blockIdx.x, tid = threadIdx.x, w = tid >> 6, l = tid & 63;
  if (b < 288) {
    const int tb = b / 9, cb = b - tb * 9;
    const int s0 = tb * 64 + w * 16 + (l >> 4) * 4;
    const int c0 = cb * 64 + (l & 15) * 4;
    u32x2 kr[7], vr[4], qr[4];
#pragma unroll
    for (int i = 0; i < 7; ++i) {
      const int s = s0 - 3 + i;
      kr[i] = *(const u32x2*)(k + (size_t)(s < 0 ? 0 : s) * D_ + c0);
    }
#pragma unroll
    for (int i = 0; i < 4; ++i) vr[i] = *(const u32x2*)(v + (size_t)(s0 + i) * D_ + c0);
    const u32x4 w0 = *(const u32x4*)(shk + c0 * 4), w1 = *(const u32x4*)(shk + c0 * 4 + 8);
#pragma unroll
    for (int i = 0; i < 4; ++i) qr[i] = *(const u32x2*)(q + (size_t)(s0 + i) * D_ + c0);
    u32x4 pf0, pf1;
    gload16<0>(pf0, (u32)(b * 8192 + tid * 16), (const char*)ssm);
    gload16<0>(pf1, (u32)(b * 8192 + 4096 + tid * 16), (const char*)ssm);
    if (s0 == 0) { kr[0] = (u32x2){0u, 0u}; kr[1] = kr[0]; kr[2] = kr[0]; }
#pragma unroll
    for (int c = 0; c < 4; ++c) {
      const u32 wa = (c < 2) ? w0[2 * (c & 1)] : w1[2 * (c & 1)], wb = (c < 2) ? w0[2 * (c & 1) + 1] : w1[2 * (c & 1) + 1];
      const float f0 = bflo(wa), f1 = bfhi(wa), f2 = bflo(wb), f3 = bfhi(wb);
      float kk[7], kvv[4];
#pragma unroll
      for (int i = 0; i < 7; ++i) kk[i] = (c & 1) ? bfhi(kr[i][c >> 1]) : bflo(kr[i][c >> 1]);
#pragma unroll
      for (int i = 0; i < 4; ++i) {
        const float ks = kk[i + 3] * f0 + kk[i + 2] * f1 + kk[i + 1] * f2 + kk[i] * f3;
        const float vv = (c & 1) ? bfhi(vr[i][c >> 1]) : bflo(vr[i][c >> 1]);
        kvv[i] = ks * vv;
      }
      u32x2 o = {pack2(kvv[3], kvv[2]), pack2(kvv[1], kvv[0])};
      u16* dst = kvt + (size_t)(c0 + c) * S_ + (s0 >> 3) * 8 + ((s0 & 4) ? 0 : 4);
      *(u32x2*)dst = o;
    }
    u32x4 xa = {qr[0][0], qr[0][1], qr[1][0], qr[1][1]}, xb = {qr[2][0], qr[2][1], qr[3][0], qr[3][1]};
    u16* xdst = xp + ((size_t)(c0 >> 2) * S_ + s0) * 4;
    *(u32x4*)xdst = xa; *(u32x4*)(xdst + 8) = xb;
    if (b < 41) {
      const u32 f = b < 9 ? 0u : 0xffffffffu;
      u32x4 z = {f, f, f, f};
      ((u32x4*)(ws + OFF_FLG))[b * 256 + tid] = z;
    }
    asm volatile("s_waitcnt vmcnt(0)" : "+v"(pf0), "+v"(pf1)::"memory");
  } else {
    if (b < 296) {
      unsigned long long c0, c1;
      asm volatile("s_getpc_b64 s[40:41]\n\ts_add_u32 s40, s40, k1_conv@rel32@lo+4\n\ts_addc_u32 s41, s41, k1_conv@rel32@hi+12\n\ts_mov_b64 %0, s[40:41]\n\t"
                   "s_getpc_b64 s[40:41]\n\ts_add_u32 s40, s40, .Lffn_code_end@rel32@lo+4\n\ts_addc_u32 s41, s41, .Lffn_code_end@rel32@hi+12\n\ts_mov_b64 %1, s[40:41]"
                   : "=s"(c0), "=s"(c1)::"s40", "s41", "scc");
      const u32 n = (u32)(c1 - c0);
      u32 acc = 0;
#pragma unroll 4
      for (u32 o = tid * 64; o < n; o += 256 * 64) acc ^= *(const GAS u32*)(c0 + o);
      asm volatile("" ::"v"(acc));
    }
    const int row = (b - 288) * 32 + w * 8 + (l >> 3);
    const u16* p = q + (size_t)row * D_ + (l & 7) * 8;
    u32x4 x[9];
#pragma unroll
    for (int i = 0; i < 9; ++i) x[i] = *(const u32x4*)(p + i * 64);
    float sum = 0.f;
#pragma unroll
    for (int i = 0; i < 9; ++i)
#pragma unroll
      for (int j = 0; j < 4; ++j) sum += bflo(x[i][j]) + bfhi(x[i][j]);
    sum += __shfl_xor(sum, 1); sum += __shfl_xor(sum, 2); sum += __shfl_xor(sum, 4);
    const float mu = sum * (1.f / D_);
    float sq = 0.f;
#pragma unroll
    for (int i = 0; i < 9; ++i)
#pragma unroll
      for (int j = 0; j < 4; ++j) { const float a = bflo(x[i][j]) - mu, c = bfhi(x[i][j]) - mu; sq += a * a + c * c; }
    sq += __shfl_xor(sq, 1); sq += __shfl_xor(sq, 2); sq += __shfl_xor(sq, 4);
    if ((l & 7) == 0) { f32x2 o = {mu, rsqrtf(sq * (1.f / D_) + 1e-5f)}; ((f32x2*)(ws + OFF_ST1))[row] = o; }
  }
  ST(1); ST_DRAIN(); ST(2);
  ST_FLUSH((u32*)(ws + (202u << 20)), b * 4 + w);
}

constexpr int K1_UPLANE = 2304, K1_UCH = 4608, K1_TOFF = 4 * K1_UCH, K1_TCOPY = 4160, K1_TCH = 4 * K1_TCOPY;
constexpr int K1_YOFF = K1_TOFF + 4 * K1_TCH, K1_YBUF = 8192;
constexpr int K1_LDS = K1_YOFF + 8 * K1_YBUF;
constexpr int K1_GRID = 256;

#define HSTORE gstore16
extern "C" __global__ __launch_bounds__(512) void k1_conv(const u16* __restrict__ ssm, const u16* __restrict__ ssmD, const u16* __restrict__ wg,
                                                          const u16* __restrict__ wu, char* __restrict__ ws) {
  extern __shared__ __attribute__((aligned(128))) char lds[];
  ST_DECL; ST(0);
  asm volatile("" ::"s"(ssm), "s"(ssmD), "s"(wg), "s"(wu), "s"(ws));
  const int tid = threadIdx.x, l = tid & 63;
  const int w = __builtin_amdgcn_readfirstlane(tid >> 6);
  const int b = blockIdx.x;
  const ldsp LP = (ldsp)lds;
  const u32 L0 = (u32)(uintptr_t)LP;
  if (b >= 144) {
    for (int i = 0; i < SIDE_DELAY; ++i) __builtin_amdgcn_s_sleep(8);
    const int sl = (b - 144) + 112 * w;
    if (sl < 576) colsum_slice(wg, wu, (float*)(ws + OFF_CS), sl, l);
  } else {
    const u16* __restrict__ kvt = (const u16*)(ws + OFF_KVT);
    const u16* __restrict__ xp = (const u16*)(ws + OFF_XP);
    const f32x2* __restrict__ st1 = (const f32x2*)(ws + OFF_ST1);
    const int g = b, ch = w & 3, hf_ = w >> 2;
    const int gch = g * 4 + ch;
    {
      const char* src = (const char*)(kvt + (size_t)gch * S_);
      const u32 vo = (2 * l + hf_) * 16;
      const u32 dst = L0 + ch * K1_UCH + hf_ * K1_UPLANE + 256;
      dma16(src, vo, dst);
      dma16(src + 2048, vo, dst + 1024);
    }
    const u16* krow = ssm + (size_t)gch * S_;
    u32x4 wv[2]; u32x2 wn[2];
  #pragma unroll
    for (int it = 0; it < 2; ++it) {
      const int idx = hf_ * 128 + it * 64 + l;
      wv[it] = *(const u32x4*)(krow + (idx > 0 ? idx - 1 : 0) * 8);
      wn[it] = *(const u32x2*)(krow + idx * 8);
    }
    const u32x4 wl = *(const u32x4*)(krow + opq(255 * 8));
    const u32 dv1 = ssmD[opq(gch)];
    SCHED0();
    u32x2 xr[4]; f32x2 st[4];
  #pragma unroll
    for (int i = 0; i < 4; ++i) { const int s = (tid >> 4) * 32 + (tid & 15) + (i & 1) * 16 + (i >> 1) * 1024; xr[i] = *(const u32x2*)(xp + ((size_t)g * S_ + s) * 4); st[i] = st1[s]; }
    SCHED0();
    const u32x4 z4 = {0u, 0u, 0u, 0u};
    if (l < 16) *(LAS u32x4*)(LP + (ch * K1_UCH + hf_ * K1_UPLANE + l * 16)) = z4;
    const ldsp tb = LP + (K1_TOFF + ch * K1_TCH);
    auto put = [&](int pos  , u32x4 a, u32x2 n) {
      const u32 o0 = __builtin_amdgcn_alignbit(a[1], a[0], 16), o1 = __builtin_amdgcn_alignbit(a[2], a[1], 16), o2 = __builtin_amdgcn_alignbit(a[3], a[2], 16),
                o3 = __builtin_amdgcn_alignbit(n[0], a[3], 16), o4 = __builtin_amdgcn_alignbit(n[1], n[0], 16);
      u32x4 c1 = {o0, o1, o2, o3}, c2 = {a[1], a[2], a[3], n[0]}, c3 = {o1, o2, o3, o4};
      *(LAS u32x4*)(tb + pos * 16) = a;
      *(LAS u32x4*)(tb + (K1_TCOPY + pos * 16)) = c1;
      *(LAS u32x4*)(tb + (2 * K1_TCOPY + pos * 16)) = c2;
      *(LAS u32x4*)(tb + (3 * K1_TCOPY + pos * 16)) = c3;
    };
    if (hf_ == 0 && l == 0) wv[0] = z4;
  #pragma unroll
    for (int it = 0; it < 2; ++it) put(hf_ * 128 + it * 64 + l + 1, wv[it], wn[it]);
    if (hf_ == 1 && l < 2) { u32x2 z2 = {0u, 0u}; put(l ? 257 : 0, l ? wl : z4, z2); }
    ST(1);
    wait_vm<8>();
    LDS_BARRIER();
    ST(2);
    const int a_ = l & 15, q_ = l >> 4;
    const int cpy = (1 + a_) & 3;
    const ldsp taddr = LP + (K1_TOFF + ch * K1_TCH + cpy * K1_TCOPY + 2 * (1 + a_ + 8 * q_ - cpy) + hf_ * 256);
    const ldsp uaddr = LP + (ch * K1_UCH + ((q_ + 1) & 1) * K1_UPLANE + (16 + a_ - (q_ >> 1) - 8 * hf_ - 6) * 16);
    f32x4 acc[8];
    u32x4 dg;
    {
      const int j = 15 - a_ - 8 * q_;
      const u32 dval = hf_ ? 0u : dv1;
  #pragma unroll
      for (int e = 0; e < 4; ++e) dg[e] = (j == 2 * e) ? dval : ((j == 2 * e + 1) ? (dval << 16) : 0u);
    }
    u32x4 U[2][8], T[2][8];
    auto ldU = [&](int e, int j) { return *(const LAS u32x4*)(uaddr + ((3 - e) * 32 + 256 * j)); };
    auto ldT = [&](int e, int m) {
      const u32x2 lo = *(volatile const LAS u32x2*)(taddr + (64 * e + 512 * m)), hi = *(volatile const LAS u32x2*)(taddr + (8 + 64 * e + 512 * m));
      u32x4 t = {lo[0], lo[1], hi[0], hi[1]};
      return t;
    };
#pragma unroll
    for (int j = 0; j < 8; ++j) U[0][j] = ldU(0, j);
#pragma unroll
    for (int j = 0; j < 8; ++j) T[0][j] = ldT(0, j);
    asm volatile("s_nop 1" : "+v"(dg));
    SCHED0();
#pragma unroll
    for (int n = 0; n < 8; ++n) mma<true>(acc[n], dg, U[0][n]);
    SCHED0();
    float xs[16], xn[16];
    static_for<0, 144>([&](auto ic) {
      constexpr int e = ic.value / 36, i = ic.value % 36;
      constexpr auto mn = [] { int k = 0; for (int m = 0; m < 8; ++m) for (int n = m; n < 8; ++n) { if (k == i) return m * 8 + n; ++k; } return 0; }();
      constexpr int m = mn >> 3, n = mn & 7;
      if constexpr (e < 3 && i < 32 && (i & 1) == 0) {
        if constexpr ((i & 2) == 0) U[(e + 1) & 1][i >> 2] = ldU(e + 1, i >> 2); else T[(e + 1) & 1][i >> 2] = ldT(e + 1, i >> 2);
      }
      mma<false>(acc[n], T[e & 1][m], U[e & 1][n - m]);
      if constexpr (e == 3 && i < 16) {
        constexpr int ti = i >> 2, c = i & 3;
        xs[i] = (c & 1) ? bfhi(xr[ti][c >> 1]) : bflo(xr[ti][c >> 1]);
        xn[i] = hide(xs[i] - st[ti][0]) * st[ti][1];
        pinf(xs[i]); pinf(xn[i]);
      }
      SCHED0();
    });
    MFMA_DRAIN(); SCHED0();
    ST(3);
    {
      const ldsp yb = LP + (K1_YOFF + (hf_ * 4 + ch) * K1_YBUF + ((4 * a_ + q_) ^ ((a_ >> 1) & 3) ^ ((a_ & 2) << 1)) * 16);
  #pragma unroll
      for (int n = 0; n < 8; ++n) *(LAS f32x4*)(yb + n * 1024) = acc[n];
    }
    LDS_BARRIER();
    ST(4);
    {
      float y[4][8];
#pragma unroll
      for (int i = 0; i < 4; ++i) {
        const int s = (tid >> 4) * 32 + (tid & 15) + (i & 1) * 16, sl = s >> 2;
        const ldsp yb = LP + (K1_YOFF + (sl ^ ((sl >> 3) & 3) ^ ((sl >> 1) & 4)) * 16 + (s & 3) * 4 + (i >> 1) * 4096);
#pragma unroll
        for (int c = 0; c < 8; ++c) y[i][c] = *(const LAS float*)(yb + c * K1_YBUF);
      }
#pragma unroll
      for (int j = 0; j < 2; ++j) {
        u32x4 o;
#pragma unroll
        for (int e = 0; e < 2; ++e) {
          const int i = 2 * j + e;
          float hv[4];
#pragma unroll
          for (int c = 0; c < 4; ++c) hv[c] = fmaf(xn[4 * i + c], hide(y[i][c] + y[i][4 + c]), xs[4 * i + c]);
          o[2 * e] = pack2(hv[0], hv[1]); o[2 * e + 1] = pack2(hv[2], hv[3]);
        }
        HSTORE(ws + OFF_HF + (size_t)j * 32 * 36864 + (g >> 3) * 2048 + (g & 1) * 1024 + ((g >> 1) & 3) * 256, (u32)((tid >> 4) * 36864 + (tid & 15) * 16), o);
      }
    }
    ST(5);
  }
  ST_DRAIN(); ST(6);
  ST_FLUSH((u32*)(ws + (203u << 20)), b * 8 + w);
}

constexpr int G1_QE = 147456;
constexpr int G1_QL = 14 * 8192;
constexpr int G1_CS = G1_QE + 8192;
constexpr int G1_CP = G1_CS + 2048;
constexpr int FF_LDS = G1_CP + 5120;
constexpr int PF_ISS0 = 86;
constexpr int STG = 18432, NST = 8;
#define G1_DV 4
constexpr int G1_D = G1_DV, G1_DW = G1_DV, HD = 5, HB = HD + 1;
constexpr int p1_ops(int x, bool even_only) { return (x + G1_DW < 18 ? 1 : 0) + (x + G1_D < 18 ? (even_only ? 1 : 2) : 0) + (x == 18 - G1_D ? 3 : 0); }
constexpr int p1_wait(int g) {
  int n = 0;
  if (g + 1 < G1_D) n += 3 * (G1_D - (g + 2));
  for (int x = (g + 2 - G1_D > 0 ? g + 2 - G1_D : 0); x < g; ++x) n += p1_ops(x, false);
  return n + p1_ops(g, true);
}
constexpr float GC = -2.3022081983f;
constexpr int U2 = 216;
constexpr int g1_q_issue(int half, int r) { return 8 + 6 * r; }
constexpr int g1_b_issue(int half, int j) { return (j < 14 ? 29 + 3 * j : 73 + 3 * (j - 14)); }
constexpr int G1_E0 = 9;
constexpr int epi_start(int i) { return i < 8 ? G1_E0 + 26 * i : U2 + 2; }
static_assert(G1_E0 + 26 * 7 + 24 < 216, "epilogue stream");
constexpr bool epi_stores(int i) { return i == 1 || i == 3 || i == 4 || i == 6 || i == 8; }
constexpr int w_issue(int n, int k) {
  return n < 4 ? 191 + 4 * n + 2 * k : n < 6 ? 209 + 4 * (n - 4) + 2 * k : n == 6 ? U2 + 7 + k : U2 + 9 * (n - 6) + 4 * k;
}
constexpr int h_issue(int n, int k) { return n < 5 ? 192 + 4 * n + 2 * k : U2 + 9 * (n - (n < 10 ? HD : 3)) + 2 + 3 * k; }
constexpr int dq_issue(int r) { return r == 0 ? 183 : r < 4 ? U2 + 9 * (4 * r - 3) + 7 : U2 + 9 * 12 + 7; }
constexpr int poll_iss(int p) { return p == 0 ? 181 : p == 1 ? U2 + 9 * 5 + 8 : U2 + 9 * 9 + 8; }
constexpr int poll_chk(int p) { return p == 0 ? 191 : p == 1 ? U2 + 9 * 7 + 1 : U2 + 9 * 11 + 1; }
constexpr int RES_ISS = U2 + 9 * 16 + 7;
template <class F> constexpr int find_at(int n, int u, F f) { for (int i = 0; i < n; ++i) if (f(i) == u) return i; return -1; }
constexpr int cnt_ops(int half, int k0, int k1) {
  int n = 0;
  auto in = [&](int key) { return key > k0 && key < k1 ? 1 : 0; };
  for (int r = 0; r < 4; ++r) n += in(4 * g1_q_issue(half, r) + 2);
  for (int j = 0; j < 18; ++j) n += in(4 * g1_b_issue(half, j) + 2);
  for (int i = 0; i < 9; ++i) if (epi_stores(i)) n += in(4 * (epi_start(i) + 18)) + in(4 * (epi_start(i) + 24));
  for (int s = 0; s < 18; ++s) for (int k = 0; k < 2; ++k) n += in(4 * w_issue(s, k) + 2) + in(4 * h_issue(s, k) + 2);
  for (int p = 0; p < 3; ++p) n += in(4 * poll_iss(p) + 2);
  n += 3 * in(4 * RES_ISS + 2);
  for (int r = 0; r < 5; ++r) n += in(4 * (PF_ISS0 + 4 * r) + 2);
  for (int r = 0; r < 5; ++r) n += in(4 * dq_issue(r) + 2);
  return n;
}
constexpr int wait_n(int k0a, int k0b, int k1) { return cmin(cnt_ops(0, k0a, k1), cnt_ops(1, k0b, k1)); }
constexpr int wait_q(int r, int tw) { return wait_n(4 * g1_q_issue(0, r) + 2, 4 * g1_q_issue(1, r) + 2, 4 * tw + 1); }
constexpr int wait_b(int j, int tw) { return wait_n(4 * g1_b_issue(0, j) + 2, 4 * g1_b_issue(1, j) + 2, 4 * tw + 1); }
constexpr int wait_k(int k0, int k1) { return wait_n(k0, k0, k1); }
constexpr int cmax(int a, int b) { return a > b ? a : b; }
static_assert(h_issue(1, 1) > epi_start(6) + 24 && h_issue(0, 0) > poll_chk(0) && h_issue(10, 0) > poll_chk(1) && h_issue(14, 0) > poll_chk(2), "schedule");
constexpr int kk_of(int s) { return s < 8 ? 9 * ((s >> 1) & 1) + 4 * (s & 1) + (s >> 2) : s < 10 ? 9 * (s - 8) + 8 : 9 * (((s - 10) >> 1) & 1) + 4 * (s & 1) + 2 + ((s - 10) >> 2); }
struct Acc { f32x4 a[2][2]; };

extern "C" __global__ __launch_bounds__(512, 2) void ffn_fused(const u16* __restrict__ wg, const u16* __restrict__ wu, const u16* __restrict__ wd,
                                                               char* __restrict__ ws, u16* __restrict__ out) {
  extern __shared__ __attribute__((aligned(128))) char lds[];
  ST_DECL; ST(0);
  asm volatile("" ::"s"(wg), "s"(wu), "s"(wd), "s"(ws), "s"(out));
  const int tid = threadIdx.x, l = tid & 63;
  const int w = __builtin_amdgcn_readfirstlane(tid >> 6);
  const int b = blockIdx.x;
  const int fg = (b & 3) * 8 + ((b >> 3) & 7), tg = ((b >> 2) & 1) * 4 + (b >> 6);
  const int tt = tg * 8 + w;
  const int rotg = fg < 6 ? 3 * fg : 0;
  auto rot = [&](int g) { const int x = g + rotg; return x >= 18 ? x - 18 : x; };
  const ldsp LP = (ldsp)lds;
  const u32 L0 = (u32)(uintptr_t)LP;
  constexpr size_t RS = DF_ * 2;
  const char* wmat = (const char*)((w & 1) ? wu : wg);
  const int wblk = w & 1, whr = (w >> 1) & 1, whalf = w >> 2;
  const u32 ab_voff = (u32)((16 * whalf + 8 * whr + (l >> 3)) * RS + ((((l & 7) >> 1) ^ (((l >> 4) & 1) | (whr << 1))) * 32) + (l & 1) * 16);
  const char* a_src = wmat + fg * 256;
  const u32 ab_dst = L0 + whalf * 4096 + wblk * 2048 + whr * 1024;
  const char* q_src = wmat + 8192 + fg * 32;
  auto mk_qvoff = [&](u32 l2) { return (u32)(((l2 >> 1) ^ ((l2 >> 2) & 4)) * RS + (l2 & 1) * 16); };
  auto issue_dma = [&](auto ic) { constexpr int g = ic.value; dma16(a_src + (u32)rot(g) * (u32)(32 * RS), ab_voff, ab_dst + g * 8192); };
  u32x4 h[2][18];
  const char* hsrc = ws + OFF_HF + (size_t)tt * 36864;
  const u32 lo16 = l * 16;
  auto issue_h = [&](auto ic, auto tc) { constexpr int g = ic.value, tl = tc.value; gload16<tl * 1024>(h[tl][g], lo16, hsrc + rot(g) * 2048); };
  static_for<0, G1_D>([&](auto ic) { issue_dma(ic); issue_h(ic, IC<0>{}); issue_h(ic, IC<1>{}); });
  ST(1);
  const int rq = l >> 4, rr = (l >> 2) & 3, rp = l & 3;
  const u32 rb0 = L0 + (rq >> 1) * 4096 + (rq & 1) * 1024 + rr * 128 + rp * 8 + (((rr >> 1) | ((rq & 1) << 1)) * 32);
  auto abase = [&](int c, int off) { return opaque((ldsp)(uintptr_t)((opq(rb0) ^ (u32)(c * 32)) + (u32)off)); };
  Acc acc[4];
  const char* stb = ws + OFF_ST2;
  u32x4 sg4; u32x2 sg2;
  {
    float s1[2] = {0.f, 0.f}, s2[2] = {0.f, 0.f};
    ldsp pb[3];
    u32x4 pf[2][3];
    auto fetch1 = [&](auto ic) {
      constexpr int s = ic.value;
      if constexpr ((s & 15) == 0) {
#pragma unroll
        for (int c = 0; c < 3; ++c) pb[c] = abase(c, s * 4096);
      }
      constexpr int o = ((s & 15) >> 1) * 8192 + (s & 1) * 2048;
#pragma unroll
      for (int c = 0; c < 3; ++c) pf[s & 1][c] = tr_frag(pb[c] + o, pb[c] + (o + 512));
    };
    wait_vm2<3 * (G1_D - 1)>(h[0][0], h[1][0]);
    BARRIER();
    swap_halves(h[0][0], h[1][0]);
    fetch1(IC<0>{});
    static_for<0, 36>([&](auto ic) {
      constexpr int s = ic.value, g = s >> 1, mt = s & 1;
      constexpr int SQ = 2 * (18 - G1_D);
      auto mm = [&](int c) {
#pragma unroll
        for (int tl = 0; tl < 2; ++tl) mma<g == 0>(acc[c].a[mt][tl], pf[s & 1][c], h[tl][g]);
      };
      if constexpr (mt == 1 && s < 35) {
        wait_vm2<p1_wait(g)>(h[0][g + 1], h[1][g + 1]);
        BARRIER();
      }
      if constexpr (s < 35) fetch1(IC<s + 1>{});
      SCHED0();
      mm(0);
      if constexpr (mt == 0 && g + G1_DW < 18) issue_dma(IC<g + G1_DW>{});
      if constexpr (mt == 1 && g + G1_D < 18) issue_h(IC<g + G1_D>{}, IC<1>{});
      if constexpr (s == SQ) {
        const int ii = min(tid, 287), qd = ii / 72, r = ii - qd * 72;
        if (w < 5) dma16(ws + OFF_CS, (u32)(qd * 2304 + (r < 64 ? fg * 64 + r : 2048 + fg * 8 + (r - 64))) * 16u, L0 + G1_CP + w * 1024);
        dma16(q_src + (u32)rot(w >> 1) * (u32)(32 * RS), mk_qvoff(l), L0 + G1_QE + w * 1024);
      }
      SCHED0();
      mm(1);
      if constexpr (g + G1_D < 18 && mt == 0) issue_h(IC<g + G1_D>{}, IC<0>{});
      if constexpr (s == SQ) { const u32 so = tt * 2048 + ((rq >> 1) * 16 + (l & 15)) * 64 + (rq & 1) * 24; ld_sc1_x4(sg4, so, stb); ld_sc1_x2<16>(sg2, so, stb); }
      if constexpr (s < 6) stat_acc(fg, h[s & 1][s >> 1], s1[s & 1], s2[s & 1]);
      SCHED0();
      mm(2);
      if constexpr (mt == 1 && s < 35) swap_halves(h[0][g + 1], h[1][g + 1]);
      if constexpr (s == 6) {
        const float a0 = swap32_sum(swap16_sum(s1[0])), c0 = swap32_sum(swap16_sum(s2[0]));
        const float a1 = swap32_sum(swap16_sum(s1[1])), c1 = swap32_sum(swap16_sum(s2[1]));
        u32x2 gr = {__float_as_uint((l & 16) ? a1 : a0), __float_as_uint((l & 16) ? c1 : c0)};
        st_sc1_x2_if(fg, stb, (u32)(tt * 2048 + fg * 8 + (l & 31) * 64), gr);
      }
      SCHED0();
      if constexpr (s % 12 == 11) { ST(2 + s / 12); }
    });
  }
  STR(28);
  u32x4 wf[4];
  ldsp fb = nullptr, fb2 = nullptr;
  auto fetch = [&](auto ic) {
    constexpr int i = ic.value, p = i / 36, s = i % 36;
    if constexpr (p != 1) {
      constexpr int c = (p == 0) ? 3 : p - 2;
      constexpr int pos = (p == 0) ? (s + 28) % 36 : s;
      if constexpr (s == 0 || (pos & 15) == 0) fb = abase(c, (pos & ~15) * 4096);
      constexpr int o = ((pos & 15) >> 1) * 8192 + (pos & 1) * 2048;
      wf[i % 3] = tr_frag(fb + o, fb + (o + 512));
    } else {
      if constexpr (s == 0 || s == 8) {
        const u32 l2 = opq(tid);
        const int qo = (l2 >> 4) & 1;
        const int qb = (8 * ((l2 >> 4) & 3) + ((l2 >> 2) & 3)) * 32 + (l2 & 3) * 8 + (s == 0 ? G1_QE : G1_QL);
        fb = opaque(LP + (qb + qo * 128)); fb2 = opaque(LP + (qb + (1 - qo) * 128));
      }
      constexpr int o = (s < 8 ? s : s - 8) * 1024;
      wf[i % 3] = tr_frag(fb + o, fb2 + o);
    }
  };
  fetch(IC<0>{}); fetch(IC<1>{});
  SCHED0();
  wait0_x6(sg4, sg2);
#pragma unroll 1
  for (int it = 0; it < (1 << 16); ++it) {
    const bool bad = (sg4[0] & sg4[1]) == 0xffffffffu || (sg4[2] & sg4[3]) == 0xffffffffu || (sg2[0] & sg2[1]) == 0xffffffffu;
    if (!__any(bad)) break;
    __builtin_amdgcn_s_sleep(2);
    const u32 so = tt * 2048 + ((rq >> 1) * 16 + (l & 15)) * 64 + (rq & 1) * 24;
    ld_sc1_x4(sg4, so, stb); ld_sc1_x2<16>(sg2, so, stb);
    wait0_x6(sg4, sg2);
  }
  float nmu[2], k1[2], k2[2], kd[2];
  {
    const float t1 = swap16_sum(__uint_as_float(sg4[0]) + __uint_as_float(sg4[2]) + __uint_as_float(sg2[0]));
    const float t2 = swap16_sum(__uint_as_float(sg4[1]) + __uint_as_float(sg4[3]) + __uint_as_float(sg2[1]));
    const float mu = hide(t1 * (1.f / D_));
    const float var = fmaxf(t2 * (1.f / D_) - mu * mu, 0.f) + 1e-5f;
    const auto m2 = __builtin_amdgcn_permlane32_swap(__float_as_uint(-mu), __float_as_uint(-mu), false, false);
    const auto v2 = __builtin_amdgcn_permlane32_swap(__float_as_uint(var), __float_as_uint(var), false, false);
#pragma unroll
    for (int tl = 0; tl < 2; ++tl) {
      kd[tl] = __uint_as_float(v2[tl]);
      const float r = __builtin_amdgcn_rsqf(kd[tl]);
      nmu[tl] = __uint_as_float(m2[tl]);
      k1[tl] = hide(GC * r);
      k2[tl] = hide(k1[tl] * 0.044715f * r * r);
    }
  }
  char* hdst = ws + OFF_HH + ((size_t)tt * 288 + (fg >> 1) * 9 + (fg & 1) * 4) * 1024;
  const ldsp csb = opaque(LP + (G1_CS + rq * 32));
  f32x4 cc;
  auto ldcs = [&](int lc, int hfx) { cc = *(const LAS f32x4*)(csb + (lc * 128 + hfx * 16)); };
  u32x4 eo[2];
  u32x2 eq[2];
  float m0, m1, tu0, tu1, y0, y1;
  auto emit = [&](int lc, int i, u32 pk) {
    const int tl = i & 1, hx = i >> 1;
    if (lc == 8) {
      eq[tl][hx] = pk;
      if (hx) st_sc1_x2(hdst + (size_t)tl * 147456 + (8 - (fg & 1) * 4) * 1024 + (fg & 1) * 8, lo16, eq[tl]);
    } else {
      eo[tl][(lc & 1) * 2 + hx] = pk;
      if ((lc & 1) && hx) st_sc1_x4(hdst + (size_t)tl * 147456 + (lc >> 1) * 1024, lo16, eo[tl]);
    }
  };
  auto epi = [&](const Acc& a, int lc, int st, int nlc) {
    const int i = st / 6, sg = st % 6, tl = i & 1, e = (i >> 1) * 2;
    if (sg == 0) {
      if (i > 0) { u32 pk = pack2(tu0, tu1 * y1); pin(pk); emit(lc, i - 1, pk); }
      if (i < 4) { m0 = fmaf(nmu[tl], cc[0], a.a[0][tl][e]); m1 = fmaf(nmu[tl], cc[1], a.a[0][tl][e + 1]); pinf(m0); pinf(m1); }
    } else if (sg == 1) {
      tu0 = fmaf(nmu[tl], cc[2], a.a[1][tl][e]); tu1 = fmaf(nmu[tl], cc[3], a.a[1][tl][e + 1]);
      y0 = hide(m0 * m0); y1 = m1 * m1;
      pinf(tu0); pinf(tu1); pinf(y0); pinf(y1);
      if (i == 1) ldcs(lc, 1);
      if (i == 3 && nlc >= 0) ldcs(nlc, 0);
    } else if (sg == 2) {
      y0 = hide(fmaf(y0, k2[tl], k1[tl])); y1 = fmaf(y1, k2[tl], k1[tl]); tu0 = hide(tu0 * m0); tu1 = tu1 * m1;
      pinf(y0); pinf(y1); pinf(tu0); pinf(tu1);
    } else if (sg == 3) {
      y0 = hide(y0 * m0); y1 = y1 * m1; y0 = __builtin_amdgcn_exp2f(y0);
      pinf(y0); pinf(y1);
    } else if (sg == 4) {
      y1 = __builtin_amdgcn_exp2f(y1); y0 = __builtin_amdgcn_rcpf(fmaf(y0, kd[tl], kd[tl]));
      pinf(y0); pinf(y1);
    } else {
      y1 = __builtin_amdgcn_rcpf(fmaf(y1, kd[tl], kd[tl])); tu0 = tu0 * y0;
      pinf(y1); pinf(tu0);
    }
  };
  const int r5 = b >> 3;
  const int ks = b & 3, tg2 = ((b >> 2) & 1) * 8 + (r5 & 7), dt = r5 >> 3;
  const int tq = w & 3, kh = w >> 2;
  constexpr u32 RD = D_ * 2;
  const char* d_src = (const char*)wd + dt * 256;
  const char* dq_src = (const char*)wd + 1024 + dt * 32;
  const u32 d_dst = L0 + kh * 9216 + tq * 2048;
  const int mb = ks * 4 + kh * 2;
  auto row0 = [&](int kk) { const int q = kk / 9, i = kk - 9 * q; return i < 8 ? (mb + q) * 256 + i * 32 : 4096 + (mb + q) * 32; };
  u32x4 hb[HB][2];
  const char* hsrc0 = ws + OFF_HH + ((size_t)(tg2 * 8 + tq * 2 + kh) * 144 + mb * 9) * 1024;
  const char* hsrc1 = ws + OFF_HH + ((size_t)(tg2 * 8 + tq * 2 + (kh ^ 1)) * 144 + mb * 9) * 1024;
  const char* myflag = ws + OFF_FLG + 16384 + b * 64;
  const char* pflags = ws + OFF_FLG + 16384 + ((b & 7) + kh * 32 + ((r5 & 7) >> 1) * 64) * 64;
  u32 d_voff = 0, dq_voff = 0, p_voff = 0, pv = 0, q_voff = 0;
  f32x4 ac2[9][2];
  ldsp pb[5];
  u32 rb2 = 0, rbq = 0;
  auto fetch2 = [&](auto ic) {
    constexpr int i = ic.value, s = i / 9, c = i % 9, stg = s % NST;
    if constexpr (c == 0 && (stg % 3 == 0)) {
#pragma unroll
      for (int u = 0; u < 4; ++u) pb[u] = opaque((ldsp)(uintptr_t)((opq(rb2) ^ (u32)(u * 32)) + (u32)(stg * STG)));
      pb[4] = opaque((ldsp)(uintptr_t)(opq(rbq) + (u32)(stg * STG)));
    }
    constexpr int o = (stg % 3) * STG;
    if constexpr (c < 8) wf[i & 3] = tr_frag(pb[c & 3] + (o + (c >> 2) * 1024), pb[c & 3] + (o + (c >> 2) * 1024 + 4096));
    else if constexpr (s < 4) { const ldsp qe = (ldsp)(uintptr_t)(opq(rbq) + (u32)(G1_QE - 8192) - kh * (9216 - 1024)); wf[i & 3] = tr_frag(qe + s * 2048, qe + (s * 2048 + 512)); }
    else wf[i & 3] = tr_frag(pb[4] + o, pb[4] + (o + 512));
  };
  auto issue2 = [&](auto uc) {
    constexpr int u = uc.value;
    constexpr int nw0 = find_at(18, u, [](int n) { return w_issue(n, 0); }), nw1 = find_at(18, u, [](int n) { return w_issue(n, 1); });
    constexpr int nh0 = find_at(18, u, [](int n) { return h_issue(n, 0); }), nh1 = find_at(18, u, [](int n) { return h_issue(n, 1); });
    constexpr int r = find_at(5, u, [](int n) { return dq_issue(n); });
    constexpr int pi = find_at(3, u, [](int n) { return poll_iss(n); }), pc = find_at(3, u, [](int n) { return poll_chk(n); });
    if constexpr (nw0 >= 0) dma16(d_src + (u32)row0(kk_of(nw0)) * RD, d_voff, d_dst + (nw0 % NST) * STG);
    if constexpr (nw1 >= 0) dma16(d_src + (u32)row0(kk_of(nw1)) * RD + 128, d_voff, d_dst + (nw1 % NST) * STG + 1024);
    if constexpr (nh0 >= 0) gload16_sc1<(kk_of(nh0) & 3) * 1024>(hb[nh0 % HB][0], lo16, hsrc0 + (kk_of(nh0) >> 2) * 4096);
    if constexpr (nh1 >= 0) gload16_sc1<(kk_of(nh1) & 3) * 1024>(hb[nh1 % HB][1], lo16, hsrc1 + (kk_of(nh1) >> 2) * 4096);
    if constexpr (r >= 0) {
      const int tsel = r == 4 ? (tq & 1) : tq;
      const int ra = row0(kk_of(4 * r)), rb = row0(kk_of(4 * r + 1)), rc_ = row0(kk_of(r == 4 ? 16 : 4 * r + 2)), rd = row0(kk_of(r == 4 ? 17 : 4 * r + 3));
      const int rsel = (tsel & 2) ? ((tsel & 1) ? rd : rc_) : ((tsel & 1) ? rb : ra);
      dma16(dq_src + (u32)rsel * RD, dq_voff, r == 0 ? L0 + G1_QE + (tq * 2 + kh) * 1024 : L0 + (((4 * r) % NST) + tsel) * STG + kh * 9216 + 8192);
    }
    if constexpr (pi >= 0) poll_issue(pv, p_voff, pflags);
    if constexpr (pc >= 0) poll_check<wait_k(4 * poll_iss(pc) + 2, 4 * u + 2), pc + 1>(pv, p_voff, pflags);
  };
  static_for<0, 216>([&](auto ic) {
    constexpr int t = ic.value, p = t / 36, s = t % 36;
    constexpr int slot = (p + 3) & 3;
    constexpr int hg = (p == 0) ? ((s >> 1) + 14) % 18 : (s >> 1);
    if constexpr (t + 2 < 216) fetch(IC<t + 2>{});
#pragma unroll
    for (int tl = 0; tl < 2; ++tl) mma<(s < 2)>(acc[slot].a[s & 1][tl], wf[t % 3], h[tl][hg]);
    if constexpr (t == 2) {
      const u32 pi = min(opq(tid), 71u);
      const ldsp cp = LP + (G1_CP + pi * 16);
      const f32x4 r = (*(const LAS f32x4*)cp + *(const LAS f32x4*)(cp + 1152)) + (*(const LAS f32x4*)(cp + 2304) + *(const LAS f32x4*)(cp + 3456));
      *(LAS f32x4*)(LP + (G1_CS + pi * 16)) = r;
    }
    if constexpr (t == G1_E0 - 1) ldcs(0, 0);
    if constexpr (t >= G1_E0) {
      constexpr int ci = (t - G1_E0) / 26, st = (t - G1_E0) % 26;
      if constexpr (ci < 8 && st <= 24) {
        constexpr int lc = ci < 4 ? ci : (ci == 4 ? 8 : ci - 1);
        constexpr int nlc = ci < 3 ? ci + 1 : (ci == 3 ? 8 : ci);
        epi(acc[ci & 3], lc, st, nlc);
      }
    }
    if constexpr (t == 1 || t == 7 || t == 28 || t == 72 || t == 190) { SCHED0(); BARRIER(); }
    if constexpr (t == 41) { wait_vm<wait_q(1, 41)>(); BARRIER(); }
    if constexpr (t == 57) { wait_vm<wait_q(3, 57)>(); BARRIER(); }
    if constexpr (t == 69) { wait_vm<wait_b(9, 69)>(); BARRIER(); }
    if constexpr (t == 89) { wait_vm<wait_b(14, 89)>(); BARRIER(); }
    if constexpr (t == 99) { wait_vm<wait_b(17, 99)>(); BARRIER(); }
    if constexpr (t == 162) { wait_vm<wait_k(4 * (epi_start(4) + 24), 4 * t + 1)>(); BARRIER(); flag_store(w, opq(0u), opq(1u), myflag); }
    if constexpr (t == 198) { SCHED0(); BARRIER(); }
    if constexpr (t == 208) { SCHED0(); wait_vm<wait_k(4 * w_issue(1, 1) + 2, 4 * t + 1)>(); BARRIER(); }
    if constexpr (t == 7) q_voff = mk_qvoff(opq(tid) & 63);
    if constexpr (t == 180) {
      const u32 l2 = opq(tid) & 63;
      d_voff = (u32)((8 * tq + (l2 >> 3)) * RD + ((((l2 & 7) >> 1) ^ ((l2 >> 4) & 3)) * 32) + (l2 & 1) * 16);
      dq_voff = (u32)((l2 >> 1) * RD + (l2 & 1) * 16);
      p_voff = (l2 & 3) * 512;
      pin(d_voff); pin(dq_voff); pin(p_voff);
    }
    {
      constexpr int r = find_at(4, t, [](int n) { return g1_q_issue(0, n); }), j = find_at(18, t, [](int n) { return g1_b_issue(0, n); });
      if constexpr (r >= 0) dma16(q_src + (u32)rot(4 + 4 * r + (w >> 1)) * (u32)(32 * RS), q_voff, L0 + G1_QL + r * 8192 + w * 1024);
      if constexpr (j >= 0) dma16(a_src + 128 + (u32)rot(j) * (u32)(32 * RS), ab_voff, ab_dst + j * 8192);
    }
    if constexpr (t >= PF_ISS0 && t < PF_ISS0 + 20 && (t - PF_ISS0) % 4 == 0) {
      const int piece = ((b >> 3) * 5 + (t - PF_ISS0) / 4) * 8 + w;
      const int byte = piece * 1024;
      const char* src = (const char*)wd + (byte < 1024 * 1152 ? (b & 3) * 1024 * 1152 + byte : 4096 * 1152 + (b & 3) * 128 * 1152 + (byte - 1024 * 1152));
      dma16(src, lo16, L0 + G1_QE + w * 1024);
    }
    if constexpr (t >= 180) issue2(ic);
    SCHED0();
    if constexpr (t % 24 == 23) { ST(5 + t / 24); }
  });
  STR(29);
  {
    const u32 l2 = opq(tid) & 63;
    const u32 q2 = l2 >> 4, r2_ = (l2 >> 2) & 3, p2 = l2 & 3;
    rb2 = L0 + kh * 9216 + (q2 >> 1) * 2048 + (4 * (q2 & 1) + r2_) * 128 + ((2 * (q2 & 1) + (r2_ >> 1)) * 32) + p2 * 8;
    rbq = L0 + kh * 9216 + 8192 + (4 * q2 + r2_) * 32 + p2 * 8;
  }
  const int ft = tg2 * 8 + ks * 2 + kh;
  const int dcol = dt * 128 + tq * 32 + 4 * rq;
  const int qcol = 512 + dt * 16 + 4 * rq;
  u32x2 r0, r1, r2;
  fetch2(IC<0>{}); fetch2(IC<1>{}); fetch2(IC<2>{});
  wait_vm2<wait_k(4 * h_issue(0, 1) + 2, 4 * U2 - 1)>(hb[0][0], hb[0][1]);
  static_for<0, 162>([&](auto ic) {
    constexpr int i = ic.value, s = i / 9, c = i % 9, u = U2 + i;
    if constexpr (c == 6 && s < 17) {
      constexpr int tgt = cmax(cmax(cmax(4 * h_issue(s + 1, 1) + 2, 4 * w_issue(s + 1, 1) + 2), 4 * dq_issue((s + 1) >> 2) + 2), s == 6 ? 4 * (epi_start(8) + 24) : 0);
      wait_vm2<wait_k(tgt, 4 * u - 1)>(hb[(s + 1) % HB][0], hb[(s + 1) % HB][1]);
      BARRIER();
      if constexpr (s == 0) flag_store(w, opq(0u), opq(2u), myflag);
      if constexpr (s == 6) flag_store(w, opq(0u), opq(3u), myflag);
    }
    if constexpr (i + 3 < 162) fetch2(IC<i + 3>{});
#pragma unroll
    for (int tl = 0; tl < 2; ++tl) mma<s == 0>(ac2[c][tl], wf[i & 3], hb[s % HB][tl]);
    if constexpr (i >= 2 && i <= 26) epi(acc[0], 7, i - 2, -1);
    issue2(IC<u>{});
    if constexpr (u == RES_ISS) {
      const char* hb_ = ws + OFF_HF + (size_t)(ft >> 1) * 36864 + (ft & 1) * 8;
      auto po = [&](int d) { return (u32)((d >> 5) * 2048 + ((d >> 2) & 1) * 1024 + ((d >> 3) & 3) * 256 + (l & 15) * 16); };
      gload8(r0, po(dcol), hb_); gload8(r1, po(dcol + 16), hb_); gload8(r2, po(qcol), hb_);
    }
    SCHED0();
    if constexpr (c == 8 && s % 3 == 2) { ST(14 + s / 3); }
  });
  MFMA_DRAIN(); SCHED0();
  {
    const ldsp xw = LP + (2 * STG + w * 9216 + l * 16);
#pragma unroll
    for (int c = 0; c < 9; ++c) *(LAS f32x4*)(xw + c * 1024) = ac2[c][1];
  }
  LDS_BARRIER();
  f32x4 sm[9];
  {
    const ldsp xr = LP + (2 * STG + (w ^ 4) * 9216 + l * 16);
#pragma unroll
    for (int c = 0; c < 9; ++c) sm[c] = ac2[c][0] + *(const LAS f32x4*)(xr + c * 1024);
  }
  ST(20);
  char* part = ws + OFF_PART;
  if (tq != ks) {
    char* pdst = part + ((((size_t)(tg2 * 4 + dt) * 4 + tq) * 4 + ks) * 2 + kh) * 4608;
#pragma unroll
    for (int pc = 0; pc < 4; ++pc) {
      u32x4 v = {pack2(sm[2 * pc][0], sm[2 * pc][1]), pack2(sm[2 * pc][2], sm[2 * pc][3]), pack2(sm[2 * pc + 1][0], sm[2 * pc + 1][1]), pack2(sm[2 * pc + 1][2], sm[2 * pc + 1][3])};
      st_sc1_x4(pdst + pc * 1024, lo16, v);
    }
    u32x2 v2 = {pack2(sm[8][0], sm[8][1]), pack2(sm[8][2], sm[8][3])};
    st_sc1_x2(pdst + 4096, l * 8, v2);
  } else {
    const ldsp ow = LP + (kh * 9216 + l * 16);
#pragma unroll
    for (int c = 0; c < 9; ++c) *(LAS f32x4*)(ow + c * 1024) = sm[c];
  }
  asm volatile("s_waitcnt vmcnt(0)" : "+v"(r0), "+v"(r1), "+v"(r2)::"memory");
  LDS_BARRIER();
  ST(21);
  const char* fl = ws + OFF_FLG + (tg2 * 4 + dt) * 256;
  flag_store(w, opq(0u), opq(1u), fl + ks * 64);
  const ldsp orp = LP + (kh * 9216 + l * 16);
  f32x4 o0 = *(const LAS f32x4*)(orp + tq * 2048), o1 = *(const LAS f32x4*)(orp + (tq * 2048 + 1024)), o2 = *(const LAS f32x4*)(orp + 8192);
  o0[0] += bflo(r0[0]); o0[1] += bfhi(r0[0]); o0[2] += bflo(r0[1]); o0[3] += bfhi(r0[1]);
  o1[0] += bflo(r1[0]); o1[1] += bfhi(r1[0]); o1[2] += bflo(r1[1]); o1[3] += bfhi(r1[1]);
  o2[0] += bflo(r2[0]); o2[1] += bfhi(r2[0]); o2[2] += bflo(r2[1]); o2[3] += bfhi(r2[1]);
  {
    const u32 fo = (l & 3) * 64;
    poll_issue(pv, fo, fl);
    poll_check<0, 1>(pv, fo, fl);
  }
  ST(22);
  {
    const char* psrc = part + (((size_t)(tg2 * 4 + dt) * 4 + ks) * 4 * 2 + kh) * 4608;
    u32x4 v[3]; u32x2 vq[3];
#pragma unroll
    for (int j = 0; j < 3; ++j) {
      const int src = (ks + 1 + j) & 3;
      ld_sc1_x4(v[j], lo16 + tq * 1024, psrc + src * 9216);
      ld_sc1_x2<0>(vq[j], l * 8 + 4096, psrc + src * 9216);
    }
    asm volatile("s_waitcnt vmcnt(0)" : "+v"(v[0]), "+v"(v[1]), "+v"(v[2]), "+v"(vq[0]), "+v"(vq[1]), "+v"(vq[2])::"memory");
#pragma unroll
    for (int j = 0; j < 3; ++j) {
      o0[0] += bflo(v[j][0]); o0[1] += bfhi(v[j][0]); o0[2] += bflo(v[j][1]); o0[3] += bfhi(v[j][1]);
      o1[0] += bflo(v[j][2]); o1[1] += bfhi(v[j][2]); o1[2] += bflo(v[j][3]); o1[3] += bfhi(v[j][3]);
      o2[0] += bflo(vq[j][0]); o2[1] += bfhi(vq[j][0]); o2[2] += bflo(vq[j][1]); o2[3] += bfhi(vq[j][1]);
    }
    u16* op = out + (size_t)(ft * 16 + (l & 15)) * D_;
    u32x2 oa = {pack2(o0[0], o0[1]), pack2(o0[2], o0[3])}, ob = {pack2(o1[0], o1[1]), pack2(o1[2], o1[3])}, oc = {pack2(o2[0], o2[1]), pack2(o2[2], o2[3])};
    *(u32x2*)(op + dcol) = oa; *(u32x2*)(op + dcol + 16) = ob;
    if (tq == 0) *(u32x2*)(op + qcol) = oc;
  }
  asm volatile(".Lffn_code_end:");
  ST(23); ST_DRAIN(); ST(24);
  ST_FLUSH((u32*)(ws + (200u << 20)), b * 8 + w);
}

extern "C" void kernel_launch(void* const* d_in, const int* in_sizes, int n_in, void* d_out, int out_size, void* d_ws, size_t ws_size,
                              hipStream_t stream) {
  const u16* q = (const u16*)d_in[0]; const u16* k = (const u16*)d_in[1]; const u16* v = (const u16*)d_in[2];
  const u16* shk = (const u16*)d_in[3]; const u16* ssm = (const u16*)d_in[4]; const u16* ssmD = (const u16*)d_in[5];
  const u16* wg = (const u16*)d_in[7]; const u16* wu = (const u16*)d_in[8]; const u16* wd = (const u16*)d_in[9];
  char* ws = (char*)d_ws;
  (void)hipFuncSetAttribute((const void*)k1_conv, hipFuncAttributeMaxDynamicSharedMemorySize, K1_LDS);
  (void)hipFuncSetAttribute((const void*)ffn_fused, hipFuncAttributeMaxDynamicSharedMemorySize, FF_LDS);
  k0_prep<<<K0_GRID, 256, 0, stream>>>(q, k, v, shk, ssm, ws);
  k1_conv<<<K1_GRID, 512, K1_LDS, stream>>>(ssm, ssmD, wg, wu, ws);
  ffn_fused<<<256, 512, FF_LDS, stream>>>(wg, wu, wd, ws, (u16*)d_out);
}
```

```cpp
#include <hip/hip_runtime.h>
#include <hip/hip_bf16.h>
#include <stdint.h>
#include <utility>
#define G2_BAR_ALL 0
#define WE0 7
#define WE1 1
#define HD2 4
#define LASTX 2
#define HH_PLAIN 0
#define HLOAD bload_sc1
#define NT0 3
#define LDK(p) ((NT0 & 1) ? __builtin_nontemporal_load(p) : *(p))
#define LDV(p) ((NT0 & 2) ? __builtin_nontemporal_load(p) : *(p))
#define LDQ(p) ((NT0 & 4) ? __builtin_nontemporal_load(p) : *(p))
#define SIDE_DELAY 4

typedef unsigned short u16;
typedef unsigned int u32;
typedef __attribute__((ext_vector_type(2))) unsigned u32x2;
typedef __attribute__((ext_vector_type(4))) unsigned u32x4;
typedef __attribute__((ext_vector_type(2))) float f32x2;
typedef __attribute__((ext_vector_type(4))) float f32x4;
typedef __attribute__((ext_vector_type(8))) __bf16 bf16x8;
typedef __attribute__((ext_vector_type(2))) __bf16 bf16x2;
typedef __attribute__((ext_vector_type(4))) short s16x4;
#define GAS __attribute__((address_space(1)))
#define LAS __attribute__((address_space(3)))
#define DI __device__ __forceinline__
typedef LAS char* ldsp;

constexpr int S_ = 2048, D_ = 576, DF_ = 4608;
constexpr size_t OFF_KVT = 0;
constexpr size_t OFF_XP  = OFF_KVT + (size_t)D_ * S_ * 2;
constexpr size_t OFF_HF  = OFF_XP + (size_t)D_ * S_ * 2;
constexpr size_t OFF_ST1 = OFF_HF + (size_t)D_ * S_ * 2;
constexpr size_t OFF_CS  = OFF_ST1 + 2048 * 8;
constexpr size_t OFF_FLG = OFF_CS + 4 * 2 * 4608 * 4;
constexpr size_t OFF_ST2 = OFF_FLG + 36864;
constexpr size_t OFF_HH  = OFF_ST2 + 131072;
constexpr size_t OFF_PART = OFF_HH + (size_t)S_ * DF_ * 2;
constexpr size_t WS_USED = OFF_PART + 64 * 4 * 4 * 2 * 4608;

DI float bflo(u32 w) { return __uint_as_float(w << 16); }
DI float bfhi(u32 w) { return __uint_as_float(w & 0xffff0000u); }
DI u32 pack2(float a, float b) { bf16x2 p; p[0] = (__bf16)a; p[1] = (__bf16)b; return __builtin_bit_cast(u32, p); }

template <int I> struct IC { static constexpr int value = I; };
template <int... I> struct Seq {};
template <int B, class F, int... I> DI void static_for_impl(F&& f, std::integer_sequence<int, I...>) { (f(IC<B + I>{}), ...); }
template <int B, int E, class F> DI void static_for(F&& f) { static_for_impl<B>(f, std::make_integer_sequence<int, E - B>{}); }
template <int N> DI void wait_vm() { asm volatile("s_waitcnt vmcnt(%0)" ::"n"(N) : "memory"); }
#define BARRIER() do { asm volatile("" ::: "memory"); __builtin_amdgcn_s_barrier(); asm volatile("" ::: "memory"); } while (0)
#define LDS_BARRIER() do { asm volatile("s_waitcnt lgkmcnt(0)" ::: "memory"); __builtin_amdgcn_s_barrier(); asm volatile("" ::: "memory"); } while (0)
#define SCHED0() __builtin_amdgcn_sched_barrier(0)
constexpr int cmin(int a, int b) { return a < b ? a : b; }

DI u32x4 tr_frag(ldsp a0, ldsp a1) {
  const s16x4 lo = __builtin_amdgcn_ds_read_tr16_b64_v4i16((LAS s16x4*)a0);
  const s16x4 hi = __builtin_amdgcn_ds_read_tr16_b64_v4i16((LAS s16x4*)a1);
  return __builtin_bit_cast(u32x4, __builtin_shufflevector(lo, hi, 0, 1, 2, 3, 4, 5, 6, 7));
}
struct Frag { s16x4 lo, hi; };
DI s16x4 tr_half(ldsp a) { return __builtin_amdgcn_ds_read_tr16_b64_v4i16((LAS s16x4*)a); }
DI u32x4 frag_of(const Frag& f) { return __builtin_bit_cast(u32x4, __builtin_shufflevector(f.lo, f.hi, 0, 1, 2, 3, 4, 5, 6, 7)); }
DI f32x4 mfma16(const u32x4& a, const u32x4& b, const f32x4& c) {
  return __builtin_amdgcn_mfma_f32_16x16x32_bf16(__builtin_bit_cast(bf16x8, a), __builtin_bit_cast(bf16x8, b), c, 0, 0, 0);
}
template <bool FIRST> DI void mma(f32x4& c, const u32x4& a, const u32x4& b) {
  if constexpr (FIRST) asm volatile("v_mfma_f32_16x16x32_bf16 %0, %1, %2, 0" : "=&v"(c) : "v"(a), "v"(b));
  else asm volatile("v_mfma_f32_16x16x32_bf16 %0, %1, %2, %0" : "+v"(c) : "v"(a), "v"(b));
}
#define MFMA_DRAIN() asm volatile("s_nop 15" ::: "memory")
template <int OFF> DI void gload16_sc1(u32x4& dst, u32 voff, const char* sbase) {
  asm volatile("global_load_dwordx4 %0, %1, %2 offset:%3 sc1" : "=v"(dst) : "v"(voff), "s"(sbase), "n"(OFF) : "memory");
}
DI void poll_issue(u32& f, u32 voff, const char* sb) { asm volatile("global_load_dword %0, %1, %2 sc1" : "=v"(f) : "v"(voff), "s"(sb) : "memory"); }
template <int N, int PH> DI void poll_check(u32& f, u32 voff, const char* sb) {
  u32 cnt = 1u << 18;
  asm volatile("s_waitcnt vmcnt(%4)\n"
               "1:\n\tv_cmp_gt_u32_e32 vcc, %5, %0\n\ts_nop 4\n\ts_cbranch_vccz 2f\n\t"
               "s_sleep 1\n\tglobal_load_dword %0, %2, %3 sc1\n\ts_waitcnt vmcnt(0)\n\t"
               "s_sub_u32 %1, %1, 1\n\ts_cbranch_scc0 1b\n"
               "2:" : "+v"(f), "+s"(cnt) : "v"(voff), "s"(sb), "n"(N), "n"(PH) : "memory", "vcc", "scc");
}
DI void poll4(u32 voff, const char* sb) {
  u32 cnt = 1u << 18;
  u32x4 f; u32 m;
  asm volatile("1:\n\tglobal_load_dwordx4 %0, %3, %4 sc1\n\ts_waitcnt vmcnt(0)\n\t"
               "v_min3_u32 %1, v100, v101, v102\n\tv_min_u32_e32 %1, %1, v103\n\tv_cmp_eq_u32_e32 vcc, 0, %1\n\ts_nop 4\n\ts_cbranch_vccz 2f\n\t"
               "s_sleep 4\n\ts_sub_u32 %2, %2, 1\n\ts_cbranch_scc0 1b\n"
               "2:" : "=&{v[100:103]}"(f), "=&v"(m), "+s"(cnt) : "v"(voff), "s"(sb) : "memory", "vcc", "scc");
}
DI void swap_halves(u32x4& x, u32x4& y) {
  u32 a = x[2], b = x[3], c = y[0], d = y[1];
  asm volatile("v_swap_b32 %0, %2\n\tv_swap_b32 %1, %3" : "+v"(a), "+v"(b), "+v"(c), "+v"(d));
  x[2] = a; x[3] = b; y[0] = c; y[1] = d;
}
DI void flag_store(int w, u32 zero_off, u32 val, const char* sb) {
  asm volatile("s_cmp_lg_u32 %3, 0\n\ts_cbranch_scc1 1f\n\tglobal_store_dword %0, %1, %2 sc1\n1:" ::"v"(zero_off), "v"(val), "s"(sb), "s"(w) : "memory", "scc");
}
template <int OFF> DI void gload16(u32x4& dst, u32 voff, const char* sbase) {
  asm volatile("global_load_dwordx4 %0, %1, %2 offset:%3" : "=v"(dst) : "v"(voff), "s"(sbase), "n"(OFF) : "memory");
}
DI void gload8(u32x2& dst, u32 voff, const char* sbase) { asm volatile("global_load_dwordx2 %0, %1, %2" : "=v"(dst) : "v"(voff), "s"(sbase) : "memory"); }
template <int N, int VAL> DI void waitbar_if(int sel, u32x4& a, u32x4& b) {
  asm volatile("s_cmp_lg_u32 %2, %4\n\ts_cbranch_scc1 1f\n\ts_waitcnt vmcnt(%3)\n\ts_barrier\n1:" : "+v"(a), "+v"(b) : "s"(sel), "n"(N), "n"(VAL) : "memory", "scc");
}
template <int N> DI void wait_vm2(u32x4& a, u32x4& b) { asm volatile("s_waitcnt vmcnt(%2)" : "+v"(a), "+v"(b) : "n"(N) : "memory"); }
DI void dma16(const char* sbase, u32 voff, u32 lds_dst) {
  asm volatile("s_mov_b32 m0, %2\n\ts_nop 0\n\tglobal_load_lds_dwordx4 %0, %1" ::"v"(voff), "s"(sbase), "s"(lds_dst) : "memory");
}
template <int VAL> DI void dma16_if(int sel, const char* sbase, u32 voff, u32 lds_dst) {
  asm volatile("s_cmp_eq_u32 %3, %4\n\ts_mov_b32 m0, %2\n\ts_cselect_b64 exec, -1, 0\n\tglobal_load_lds_dwordx4 %0, %1\n\ts_mov_b64 exec, -1" ::"v"(voff), "s"(sbase), "s"(lds_dst), "s"(sel), "n"(VAL) : "memory", "scc");
}
typedef __amdgpu_buffer_rsrc_t rsrc_t;
DI rsrc_t mk_rsrc(const void* p, u32 bytes) { return __builtin_amdgcn_make_buffer_rsrc((void*)p, (short)0, (int)bytes, 0x00020000); }
template <int LOFF> DI void bdma(rsrc_t r, u32 voff, u32 soff, u32 ldsb) {
  asm volatile("s_add_u32 m0, %3, %4\n\ts_nop 0\n\tbuffer_load_dwordx4 %0, %1, %2 offen lds" ::"v"(voff), "s"(r), "s"(soff), "s"(ldsb), "n"(LOFF) : "memory", "scc");
}
template <int OFF> DI void bload(u32x4& d, rsrc_t r, u32 voff, u32 soff) {
  asm volatile("buffer_load_dwordx4 %0, %1, %2, %3 offen offset:%4" : "=v"(d) : "v"(voff), "s"(r), "s"(soff), "n"(OFF) : "memory");
}
template <int OFF> DI void bload_sc1(u32x4& d, rsrc_t r, u32 voff, u32 soff) {
  asm volatile("buffer_load_dwordx4 %0, %1, %2, %3 offen offset:%4 sc1" : "=v"(d) : "v"(voff), "s"(r), "s"(soff), "n"(OFF) : "memory");
}
DI void stat_loads(int fg, u32x4 (&x)[6], rsrc_t r, u32 voff, u32 soff, u32 soff2) {
  asm volatile("s_cmp_gt_u32 %10, 5\n\ts_cbranch_scc1 1f\n\t"
               "buffer_load_dwordx4 %0, %6, %7, %8 offen\n\tbuffer_load_dwordx4 %1, %6, %7, %8 offen offset:1024\n\t"
               "buffer_load_dwordx4 %2, %6, %7, %8 offen offset:2048\n\tbuffer_load_dwordx4 %3, %6, %7, %8 offen offset:3072\n\t"
               "buffer_load_dwordx4 %4, %6, %7, %9 offen\n\tbuffer_load_dwordx4 %5, %6, %7, %9 offen offset:1024\n1:"
               : "=&v"(x[0]), "=&v"(x[1]), "=&v"(x[2]), "=&v"(x[3]), "=&v"(x[4]), "=&v"(x[5])
               : "v"(voff), "s"(r), "s"(soff), "s"(soff2), "s"(fg) : "memory", "scc");
}
DI ldsp opaque(ldsp p) { asm volatile("" : "+v"(p)); return p; }
DI u32 opq(u32 p) { asm volatile("" : "+v"(p)); return p; }
DI void pin(u32& p) { asm volatile("" : "+v"(p)); }
DI void pin6(u32x4 (&x)[6]) { asm volatile("" : "+v"(x[0]), "+v"(x[1]), "+v"(x[2]), "+v"(x[3]), "+v"(x[4]), "+v"(x[5])); }
DI void pinf(float& p) { asm volatile("" : "+v"(p)); }
DI float hide(float p) { asm("" : "+v"(p)); return p; }
DI float swap16_sum(float x) { const auto r = __builtin_amdgcn_permlane16_swap(__float_as_uint(x), __float_as_uint(x), false, false); return __uint_as_float(r[0]) + __uint_as_float(r[1]); }
DI float swap32_sum(float x) { const auto r = __builtin_amdgcn_permlane32_swap(__float_as_uint(x), __float_as_uint(x), false, false); return __uint_as_float(r[0]) + __uint_as_float(r[1]); }
#define STAT1(R, A, B) "v_lshlrev_b32_e32 %0, 16, " R "\n\tv_and_b32_e32 %1, %10, " R "\n\tv_add_f32_e32 " A ", " A ", %0\n\tv_fmac_f32_e32 " B ", %0, %0\n\tv_add_f32_e32 " A ", " A ", %1\n\tv_fmac_f32_e32 " B ", %1, %1\n\t"
DI void stat_acc(int fg, const u32x4& v, float (&s1)[2], float (&s2)[2]) {
  float x, y;
  asm volatile("s_cmp_gt_u32 %11, 5\n\ts_cbranch_scc1 1f\n\t" STAT1("%6", "%2", "%3") STAT1("%8", "%4", "%5") STAT1("%7", "%2", "%3") STAT1("%9", "%4", "%5") "\n1:"
               : "=&v"(x), "=&v"(y), "+v"(s1[0]), "+v"(s2[0]), "+v"(s1[1]), "+v"(s2[1]) : "v"(v[0]), "v"(v[1]), "v"(v[2]), "v"(v[3]), "s"(0xffff0000u), "s"(fg) : "scc");
}
DI void ld_sc1_x4(u32x4& d, u32 voff, const char* sb) { asm volatile("global_load_dwordx4 %0, %1, %2 sc1" : "=v"(d) : "v"(voff), "s"(sb) : "memory"); }
template <int OFF> DI void ld_sc1_x2(u32x2& d, u32 voff, const char* sb) { asm volatile("global_load_dwordx2 %0, %1, %2 offset:%3 sc1" : "=v"(d) : "v"(voff), "s"(sb), "n"(OFF) : "memory"); }
DI void wait0_x6(u32x4& a, u32x2& b) { asm volatile("s_waitcnt vmcnt(0)" : "+v"(a), "+v"(b)::"memory"); }
DI void st_sc1_x4(char* sb, u32 voff, const u32x4& v) { asm volatile("global_store_dwordx4 %0, %1, %2 sc1\n\ts_nop 1" ::"v"(voff), "v"(v), "s"(sb) : "memory"); }
DI void gstore16(char* sb, u32 voff, const u32x4& v) { asm volatile("global_store_dwordx4 %0, %1, %2\n\ts_nop 1" ::"v"(voff), "v"(v), "s"(sb) : "memory"); }
DI void gstore8(char* sb, u32 voff, const u32x2& v) { asm volatile("global_store_dwordx2 %0, %1, %2" ::"v"(voff), "v"(v), "s"(sb) : "memory"); }
DI void st_sc1_x2_if(int skip, const char* sb, u32 voff, const u32x2& v) {
  asm volatile("s_cmp_gt_u32 %3, 5\n\ts_cbranch_scc1 1f\n\tglobal_store_dwordx2 %0, %1, %2 sc1\n1:" ::"v"(voff), "v"(v), "s"(sb), "s"(skip) : "memory", "scc");
}
DI void st_sc1_x2(char* sb, u32 voff, const u32x2& v) { asm volatile("global_store_dwordx2 %0, %1, %2 sc1" ::"v"(voff), "v"(v), "s"(sb) : "memory"); }

#define STR(i)
#define ST(i)
#define ST_DECL
#define ST_FLUSH(dbg, wv)
#define ST_DRAIN()

DI void colsum_slice(const u16* __restrict__ wg, const u16* __restrict__ wu, float* __restrict__ cs, int sl, int l) {
  const int mat = sl & 1, qr = (sl >> 1) & 3, cb = sl >> 3;
  const u16* p = (mat ? wu : wg) + (size_t)(qr * 144 + (l >> 3)) * DF_ + cb * 64 + (l & 7) * 8;
  u32x4 x[18];
#pragma unroll
  for (int i = 0; i < 18; ++i) x[i] = *(const u32x4*)(p + (size_t)i * 8 * DF_);
  float a[8];
#pragma unroll
  for (int e = 0; e < 8; ++e) a[e] = 0.f;
#pragma unroll
  for (int i = 0; i < 18; ++i)
#pragma unroll
    for (int e = 0; e < 4; ++e) { a[2 * e] += bflo(x[i][e]); a[2 * e + 1] += bfhi(x[i][e]); }
#pragma unroll
  for (int e = 0; e < 8; ++e) { a[e] += __shfl_xor(a[e], 8); a[e] = swap32_sum(swap16_sum(a[e])); }
  if (l < 8) {
    float* d = cs + qr * 9216 + (cb * 32 + l * 4) * 4 + mat * 2;
#pragma unroll
    for (int e = 0; e < 4; ++e) { f32x2 o = {a[2 * e], a[2 * e + 1]}; *(f32x2*)(d + e * 4) = o; }
  }
}

constexpr int K0_GRID = 64 + 288;
template <bool NT> DI void dma16p(const char* sbase, u32 voff, u32 lds_dst) {
  if constexpr (NT) asm volatile("s_mov_b32 m0, %2\n\ts_nop 0\n\tglobal_load_lds_dwordx4 %0, %1 nt" ::"v"(voff), "s"(sbase), "s"(lds_dst) : "memory");
  else dma16(sbase, voff, lds_dst);
}
extern "C" __global__ __launch_bounds__(256) void k0_prep(const u16* __restrict__ q, const u16* __restrict__ k, const u16* __restrict__ v,
                                                          const u16* __restrict__ shk, const u16* __restrict__ ssm, char* __restrict__ ws) {
  __shared__ __attribute__((aligned(128))) char lds[(72 + 64 + 64 + 64) * 128];
  ST_DECL; ST(0);
  asm volatile("" ::"s"(q), "s"(k), "s"(v), "s"(shk), "s"(ssm), "s"(ws));
  const int tid = threadIdx.x, l = tid & 63;
  const int w = __builtin_amdgcn_readfirstlane(tid >> 6);
  if (blockIdx.x >= 64) {
    const int b = blockIdx.x - 64;
    const int r8 = b >> 3, tb = r8 < 32 ? r8 : (b & 7) * 4 + (r8 - 32), cb = r8 < 32 ? (b & 7) : 8;
    const ldsp LP = (ldsp)lds;
    const u32 L0 = (u32)(uintptr_t)LP;
    constexpr int KT = 0, VT = 72 * 128, QT = VT + 8192, OT = QT + 8192;
    const int col = cb * 128 + (l & 7) * 16, rl = l >> 3;
    {
      const int r0 = tb * 64 - 8 + w * 8 + rl;
      dma16p<true>((const char*)k, (u32)(max(r0, 0) * (D_ * 2) + col), L0 + KT + w * 1024);
      const u32 vo = (u32)((tb * 64 + w * 8 + rl) * (D_ * 2) + col);
      dma16p<true>((const char*)k + 24 * D_ * 2, vo, L0 + KT + (w + 4) * 1024);
      if (w == 0) dma16p<true>((const char*)k + 56 * D_ * 2, vo, L0 + KT + 8 * 1024);
      dma16p<true>((const char*)v, vo, L0 + VT + w * 1024);
      dma16p<true>((const char*)v + 32 * D_ * 2, vo, L0 + VT + (w + 4) * 1024);
      dma16p<false>((const char*)q, vo, L0 + QT + w * 1024);
      dma16p<false>((const char*)q + 32 * D_ * 2, vo, L0 + QT + (w + 4) * 1024);
    }
    const int sl = w * 16 + (l >> 4) * 4, cq = l & 15;
    u32x4 w0, w1, pf0, pf1;
    gload16<0>(w0, (u32)((cb * 64 + cq * 4) * 8), (const char*)shk);
    gload16<16>(w1, (u32)((cb * 64 + cq * 4) * 8), (const char*)shk);
    const u32 po = (u32)((r8 < 32 ? (b & 7) * 64 + r8 * 2 : 512 + (b & 7) * 8 + (r8 - 32) * 2) * 4096 + tid * 16);
    gload16<0>(pf0, po, (const char*)ssm);
    gload16<0>(pf1, po + 4096, (const char*)ssm);
    if (b < 41) {
      const u32 f = b < 9 ? 0u : 0xffffffffu;
      u32x4 z = {f, f, f, f};
      gstore16(ws + OFF_FLG, (u32)(b * 4096 + tid * 16), z);
    }
    ST(3);
    wait_vm2<2>(w0, w1);
    BARRIER();
    ST(4);
    u32x2 kr[7], vr[4];
#pragma unroll
    for (int i = 0; i < 7; ++i) kr[i] = *(const LAS u32x2*)(LP + (KT + (sl + 5 + i) * 128 + cq * 8));
#pragma unroll
    for (int i = 0; i < 4; ++i) vr[i] = *(const LAS u32x2*)(LP + (VT + (sl + i) * 128 + cq * 8));
#pragma unroll
    for (int i = 0; i < 2; ++i) {
      const int gl = i * 8 + (l >> 3), t2 = w * 16 + (l & 7) * 2;
      const u32x2 a = *(const LAS u32x2*)(LP + (QT + t2 * 128 + gl * 8)), c = *(const LAS u32x2*)(LP + (QT + (t2 + 1) * 128 + gl * 8));
      u32x4 o = {a[0], a[1], c[0], c[1]};
      gstore16(ws + OFF_XP, (u32)(((cb * 16 + gl) * S_ + tb * 64 + t2) * 8), o);
    }
    if (tb == 0 && sl == 0) { kr[0] = (u32x2){0u, 0u}; kr[1] = kr[0]; kr[2] = kr[0]; }
#pragma unroll
    for (int c = 0; c < 4; ++c) {
      const u32 wa = (c < 2) ? w0[2 * (c & 1)] : w1[2 * (c & 1)], wb = (c < 2) ? w0[2 * (c & 1) + 1] : w1[2 * (c & 1) + 1];
      const float f0 = bflo(wa), f1 = bfhi(wa), f2 = bflo(wb), f3 = bfhi(wb);
      float kk[7], kvv[4];
#pragma unroll
      for (int i = 0; i < 7; ++i) kk[i] = (c & 1) ? bfhi(kr[i][c >> 1]) : bflo(kr[i][c >> 1]);
#pragma unroll
      for (int i = 0; i < 4; ++i) {
        const float ks = kk[i + 3] * f0 + kk[i + 2] * f1 + kk[i + 1] * f2 + kk[i] * f3;
        const float vv = (c & 1) ? bfhi(vr[i][c >> 1]) : bflo(vr[i][c >> 1]);
        kvv[i] = ks * vv;
      }
      u32x2 o = {pack2(kvv[3], kvv[2]), pack2(kvv[1], kvv[0])};
      *(LAS u32x2*)(LP + (OT + (cq * 4 + c) * 128 + (((sl >> 3) ^ (cq & 7)) * 16) + ((sl & 4) ? 0 : 8))) = o;
    }
    LDS_BARRIER();
#pragma unroll
    for (int i = 0; i < 2; ++i) {
      const int ch = w * 16 + i * 8 + (l >> 3);
      const u32x4 o = *(const LAS u32x4*)(LP + (OT + (w * 16 + i * 8) * 128 + l * 16));
      gstore16(ws + OFF_KVT, (u32)((cb * 64 + ch) * 4096 + (tb * 8 + ((l & 7) ^ ((ch >> 2) & 7))) * 16), o);
    }
    ST(5);
    asm volatile("s_waitcnt vmcnt(0)" : "+v"(pf0), "+v"(pf1)::"memory");
  } else {
    const int row = blockIdx.x * 32 + w * 8 + (l >> 3);
    const u16* p = q + (size_t)row * D_ + (l & 7) * 8;
    u32x4 x[9];
#pragma unroll
    for (int i = 0; i < 9; ++i) x[i] = *(const u32x4*)(p + i * 64);
    float sum = 0.f;
#pragma unroll
    for (int i = 0; i < 9; ++i)
#pragma unroll
      for (int j = 0; j < 4; ++j) sum += bflo(x[i][j]) + bfhi(x[i][j]);
    sum += __shfl_xor(sum, 1); sum += __shfl_xor(sum, 2); sum += __shfl_xor(sum, 4);
    const float mu = sum * (1.f / D_);
    float sq = 0.f;
#pragma unroll
    for (int i = 0; i < 9; ++i)
#pragma unroll
      for (int j = 0; j < 4; ++j) { const float a = bflo(x[i][j]) - mu, c = bfhi(x[i][j]) - mu; sq += a * a + c * c; }
    sq += __shfl_xor(sq, 1); sq += __shfl_xor(sq, 2); sq += __shfl_xor(sq, 4);
    if ((l & 7) == 0) { f32x2 o = {mu, rsqrtf(sq * (1.f / D_) + 1e-5f)}; ((f32x2*)(ws + OFF_ST1))[row] = o; }
  }
  ST(1); ST_DRAIN(); ST(2);
  ST_FLUSH((u32*)(ws + (202u << 20)), (blockIdx.x >= 64 ? blockIdx.x - 64 : blockIdx.x + 288) * 4 + w);
}

constexpr int K1_UPLANE = 2304, K1_UCH = 4608, K1_TOFF = 4 * K1_UCH, K1_TCOPY = 4160, K1_TCH = 4 * K1_TCOPY;
constexpr int K1_YOFF = K1_TOFF + 4 * K1_TCH, K1_YBUF = 8192;
constexpr int K1_LDS = K1_YOFF + 8 * K1_YBUF;
constexpr int K1_GRID = 256;

#define HSTORE gstore16
extern "C" __global__ __launch_bounds__(512) void k1_conv(const u16* __restrict__ ssm, const u16* __restrict__ ssmD, const u16* __restrict__ wg,
                                                          const u16* __restrict__ wu, char* __restrict__ ws) {
  extern __shared__ __attribute__((aligned(128))) char lds[];
  ST_DECL; ST(0);
  asm volatile("" ::"s"(ssm), "s"(ssmD), "s"(wg), "s"(wu), "s"(ws));
  const int tid = threadIdx.x, l = tid & 63;
  const int w = __builtin_amdgcn_readfirstlane(tid >> 6);
  const int b = blockIdx.x;
  const ldsp LP = (ldsp)lds;
  const u32 L0 = (u32)(uintptr_t)LP;
  if (b >= 144) {
    for (int i = 0; i < SIDE_DELAY; ++i) __builtin_amdgcn_s_sleep(8);
    const int j = ((b - 144) >> 3) + 14 * w, jj = j * 2 + ((b >> 2) & 1), cbl = jj >> 3;
    if (j < 72) colsum_slice(wg, wu, (float*)(ws + OFF_CS), (cbl < 16 ? (b & 3) * 16 + cbl : 64 + (b & 3) * 2 + (cbl - 16)) * 8 + (jj & 7), l);
  } else {
    const u16* __restrict__ kvt = (const u16*)(ws + OFF_KVT);
    const u16* __restrict__ xp = (const u16*)(ws + OFF_XP);
    const f32x2* __restrict__ st1 = (const f32x2*)(ws + OFF_ST1);
    const int g = (b >> 3) < 16 ? (b & 7) * 16 + (b >> 3) : 128 + (b & 7) * 2 + ((b >> 3) - 16), ch = w & 3, hf_ = w >> 2;
    const int gch = g * 4 + ch;
    {
      const char* src = (const char*)(kvt + (size_t)gch * S_);
      const u32 vo = (2 * l + hf_) * 16;
      const u32 dst = L0 + ch * K1_UCH + hf_ * K1_UPLANE + 256;
      dma16(src, vo, dst);
      dma16(src + 2048, vo, dst + 1024);
    }
    const u16* krow = ssm + (size_t)gch * S_;
    u32x4 wv[2]; u32x2 wn[2];
  #pragma unroll
    for (int it = 0; it < 2; ++it) {
      const int idx = hf_ * 128 + it * 64 + l;
      wv[it] = *(const u32x4*)(krow + (idx > 0 ? idx - 1 : 0) * 8);
      wn[it] = *(const u32x2*)(krow + idx * 8);
    }
    const u32x4 wl = *(const u32x4*)(krow + opq(255 * 8));
    const u32 dv1 = ssmD[opq(gch)];
    SCHED0();
    u32x2 xr[4]; f32x2 st[4];
  #pragma unroll
    for (int i = 0; i < 4; ++i) { const int s = (tid >> 4) * 32 + (tid & 15) + (i & 1) * 16 + (i >> 1) * 1024; xr[i] = *(const u32x2*)(xp + ((size_t)g * S_ + s) * 4); st[i] = st1[s]; }
    SCHED0();
    const u32x4 z4 = {0u, 0u, 0u, 0u};
    if (l < 16) *(LAS u32x4*)(LP + (ch * K1_UCH + hf_ * K1_UPLANE + l * 16)) = z4;
    const ldsp tb = LP + (K1_TOFF + ch * K1_TCH);
    auto put = [&](int pos  , u32x4 a, u32x2 n) {
      const u32 o0 = __builtin_amdgcn_alignbit(a[1], a[0], 16), o1 = __builtin_amdgcn_alignbit(a[2], a[1], 16), o2 = __builtin_amdgcn_alignbit(a[3], a[2], 16),
                o3 = __builtin_amdgcn_alignbit(n[0], a[3], 16), o4 = __builtin_amdgcn_alignbit(n[1], n[0], 16);
      u32x4 c1 = {o0, o1, o2, o3}, c2 = {a[1], a[2], a[3], n[0]}, c3 = {o1, o2, o3, o4};
      *(LAS u32x4*)(tb + pos * 16) = a;
      *(LAS u32x4*)(tb + (K1_TCOPY + pos * 16)) = c1;
      *(LAS u32x4*)(tb + (2 * K1_TCOPY + pos * 16)) = c2;
      *(LAS u32x4*)(tb + (3 * K1_TCOPY + pos * 16)) = c3;
    };
    if (hf_ == 0 && l == 0) wv[0] = z4;
  #pragma unroll
    for (int it = 0; it < 2; ++it) put(hf_ * 128 + it * 64 + l + 1, wv[it], wn[it]);
    if (hf_ == 1 && l < 2) { u32x2 z2 = {0u, 0u}; put(l ? 257 : 0, l ? wl : z4, z2); }
    ST(1);
    wait_vm<8>();
    LDS_BARRIER();
    ST(2);
    const int a_ = l & 15, q_ = l >> 4;
    const int cpy = (1 + a_) & 3;
    const ldsp taddr = LP + (K1_TOFF + ch * K1_TCH + cpy * K1_TCOPY + 2 * (1 + a_ + 8 * q_ - cpy) + hf_ * 256);
    const ldsp uaddr = LP + (ch * K1_UCH + ((q_ + 1) & 1) * K1_UPLANE + (16 + a_ - (q_ >> 1) - 8 * hf_ - 6) * 16);
    f32x4 acc[8];
    u32x4 dg;
    {
      const int j = 15 - a_ - 8 * q_;
      const u32 dval = hf_ ? 0u : dv1;
  #pragma unroll
      for (int e = 0; e < 4; ++e) dg[e] = (j == 2 * e) ? dval : ((j == 2 * e + 1) ? (dval << 16) : 0u);
    }
    u32x4 U[2][8], T[2][8];
    auto ldU = [&](int e, int j) { return *(const LAS u32x4*)(uaddr + ((3 - e) * 32 + 256 * j)); };
    auto ldT = [&](int e, int m) {
      const u32x2 lo = *(volatile const LAS u32x2*)(taddr + (64 * e + 512 * m)), hi = *(volatile const LAS u32x2*)(taddr + (8 + 64 * e + 512 * m));
      u32x4 t = {lo[0], lo[1], hi[0], hi[1]};
      return t;
    };
#pragma unroll
    for (int j = 0; j < 8; ++j) U[0][j] = ldU(0, j);
#pragma unroll
    for (int j = 0; j < 8; ++j) T[0][j] = ldT(0, j);
    asm volatile("s_nop 1" : "+v"(dg));
    SCHED0();
#pragma unroll
    for (int n = 0; n < 8; ++n) mma<true>(acc[n], dg, U[0][n]);
    SCHED0();
    float xs[16], xn[16];
    static_for<0, 144>([&](auto ic) {
      constexpr int e = ic.value / 36, i = ic.value % 36;
      constexpr auto mn = [] { int k = 0; for (int m = 0; m < 8; ++m) for (int n = m; n < 8; ++n) { if (k == i) return m * 8 + n; ++k; } return 0; }();
      constexpr int m = mn >> 3, n = mn & 7;
      if constexpr (e < 3 && i < 32 && (i & 1) == 0) {
        if constexpr ((i & 2) == 0) U[(e + 1) & 1][i >> 2] = ldU(e + 1, i >> 2); else T[(e + 1) & 1][i >> 2] = ldT(e + 1, i >> 2);
      }
      mma<false>(acc[n], T[e & 1][m], U[e & 1][n - m]);
      if constexpr (e == 3 && i < 16) {
        constexpr int ti = i >> 2, c = i & 3;
        xs[i] = (c & 1) ? bfhi(xr[ti][c >> 1]) : bflo(xr[ti][c >> 1]);
        xn[i] = hide(xs[i] - st[ti][0]) * st[ti][1];
        pinf(xs[i]); pinf(xn[i]);
      }
      SCHED0();
    });
    MFMA_DRAIN(); SCHED0();
    ST(3);
    {
      const ldsp yb = LP + (K1_YOFF + (hf_ * 4 + ch) * K1_YBUF + ((4 * a_ + q_) ^ ((a_ >> 1) & 3) ^ ((a_ & 2) << 1)) * 16);
  #pragma unroll
      for (int n = 0; n < 8; ++n) *(LAS f32x4*)(yb + n * 1024) = acc[n];
    }
    LDS_BARRIER();
    ST(4);
    {
      float y[4][8];
#pragma unroll
      for (int i = 0; i < 4; ++i) {
        const int s = (tid >> 4) * 32 + (tid & 15) + (i & 1) * 16, sl = s >> 2;
        const ldsp yb = LP + (K1_YOFF + (sl ^ ((sl >> 3) & 3) ^ ((sl >> 1) & 4)) * 16 + (s & 3) * 4 + (i >> 1) * 4096);
#pragma unroll
        for (int c = 0; c < 8; ++c) y[i][c] = *(const LAS float*)(yb + c * K1_YBUF);
      }
#pragma unroll
      for (int j = 0; j < 2; ++j) {
        u32x4 o;
#pragma unroll
        for (int e = 0; e < 2; ++e) {
          const int i = 2 * j + e;
          float hv[4];
#pragma unroll
          for (int c = 0; c < 4; ++c) hv[c] = fmaf(xn[4 * i + c], hide(y[i][c] + y[i][4 + c]), xs[4 * i + c]);
          o[2 * e] = pack2(hv[0], hv[1]); o[2 * e + 1] = pack2(hv[2], hv[3]);
        }
        HSTORE(ws + OFF_HF + (size_t)j * 32 * 36864 + (g >> 3) * 2048 + (g & 1) * 1024 + ((g >> 1) & 3) * 256, (u32)((tid >> 4) * 36864 + (tid & 15) * 16), o);
      }
    }
    ST(5);
  }
  ST_DRAIN(); ST(6);
  ST_FLUSH((u32*)(ws + (203u << 20)), b * 8 + w);
}

constexpr int G1_QE = 147456;
constexpr int G1_QL = 14 * 8192;
constexpr int G1_CS = G1_QE + 8192;
constexpr int G1_CP = G1_CS + 2048;
constexpr int FF_LDS = G1_CP + 5120;
constexpr int PF_ISS0 = 86;
constexpr int STG = 18432, NST = 8;
constexpr int G1_D = 4, G1_DW = 6, HD = 5, HB = HD + 1;
constexpr int p1_ops(int x, bool even_only) { return (x + G1_DW < 18 ? 1 : 0) + (x + G1_D < 18 ? (even_only ? 1 : 2) : 0) + (x == 18 - G1_D ? 3 : 0); }
constexpr int p1_wait(int g) {
  int n = 0;
  if (g + 1 < G1_D) n += 3 * (G1_D - (g + 2)) + (G1_DW - G1_D);
  for (int x = (g + 2 - G1_D > 0 ? g + 2 - G1_D : 0); x < g; ++x) n += p1_ops(x, false);
  return n + p1_ops(g, true);
}
constexpr bool p1_bar(int g) { return g < G1_D - 1 || (g - (G1_D - 1)) % (G1_DW - G1_D + 1) == 0; }
constexpr float GC = -2.3022081983f;
constexpr int U2 = 216;
constexpr int g1_q_issue(int half, int r) { return 8 + 6 * r; }
constexpr int g1_b_issue(int half, int j) { return (j < 14 ? 29 + 3 * j : 73 + 3 * (j - 14)); }
constexpr int G1_E0 = 9;
constexpr int epi_start(int i) { return i < 8 ? G1_E0 + 26 * i : U2 + 2; }
static_assert(G1_E0 + 26 * 7 + 24 < 216, "epilogue stream");
constexpr bool epi_stores(int i) { return i == 1 || i == 3 || i == 4 || i == 6 || i == 8; }
constexpr int w_issue(int n, int k) {
  return n < 4 ? 191 + 4 * n + 2 * k : n < 6 ? 209 + 4 * (n - 4) + 2 * k : ((n & 1) || (G2_BAR_ALL && n > 6)) ? U2 + 9 * (n - 6) + 4 * k : U2 + 9 * (n - 6) + WE0 + WE1 * k;
}
constexpr int h_issue(int n, int k) { return n < 5 ? 192 + 4 * n + 2 * k : U2 + 9 * (n - (n < 10 ? HD : 4)) + 2 + 3 * k; }
constexpr int dq_issue(int r) { return r == 0 ? 183 : r < 4 ? U2 + 9 * (4 * r - 3) + 1 : U2 + 9 * 11 + 1; }
constexpr int poll_iss(int p) { return p == 0 ? 180 : p == 1 ? U2 + 9 * 2 + 3 : U2 + 9 * 7 + 3; }
constexpr int poll_chk(int p) { return p == 0 ? 191 : p == 1 ? U2 + 9 * 6 + 1 : U2 + 9 * 10 + 1; }
constexpr int RES_ISS = U2 + 9 * 16 + 7;
template <class F> constexpr int find_at(int n, int u, F f) { for (int i = 0; i < n; ++i) if (f(i) == u) return i; return -1; }
constexpr int cnt_ops(int half, int k0, int k1) {
  int n = 0;
  auto in = [&](int key) { return key > k0 && key < k1 ? 1 : 0; };
  for (int r = 0; r < 4; ++r) n += in(4 * g1_q_issue(half, r) + 2);
  for (int j = 0; j < 18; ++j) n += in(4 * g1_b_issue(half, j) + 2);
  for (int i = 0; i < 8; ++i) if (epi_stores(i)) n += in(4 * (epi_start(i) + 18)) + in(4 * (epi_start(i) + 24));
  n += in(4 * (U2 + 11)) + in(4 * (U2 + 14));
  for (int s = 0; s < 18; ++s) for (int k = 0; k < 2; ++k) n += in(4 * w_issue(s, k) + 2) + in(4 * h_issue(s, k) + 2);
  for (int p = 0; p < 3; ++p) n += in(4 * poll_iss(p) + 2);
  n += 3 * in(4 * RES_ISS + 2);
  for (int r = 0; r < 5; ++r) n += in(4 * (PF_ISS0 + 4 * r) + 2);
  for (int r = 0; r < 5; ++r) n += in(4 * dq_issue(r) + 2);
  return n;
}
constexpr int wait_n(int k0a, int k0b, int k1) { return cmin(cnt_ops(0, k0a, k1), cnt_ops(1, k0b, k1)); }
constexpr int wait_q(int r, int tw) { return wait_n(4 * g1_q_issue(0, r) + 2, 4 * g1_q_issue(1, r) + 2, 4 * tw + 1); }
constexpr int wait_b(int j, int tw) { return wait_n(4 * g1_b_issue(0, j) + 2, 4 * g1_b_issue(1, j) + 2, 4 * tw + 1); }
constexpr int wait_k(int k0, int k1) { return wait_n(k0, k0, k1); }
constexpr int cmax(int a, int b) { return a > b ? a : b; }
static_assert(w_issue(1, 1) > epi_start(6) + 24 && h_issue(0, 0) > poll_chk(0) && h_issue(10, 0) > poll_chk(1) && h_issue(14, 0) > poll_chk(2), "schedule");
constexpr int kk_of(int s) { return s < 8 ? 9 * ((s >> 1) & 1) + 4 * (s & 1) + (s >> 2) : s < 10 ? 9 * (s - 8) + 8 : 9 * (((s - 10) >> 1) & 1) + 4 * (s & 1) + 2 + ((s - 10) >> 2); }
struct Acc { f32x4 a[2][2]; };

extern "C" __global__ __launch_bounds__(512, 2) void ffn_fused(const u16* __restrict__ wg, const u16* __restrict__ wu, const u16* __restrict__ wd,
                                                               char* __restrict__ ws, u16* __restrict__ out) {
  extern __shared__ __attribute__((aligned(128))) char lds[];
  ST_DECL; ST(0);
  asm volatile("" ::"s"(wg), "s"(wu), "s"(wd), "s"(ws), "s"(out));
  const int tid = threadIdx.x, l = tid & 63;
  const int w = __builtin_amdgcn_readfirstlane(tid >> 6);
  const int b = blockIdx.x;
  const int fg = (b & 3) * 8 + ((b >> 3) & 7), tg = ((b >> 2) & 1) * 4 + (b >> 6);
  const int tt = tg * 8 + w;
  const ldsp LP = (ldsp)lds;
  const u32 L0 = (u32)(uintptr_t)LP;
  constexpr u32 RS = DF_ * 2, GS = 32 * RS;
  const int wblk = w & 1, whr = (w >> 1) & 1, whalf = w >> 2;
  const rsrc_t rW = mk_rsrc((w & 1) ? wu : wg, D_ * DF_ * 2), rS = mk_rsrc(ws, (u32)WS_USED), rD = mk_rsrc(wd, D_ * DF_ * 2);
  const u32 ab_voff = (u32)((16 * whalf + 8 * whr + (l >> 3)) * RS + ((((l & 7) >> 1) ^ (((l >> 4) & 1) | (whr << 1))) * 32) + (l & 1) * 16 + fg * 256);
  const u32 ab_dst = L0 + whalf * 4096 + wblk * 2048 + whr * 1024;
  auto mk_qvoff = [&](u32 l2) { return (u32)(((l2 >> 1) ^ ((l2 >> 2) & 4)) * RS + (l2 & 1) * 16 + (w >> 1) * GS + 8192 + fg * 32); };
  u32x4 h[2][18];
  const u32 lo16 = l * 16;
  const u32 h_voff = lo16 + (u32)OFF_HF + tt * 36864;
  auto issue_dma = [&](auto ic) { constexpr int g = ic.value; bdma<g * 8192>(rW, ab_voff, g * GS, ab_dst); };
  auto issue_h = [&](auto ic, auto tc) { constexpr int g = ic.value, tl = tc.value; bload<(g & 1) * 2048 + tl * 1024>(h[tl][g], rS, h_voff, (g >> 1) * 4096); };
  static_for<0, G1_D>([&](auto ic) { issue_dma(ic); issue_h(ic, IC<0>{}); issue_h(ic, IC<1>{}); });
  static_for<G1_D, G1_DW>(issue_dma);
  u32x4 sx[6];
  stat_loads(fg, sx, rS, h_voff, fg * 6144, fg * 6144 + 4096);
  ST(1);
  const u32 lw = L0 + w * 1024;
  const int rq = l >> 4, rr = (l >> 2) & 3, rp = l & 3;
  const u32 rb0 = L0 + (rq >> 1) * 4096 + (rq & 1) * 1024 + rr * 128 + rp * 8 + (((rr >> 1) | ((rq & 1) << 1)) * 32);
  auto abase = [&](int c, int off) { return opaque((ldsp)(uintptr_t)((opq(rb0) ^ (u32)(c * 32)) + (u32)off)); };
  Acc acc[4];
  const char* stb = ws + OFF_ST2;
  u32x4 sg4; u32x2 sg2;
  {
    float s1[2] = {0.f, 0.f}, s2[2] = {0.f, 0.f};
    ldsp pb[3];
    Frag pf[2][3];
    auto fetch1 = [&](auto ic, int c, int hi) {
      constexpr int s = ic.value;
      if constexpr ((s & 15) == 0) { if (hi == 0) pb[c] = abase(c, s * 4096); }
      constexpr int o = ((s & 15) >> 1) * 8192 + (s & 1) * 2048;
      if (hi) pf[s & 1][c].hi = tr_half(pb[c] + (o + 512)); else pf[s & 1][c].lo = tr_half(pb[c] + o);
    };
    wait_vm2<3 * (G1_D - 1) + G1_DW - G1_D>(h[0][0], h[1][0]);
    BARRIER();
    swap_halves(h[0][0], h[1][0]);
#pragma unroll
    for (int c = 0; c < 3; ++c) { fetch1(IC<0>{}, c, 0); fetch1(IC<0>{}, c, 1); }
    static_for<0, 36>([&](auto ic) {
      constexpr int s = ic.value, g = s >> 1, mt = s & 1;
      constexpr int SQ = 2 * (18 - G1_D);
      auto mm = [&](int c) {
#pragma unroll
        for (int tl = 0; tl < 2; ++tl) {
          mma<g == 0>(acc[c].a[mt][tl], frag_of(pf[s & 1][c]), h[tl][g]);
          SCHED0();
          if constexpr (s < 35) fetch1(IC<s + 1>{}, c, tl);
          SCHED0();
        }
      };
      if constexpr (mt == 1 && s < 35) {
        wait_vm2<p1_wait(g)>(h[0][g + 1], h[1][g + 1]);
        if constexpr (p1_bar(g)) BARRIER();
      }
      SCHED0();
      mm(0);
      if constexpr (mt == 0 && g + G1_DW < 18) issue_dma(IC<g + G1_DW>{});
      if constexpr (mt == 1 && g + G1_D < 18) issue_h(IC<g + G1_D>{}, IC<1>{});
      if constexpr (s == SQ) {
        const int ii = min(tid, 287), qd = ii / 72, r = ii - qd * 72;
        if (w < 5) dma16(ws + OFF_CS, (u32)(qd * 2304 + (r < 64 ? fg * 64 + r : 2048 + fg * 8 + (r - 64))) * 16u, L0 + G1_CP + w * 1024);
        bdma<G1_QE>(rW, mk_qvoff(l), 0, lw);
      }
      SCHED0();
      mm(1);
      if constexpr (g + G1_D < 18 && mt == 0) issue_h(IC<g + G1_D>{}, IC<0>{});
      if constexpr (s == SQ) { const u32 so = tt * 2048 + ((rq >> 1) * 16 + (l & 15)) * 64 + (rq & 1) * 24; ld_sc1_x4(sg4, so, stb); ld_sc1_x2<16>(sg2, so, stb); }
      if constexpr (mt == 1 && s < 35) swap_halves(h[0][g + 1], h[1][g + 1]);
      if constexpr (s >= 8 && s < 14) { if constexpr (s == 8) pin6(sx); stat_acc(fg, sx[s - 8], s1, s2); }
      SCHED0();
      mm(2);
      if constexpr (s == 14) {
        const float a0 = swap32_sum(swap16_sum(s1[0])), c0 = swap32_sum(swap16_sum(s2[0]));
        const float a1 = swap32_sum(swap16_sum(s1[1])), c1 = swap32_sum(swap16_sum(s2[1]));
        u32x2 gr = {__float_as_uint((l & 16) ? a1 : a0), __float_as_uint((l & 16) ? c1 : c0)};
        st_sc1_x2_if(fg, stb, (u32)(tt * 2048 + fg * 8 + (l & 31) * 64), gr);
      }
      SCHED0();
      if constexpr (s % 12 == 11) { ST(2 + s / 12); }
    });
  }
  STR(28);
  Frag wf[4];
  ldsp fb = nullptr, fb2 = nullptr;
  auto fetch = [&](auto ic, int hi) {
    constexpr int i = ic.value, p = i / 36, s = i % 36;
    if constexpr (p != 1) {
      constexpr int c = (p == 0) ? 3 : p - 2;
      constexpr int pos = (p == 0) ? (s + 28) % 36 : s;
      if constexpr (s == 0 || (pos & 15) == 0) { if (!hi) fb = abase(c, (pos & ~15) * 4096); }
      constexpr int o = ((pos & 15) >> 1) * 8192 + (pos & 1) * 2048;
      if (hi) wf[i % 3].hi = tr_half(fb + (o + 512)); else wf[i % 3].lo = tr_half(fb + o);
    } else {
      if constexpr (s == 0 || s == 8) {
        if (!hi) {
          const u32 l2 = opq(tid);
          const int qo = (l2 >> 4) & 1;
          const int qb = (8 * ((l2 >> 4) & 3) + ((l2 >> 2) & 3)) * 32 + (l2 & 3) * 8 + (s == 0 ? G1_QE : G1_QL);
          fb = opaque(LP + (qb + qo * 128)); fb2 = opaque(LP + (qb + (1 - qo) * 128));
        }
      }
      constexpr int o = (s < 8 ? s : s - 8) * 1024;
      if (hi) wf[i % 3].hi = tr_half(fb2 + o); else wf[i % 3].lo = tr_half(fb + o);
    }
  };
  fetch(IC<0>{}, 0); fetch(IC<0>{}, 1); fetch(IC<1>{}, 0); fetch(IC<1>{}, 1);
  SCHED0();
  wait0_x6(sg4, sg2);
#pragma unroll 1
  for (int it = 0; it < (1 << 16); ++it) {
    const bool bad = (sg4[0] & sg4[1]) == 0xffffffffu || (sg4[2] & sg4[3]) == 0xffffffffu || (sg2[0] & sg2[1]) == 0xffffffffu;
    if (!__any(bad)) break;
    __builtin_amdgcn_s_sleep(2);
    const u32 so = tt * 2048 + ((rq >> 1) * 16 + (l & 15)) * 64 + (rq & 1) * 24;
    ld_sc1_x4(sg4, so, stb); ld_sc1_x2<16>(sg2, so, stb);
    wait0_x6(sg4, sg2);
  }
  float nmu[2], k1[2], k2[2], kd[2];
  {
    const float t1 = swap16_sum(__uint_as_float(sg4[0]) + __uint_as_float(sg4[2]) + __uint_as_float(sg2[0]));
    const float t2 = swap16_sum(__uint_as_float(sg4[1]) + __uint_as_float(sg4[3]) + __uint_as_float(sg2[1]));
    const float mu = hide(t1 * (1.f / D_));
    const float var = fmaxf(t2 * (1.f / D_) - mu * mu, 0.f) + 1e-5f;
    const auto m2 = __builtin_amdgcn_permlane32_swap(__float_as_uint(-mu), __float_as_uint(-mu), false, false);
    const auto v2 = __builtin_amdgcn_permlane32_swap(__float_as_uint(var), __float_as_uint(var), false, false);
#pragma unroll
    for (int tl = 0; tl < 2; ++tl) {
      kd[tl] = __uint_as_float(v2[tl]);
      const float r = __builtin_amdgcn_rsqf(kd[tl]);
      nmu[tl] = __uint_as_float(m2[tl]);
      k1[tl] = hide(GC * r);
      k2[tl] = hide(k1[tl] * 0.044715f * r * r);
    }
  }
  char* hdst = ws + OFF_HH + ((size_t)tt * 288 + (fg >> 1) * 9 + (fg & 1) * 4) * 1024;
  const ldsp csb = opaque(LP + (G1_CS + rq * 32));
  f32x4 cc;
  auto ldcs = [&](int lc, int hfx) { cc = *(const LAS f32x4*)(csb + (lc * 128 + hfx * 16)); };
  u32x4 eo[2];
  u32x2 eq[2];
  float m0, m1, tu0, tu1, y0, y1;
  auto emit = [&](int lc, int i, u32 pk) {
    const int tl = i & 1, hx = i >> 1;
    if (lc == 8) {
      eq[tl][hx] = pk;
      if (hx) st_sc1_x2(hdst + (size_t)tl * 147456 + (8 - (fg & 1) * 4) * 1024 + (fg & 1) * 8, lo16, eq[tl]);
    } else {
      eo[tl][(lc & 1) * 2 + hx] = pk;
      if ((lc & 1) && hx) st_sc1_x4(hdst + (size_t)tl * 147456 + (lc >> 1) * 1024, lo16, eo[tl]);
    }
  };
  auto epi = [&](const Acc& a, int lc, int st, int nlc, int part) {
    const int i = st / 6, sg = st % 6, tl = i & 1, e = (i >> 1) * 2;
    if (sg == 0) {
      if (part == 0) { if (i > 0) { u32 pk = pack2(tu0, tu1 * y1); pin(pk); emit(lc, i - 1, pk); } }
      else if (i < 4) { m0 = fmaf(nmu[tl], cc[0], a.a[0][tl][e]); m1 = fmaf(nmu[tl], cc[1], a.a[0][tl][e + 1]); pinf(m0); pinf(m1); }
    } else if (sg == 1) {
      if (part == 0) { tu0 = fmaf(nmu[tl], cc[2], a.a[1][tl][e]); tu1 = fmaf(nmu[tl], cc[3], a.a[1][tl][e + 1]); pinf(tu0); pinf(tu1); }
      else {
        y0 = hide(m0 * m0); y1 = m1 * m1; pinf(y0); pinf(y1);
        if (i == 1) ldcs(lc, 1);
        if (i == 3 && nlc >= 0) ldcs(nlc, 0);
      }
    } else if (sg == 2) {
      if (part == 0) { tu0 = hide(tu0 * m0); tu1 = tu1 * m1; pinf(tu0); pinf(tu1); }
      else { y0 = hide(fmaf(y0, k2[tl], k1[tl])); y1 = fmaf(y1, k2[tl], k1[tl]); pinf(y0); pinf(y1); }
    } else if (sg == 3) {
      if (part == 0) { y0 = hide(y0 * m0); y1 = y1 * m1; pinf(y0); pinf(y1); }
      else { y0 = __builtin_amdgcn_exp2f(y0); pinf(y0); }
    } else if (sg == 4) {
      if (part == 0) { y1 = __builtin_amdgcn_exp2f(y1); pinf(y1); }
      else { y0 = fmaf(y0, kd[tl], kd[tl]); pinf(y0); }
    } else {
      if (part == 0) { y0 = __builtin_amdgcn_rcpf(y0); y1 = fmaf(y1, kd[tl], kd[tl]); pinf(y0); pinf(y1); }
      else { y1 = __builtin_amdgcn_rcpf(y1); tu0 = tu0 * y0; pinf(y1); pinf(tu0); }
    }
  };
  const int r5 = b >> 3;
  const int ks = b & 3, tg2 = ((b >> 2) & 1) * 8 + (r5 & 7), dt = r5 >> 3;
  const int tq = w & 3, kh = w >> 2;
  constexpr u32 RD = D_ * 2;
  const u32 d_dst = L0 + kh * 9216 + tq * 2048;
  const int mb = ks * 4 + kh * 2;
  const u32 rowA = mb * 256 * RD, rowQ = (4096 + mb * 32) * RD;
  auto row0 = [&](int kk) { const int q = kk / 9, i = kk - 9 * q; return i < 8 ? rowA + (q * 256 + i * 32) * RD : rowQ + q * 32 * RD; };
  u32x4 hb[HB][2];
  u32 hv0 = 0, hv1 = 0;
  const char* myflag = ws + OFF_FLG + 16384 + b * 64;
  const char* pflags = ws + OFF_FLG + 16384 + ((b & 7) + kh * 32 + ((r5 & 7) >> 1) * 64) * 64;
  u32 d_voff = 0, dq_voff = 0, p_voff = 0, pv = 0, q_voff = 0;
  f32x4 ac2[9][2];
  ldsp pb[5];
  u32 rb2 = 0, rbq = 0;
  auto fetch2 = [&](auto ic, int hi) {
    constexpr int i = ic.value, s = i / 9, c = i % 9, stg = s % NST;
    if constexpr (stg % 3 == 0 && c < 4) { if (!hi) pb[c] = opaque((ldsp)(uintptr_t)((opq(rb2) ^ (u32)(c * 32)) + (u32)(stg * STG))); }
    if constexpr (c == 8 && (stg % 3 == 0 || s == 4)) { if (!hi) pb[4] = opaque((ldsp)(uintptr_t)(opq(rbq) + (s < 4 ? (u32)(G1_QE - 8192) - kh * (9216 - 1024) : (u32)((stg - stg % 3) * STG)))); }
    constexpr int o = c < 8 ? (stg % 3) * STG + (c >> 2) * 1024 : (s < 4 ? s * 2048 : (stg % 3) * STG);
    if (hi) wf[i & 3].hi = tr_half(pb[c < 8 ? (c & 3) : 4] + (o + (c < 8 ? 4096 : 512))); else wf[i & 3].lo = tr_half(pb[c < 8 ? (c & 3) : 4] + o);
  };
  auto issue2 = [&](auto uc) {
    constexpr int u = uc.value;
    constexpr int nw0 = find_at(18, u, [](int n) { return w_issue(n, 0); }), nw1 = find_at(18, u, [](int n) { return w_issue(n, 1); });
    constexpr int nh0 = find_at(18, u, [](int n) { return h_issue(n, 0); }), nh1 = find_at(18, u, [](int n) { return h_issue(n, 1); });
    constexpr int r = find_at(5, u, [](int n) { return dq_issue(n); });
    constexpr int pi = find_at(3, u, [](int n) { return poll_iss(n); }), pc = find_at(3, u, [](int n) { return poll_chk(n); });
    if constexpr (nw0 >= 0) bdma<(nw0 % NST) * STG>(rD, d_voff, row0(kk_of(nw0)), d_dst);
    if constexpr (nw1 >= 0) bdma<(nw1 % NST) * STG + 1024>(rD, d_voff, row0(kk_of(nw1)) + 128, d_dst);
    if constexpr (nh0 >= 0) HLOAD<(kk_of(nh0) & 3) * 1024>(hb[nh0 % HB][0], rS, hv0, (kk_of(nh0) >> 2) * 4096);
    if constexpr (nh1 >= 0) HLOAD<(kk_of(nh1) & 3) * 1024>(hb[nh1 % HB][1], rS, hv1, (kk_of(nh1) >> 2) * 4096);
    if constexpr (r >= 0) {
      const int tsel = r == 4 ? (tq & 1) : tq;
      const u32 ra = row0(kk_of(4 * r)), rb = row0(kk_of(4 * r + 1)), rc_ = row0(kk_of(r == 4 ? 16 : 4 * r + 2)), rd = row0(kk_of(r == 4 ? 17 : 4 * r + 3));
      const u32 rsel = (tsel & 2) ? ((tsel & 1) ? rd : rc_) : ((tsel & 1) ? rb : ra);
      bdma<0>(rD, dq_voff, rsel, r == 0 ? L0 + G1_QE + (tq * 2 + kh) * 1024 : L0 + (((4 * r) % NST) + tsel) * STG + kh * 9216 + 8192);
    }
    if constexpr (pi >= 0) poll_issue(pv, p_voff, pflags);
    if constexpr (pc >= 0) poll_check<wait_k(4 * poll_iss(pc) + 2, 4 * u + 2), pc + 1>(pv, p_voff, pflags);
  };
  static_for<0, 216>([&](auto ic) {
    constexpr int t = ic.value, p = t / 36, s = t % 36;
    constexpr int slot = (p + 3) & 3;
    constexpr int hg = (p == 0) ? ((s >> 1) + 14) % 18 : (s >> 1);
    constexpr int ci = (t - G1_E0) / 26, st = (t - G1_E0) % 26;
    constexpr int lc = ci < 4 ? ci : (ci == 4 ? 8 : ci - 1), nlc = ci < 3 ? ci + 1 : (ci == 3 ? 8 : ci);
#pragma unroll
    for (int tl = 0; tl < 2; ++tl) {
      mma<(s < 2)>(acc[slot].a[s & 1][tl], frag_of(wf[t % 3]), h[tl][hg]);
      SCHED0();
      if constexpr (t + 2 < 216) fetch(IC<t + 2>{}, tl);
      SCHED0();
      if constexpr (t >= G1_E0 && ci < 8 && st <= 24) epi(acc[ci & 3], lc, st, nlc, tl);
      SCHED0();
    }
    if constexpr (t == 2) {
      const u32 pi = min(opq(tid), 71u);
      const ldsp cp = LP + (G1_CP + pi * 16);
      const f32x4 r = (*(const LAS f32x4*)cp + *(const LAS f32x4*)(cp + 1152)) + (*(const LAS f32x4*)(cp + 2304) + *(const LAS f32x4*)(cp + 3456));
      *(LAS f32x4*)(LP + (G1_CS + pi * 16)) = r;
    }
    if constexpr (t == G1_E0 - 1) ldcs(0, 0);
    if constexpr (t == 1 || t == 7 || t == 28 || t == 72 || t == 190) { SCHED0(); BARRIER(); }
    if constexpr (t == 41) { wait_vm<wait_q(1, 41)>(); BARRIER(); }
    if constexpr (t == 57) { wait_vm<wait_q(3, 57)>(); BARRIER(); }
    if constexpr (t == 69) { wait_vm<wait_b(9, 69)>(); BARRIER(); }
    if constexpr (t == 89) { wait_vm<wait_b(14, 89)>(); BARRIER(); }
    if constexpr (t == 99) { wait_vm<wait_b(17, 99)>(); BARRIER(); }
    if constexpr (t == 160) { wait_vm<wait_k(4 * (epi_start(4) + 24), 4 * t + 1)>(); BARRIER(); flag_store(w, opq(0u), opq(1u), myflag); }
    if constexpr (t == 198) { SCHED0(); BARRIER(); }
    if constexpr (t == 208) { SCHED0(); wait_vm<wait_k(4 * w_issue(1, 1) + 2, 4 * t + 1)>(); BARRIER(); flag_store(w, opq(0u), opq(2u), myflag); }
    if constexpr (t == 7) q_voff = mk_qvoff(opq(tid) & 63);
    if constexpr (t == 179) {
      const u32 l2 = opq(tid) & 63;
      d_voff = (u32)((8 * tq + (l2 >> 3)) * RD + ((((l2 & 7) >> 1) ^ ((l2 >> 4) & 3)) * 32) + (l2 & 1) * 16 + dt * 256);
      dq_voff = (u32)((l2 >> 1) * RD + (l2 & 1) * 16 + 1024 + dt * 32);
      p_voff = (l2 & 3) * 512;
      hv0 = l2 * 16 + (u32)OFF_HH + ((tg2 * 8 + tq * 2 + kh) * 144 + mb * 9) * 1024;
      hv1 = l2 * 16 + (u32)OFF_HH + ((tg2 * 8 + tq * 2 + (kh ^ 1)) * 144 + mb * 9) * 1024;
      pin(d_voff); pin(dq_voff); pin(p_voff); pin(hv0); pin(hv1);
    }
    {
      constexpr int r = find_at(4, t, [](int n) { return g1_q_issue(0, n); }), j = find_at(18, t, [](int n) { return g1_b_issue(0, n); });
      if constexpr (r >= 0) bdma<G1_QL + r * 8192>(rW, q_voff, (4 + 4 * r) * GS, lw);
      if constexpr (j >= 0) bdma<j * 8192>(rW, ab_voff, j * GS + 128, ab_dst);
    }
    if constexpr (t >= PF_ISS0 && t < PF_ISS0 + 20 && (t - PF_ISS0) % 4 == 0) {
      const int piece = ((b >> 3) * 5 + (t - PF_ISS0) / 4) * 8 + w;
      const int byte = piece * 1024;
      bdma<G1_QE>(rD, lo16, (u32)(byte < 1024 * 1152 ? (b & 3) * 1024 * 1152 + byte : 4096 * 1152 + (b & 3) * 128 * 1152 + (byte - 1024 * 1152)), lw);
    }
    if constexpr (t >= 180) issue2(ic);
    SCHED0();
    if constexpr (t % 24 == 23) { ST(5 + t / 24); }
  });
  STR(29);
  {
    const u32 l2 = opq(tid) & 63;
    const u32 q2 = l2 >> 4, r2_ = (l2 >> 2) & 3, p2 = l2 & 3;
    rb2 = L0 + kh * 9216 + (q2 >> 1) * 2048 + (4 * (q2 & 1) + r2_) * 128 + ((2 * (q2 & 1) + (r2_ >> 1)) * 32) + p2 * 8;
    rbq = L0 + kh * 9216 + 8192 + (4 * q2 + r2_) * 32 + p2 * 8;
  }
  const int ft = tg2 * 8 + ks * 2 + kh;
  const int dcol = dt * 128 + tq * 32 + 4 * rq;
  const int qcol = 512 + dt * 16 + 4 * rq;
  u32x2 r0, r1, r2;
  static_for<0, 3>([&](auto ic) { fetch2(ic, 0); fetch2(ic, 1); });
  wait_vm2<wait_k(4 * h_issue(0, 1) + 2, 4 * U2 - 1)>(hb[0][0], hb[0][1]);
  static_for<0, 162>([&](auto ic) {
    constexpr int i = ic.value, s = i / 9, c = i % 9, u = U2 + i;
    if constexpr (c == ((s & 1) ? 6 : 4) && s < 17) {
      constexpr int s2 = cmin(s + 2, 17);
      constexpr int tgt = (s & 1) ? 4 * h_issue(s + 1, 1) + 2
                                  : cmax(cmax(cmax(4 * h_issue(s + 1, 1) + 2, 4 * w_issue(s2, 1) + 2), 4 * dq_issue((s + 1) >> 2) + 2), s == 4 ? 4 * (U2 + 14) : 0);
      wait_vm2<wait_k(tgt, 4 * u - 1)>(hb[(s + 1) % HB][0], hb[(s + 1) % HB][1]);
      if constexpr ((s & 1) == 0 || G2_BAR_ALL) BARRIER();
      if constexpr (s == 4) flag_store(w, opq(0u), opq(3u), myflag);
    }
    SCHED0();
#pragma unroll
    for (int tl = 0; tl < 2; ++tl) {
      mma<s == 0>(ac2[c][tl], frag_of(wf[i & 3]), hb[s % HB][tl]);
      SCHED0();
      if constexpr (i + 3 < 162) fetch2(IC<i + 3>{}, tl);
      SCHED0();
      if constexpr (i >= 2 && i <= 14) { if (i < 14 || tl == 0) { epi(acc[0], 7, 2 * (i - 2) + tl, -1, 0); epi(acc[0], 7, 2 * (i - 2) + tl, -1, 1); } }
      SCHED0();
    }
    issue2(IC<u>{});
    if constexpr (u == RES_ISS) {
      const char* hb_ = ws + OFF_HF + (size_t)(ft >> 1) * 36864 + (ft & 1) * 8;
      auto po = [&](int d) { return (u32)((d >> 5) * 2048 + ((d >> 2) & 1) * 1024 + ((d >> 3) & 3) * 256 + (l & 15) * 16); };
      gload8(r0, po(dcol), hb_); gload8(r1, po(dcol + 16), hb_); gload8(r2, po(qcol), hb_);
    }
    SCHED0();
    if constexpr (c == 8 && s % 3 == 2) { ST(14 + s / 3); }
  });
  MFMA_DRAIN(); SCHED0();
  {
    const ldsp xw = LP + (2 * STG + w * 9216 + l * 16);
#pragma unroll
    for (int c = 0; c < 9; ++c) *(LAS f32x4*)(xw + c * 1024) = ac2[c][1];
  }
  LDS_BARRIER();
  f32x4 sm[9];
  {
    const ldsp xr = LP + (2 * STG + (w ^ 4) * 9216 + l * 16);
#pragma unroll
    for (int c = 0; c < 9; ++c) sm[c] = ac2[c][0] + *(const LAS f32x4*)(xr + c * 1024);
  }
  ST(20);
  char* part = ws + OFF_PART;
  if (tq != ks) {
    char* pdst = part + ((((size_t)(tg2 * 4 + dt) * 4 + tq) * 4 + ks) * 2 + kh) * 4608;
#pragma unroll
    for (int pc = 0; pc < 4; ++pc) {
      u32x4 v = {pack2(sm[2 * pc][0], sm[2 * pc][1]), pack2(sm[2 * pc][2], sm[2 * pc][3]), pack2(sm[2 * pc + 1][0], sm[2 * pc + 1][1]), pack2(sm[2 * pc + 1][2], sm[2 * pc + 1][3])};
      st_sc1_x4(pdst + pc * 1024, lo16, v);
    }
    u32x2 v2 = {pack2(sm[8][0], sm[8][1]), pack2(sm[8][2], sm[8][3])};
    st_sc1_x2(pdst + 4096, l * 8, v2);
  } else {
    const ldsp ow = LP + (kh * 9216 + l * 16);
#pragma unroll
    for (int c = 0; c < 9; ++c) *(LAS f32x4*)(ow + c * 1024) = sm[c];
  }
  asm volatile("s_waitcnt vmcnt(0)" : "+v"(r0), "+v"(r1), "+v"(r2)::"memory");
  LDS_BARRIER();
  ST(21);
  const char* fl = ws + OFF_FLG + (tg2 * 4 + dt) * 256;
  flag_store(w, opq(0u), opq(1u), fl + ks * 64);
  const ldsp orp = LP + (kh * 9216 + l * 16);
  f32x4 o0 = *(const LAS f32x4*)(orp + tq * 2048), o1 = *(const LAS f32x4*)(orp + (tq * 2048 + 1024)), o2 = *(const LAS f32x4*)(orp + 8192);
  o0[0] += bflo(r0[0]); o0[1] += bfhi(r0[0]); o0[2] += bflo(r0[1]); o0[3] += bfhi(r0[1]);
  o1[0] += bflo(r1[0]); o1[1] += bfhi(r1[0]); o1[2] += bflo(r1[1]); o1[3] += bfhi(r1[1]);
  o2[0] += bflo(r2[0]); o2[1] += bfhi(r2[0]); o2[2] += bflo(r2[1]); o2[3] += bfhi(r2[1]);
  {
    const u32 fo = (l & 3) * 64;
    poll_issue(pv, fo, fl);
    poll_check<0, 1>(pv, fo, fl);
  }
  ST(22);
  {
    const char* psrc = part + (((size_t)(tg2 * 4 + dt) * 4 + ks) * 4 * 2 + kh) * 4608;
    u32x4 v[3]; u32x2 vq[3];
#pragma unroll
    for (int j = 0; j < 3; ++j) {
      const int src = (ks + 1 + j) & 3;
      ld_sc1_x4(v[j], lo16 + tq * 1024, psrc + src * 9216);
      ld_sc1_x2<0>(vq[j], l * 8 + 4096, psrc + src * 9216);
    }
    asm volatile("s_waitcnt vmcnt(0)" : "+v"(v[0]), "+v"(v[1]), "+v"(v[2]), "+v"(vq[0]), "+v"(vq[1]), "+v"(vq[2])::"memory");
#pragma unroll
    for (int j = 0; j < 3; ++j) {
      o0[0] += bflo(v[j][0]); o0[1] += bfhi(v[j][0]); o0[2] += bflo(v[j][1]); o0[3] += bfhi(v[j][1]);
      o1[0] += bflo(v[j][2]); o1[1] += bfhi(v[j][2]); o1[2] += bflo(v[j][3]); o1[3] += bfhi(v[j][3]);
      o2[0] += bflo(vq[j][0]); o2[1] += bfhi(vq[j][0]); o2[2] += bflo(vq[j][1]); o2[3] += bfhi(vq[j][1]);
    }
    u16* op = out + (size_t)(ft * 16 + (l & 15)) * D_;
    u32x2 oa = {pack2(o0[0], o0[1]), pack2(o0[2], o0[3])}, ob = {pack2(o1[0], o1[1]), pack2(o1[2], o1[3])}, oc = {pack2(o2[0], o2[1]), pack2(o2[2], o2[3])};
    *(u32x2*)(op + dcol) = oa; *(u32x2*)(op + dcol + 16) = ob;
    if (tq == 0) *(u32x2*)(op + qcol) = oc;
  }
  ST(23); ST_DRAIN(); ST(24);
  ST_FLUSH((u32*)(ws + (200u << 20)), b * 8 + w);
}

extern "C" void kernel_launch(void* const* d_in, const int* in_sizes, int n_in, void* d_out, int out_size, void* d_ws, size_t ws_size,
                              hipStream_t stream) {
  const u16* q = (const u16*)d_in[0]; const u16* k = (const u16*)d_in[1]; const u16* v = (const u16*)d_in[2];
  const u16* shk = (const u16*)d_in[3]; const u16* ssm = (const u16*)d_in[4]; const u16* ssmD = (const u16*)d_in[5];
  const u16* wg = (const u16*)d_in[7]; const u16* wu = (const u16*)d_in[8]; const u16* wd = (const u16*)d_in[9];
  char* ws = (char*)d_ws;
  (void)hipFuncSetAttribute((const void*)k1_conv, hipFuncAttributeMaxDynamicSharedMemorySize, K1_LDS);
  (void)hipFuncSetAttribute((const void*)ffn_fused, hipFuncAttributeMaxDynamicSharedMemorySize, FF_LDS);
  k0_prep<<<K0_GRID, 256, 0, stream>>>(q, k, v, shk, ssm, ws);
  k1_conv<<<K1_GRID, 512, K1_LDS, stream>>>(ssm, ssmD, wg, wu, ws);
  ffn_fused<<<256, 512, FF_LDS, stream>>>(wg, wu, wd, ws, (u16*)d_out);
}
```

```cpp
#include <hip/hip_runtime.h>
#include <hip/hip_bf16.h>
#include <stdint.h>
#include <utility>
#define G2_BAR_ALL 0
#define WE0 7
#define WE1 1
#define HD2 4
#define LASTX 2
#define HH_PLAIN 0
#define HLOAD bload_sc1
#define NT0 3
#define LDK(p) ((NT0 & 1) ? __builtin_nontemporal_load(p) : *(p))
#define LDV(p) ((NT0 & 2) ? __builtin_nontemporal_load(p) : *(p))
#define LDQ(p) ((NT0 & 4) ? __builtin_nontemporal_load(p) : *(p))
#define SIDE_DELAY 4

typedef unsigned short u16;
typedef unsigned int u32;
typedef __attribute__((ext_vector_type(2))) unsigned u32x2;
typedef __attribute__((ext_vector_type(4))) unsigned u32x4;
typedef __attribute__((ext_vector_type(2))) float f32x2;
typedef __attribute__((ext_vector_type(4))) float f32x4;
typedef __attribute__((ext_vector_type(8))) __bf16 bf16x8;
typedef __attribute__((ext_vector_type(2))) __bf16 bf16x2;
typedef __attribute__((ext_vector_type(4))) short s16x4;
#define GAS __attribute__((address_space(1)))
#define LAS __attribute__((address_space(3)))
#define DI __device__ __forceinline__
typedef LAS char* ldsp;

constexpr int S_ = 2048, D_ = 576, DF_ = 4608;
constexpr size_t OFF_KVT = 0;
constexpr size_t OFF_XP  = OFF_KVT + (size_t)D_ * S_ * 2;
constexpr size_t OFF_HF  = OFF_XP + (size_t)D_ * S_ * 2;
constexpr size_t OFF_ST1 = OFF_HF + (size_t)D_ * S_ * 2;
constexpr size_t OFF_CS  = OFF_ST1 + 2048 * 8;
constexpr size_t OFF_FLG = OFF_CS + 4 * 2 * 4608 * 4;
constexpr size_t OFF_ST2 = OFF_FLG + 36864;
constexpr size_t OFF_HH  = OFF_ST2 + 131072;
constexpr size_t OFF_PART = OFF_HH + (size_t)S_ * DF_ * 2;
constexpr size_t WS_USED = OFF_PART + 64 * 4 * 4 * 2 * 4608;

DI float bflo(u32 w) { return __uint_as_float(w << 16); }
DI float bfhi(u32 w) { return __uint_as_float(w & 0xffff0000u); }
DI u32 pack2(float a, float b) { bf16x2 p; p[0] = (__bf16)a; p[1] = (__bf16)b; return __builtin_bit_cast(u32, p); }

template <int I> struct IC { static constexpr int value = I; };
template <int... I> struct Seq {};
template <int B, class F, int... I> DI void static_for_impl(F&& f, std::integer_sequence<int, I...>) { (f(IC<B + I>{}), ...); }
template <int B, int E, class F> DI void static_for(F&& f) { static_for_impl<B>(f, std::make_integer_sequence<int, E - B>{}); }
template <int N> DI void wait_vm() { asm volatile("s_waitcnt vmcnt(%0)" ::"n"(N) : "memory"); }
#define BARRIER() do { asm volatile("" ::: "memory"); __builtin_amdgcn_s_barrier(); asm volatile("" ::: "memory"); } while (0)
#define LDS_BARRIER() do { asm volatile("s_waitcnt lgkmcnt(0)" ::: "memory"); __builtin_amdgcn_s_barrier(); asm volatile("" ::: "memory"); } while (0)
#define SCHED0() __builtin_amdgcn_sched_barrier(0)
constexpr int cmin(int a, int b) { return a < b ? a : b; }

DI u32x4 tr_frag(ldsp a0, ldsp a1) {
  const s16x4 lo = __builtin_amdgcn_ds_read_tr16_b64_v4i16((LAS s16x4*)a0);
  const s16x4 hi = __builtin_amdgcn_ds_read_tr16_b64_v4i16((LAS s16x4*)a1);
  return __builtin_bit_cast(u32x4, __builtin_shufflevector(lo, hi, 0, 1, 2, 3, 4, 5, 6, 7));
}
struct Frag { s16x4 lo, hi; };
DI s16x4 tr_half(ldsp a) { return __builtin_amdgcn_ds_read_tr16_b64_v4i16((LAS s16x4*)a); }
DI u32x4 frag_of(const Frag& f) { return __builtin_bit_cast(u32x4, __builtin_shufflevector(f.lo, f.hi, 0, 1, 2, 3, 4, 5, 6, 7)); }
DI f32x4 mfma16(const u32x4& a, const u32x4& b, const f32x4& c) {
  return __builtin_amdgcn_mfma_f32_16x16x32_bf16(__builtin_bit_cast(bf16x8, a), __builtin_bit_cast(bf16x8, b), c, 0, 0, 0);
}
template <bool FIRST> DI void mma(f32x4& c, const u32x4& a, const u32x4& b) {
  if constexpr (FIRST) asm volatile("v_mfma_f32_16x16x32_bf16 %0, %1, %2, 0" : "=&v"(c) : "v"(a), "v"(b));
  else asm volatile("v_mfma_f32_16x16x32_bf16 %0, %1, %2, %0" : "+v"(c) : "v"(a), "v"(b));
}
#define MFMA_DRAIN() asm volatile("s_nop 15" ::: "memory")
template <int OFF> DI void gload16_sc1(u32x4& dst, u32 voff, const char* sbase) {
  asm volatile("global_load_dwordx4 %0, %1, %2 offset:%3 sc1" : "=v"(dst) : "v"(voff), "s"(sbase), "n"(OFF) : "memory");
}
DI void poll_issue(u32& f, u32 voff, const char* sb) { asm volatile("global_load_dword %0, %1, %2 sc1" : "=v"(f) : "v"(voff), "s"(sb) : "memory"); }
template <int N, int PH> DI void poll_check(u32& f, u32 voff, const char* sb) {
  u32 cnt = 1u << 18;
  asm volatile("s_waitcnt vmcnt(%4)\n"
               "1:\n\tv_cmp_gt_u32_e32 vcc, %5, %0\n\ts_nop 4\n\ts_cbranch_vccz 2f\n\t"
               "s_sleep 1\n\tglobal_load_dword %0, %2, %3 sc1\n\ts_waitcnt vmcnt(0)\n\t"
               "s_sub_u32 %1, %1, 1\n\ts_cbranch_scc0 1b\n"
               "2:" : "+v"(f), "+s"(cnt) : "v"(voff), "s"(sb), "n"(N), "n"(PH) : "memory", "vcc", "scc");
}
DI void poll4(u32 voff, const char* sb) {
  u32 cnt = 1u << 18;
  u32x4 f; u32 m;
  asm volatile("1:\n\tglobal_load_dwordx4 %0, %3, %4 sc1\n\ts_waitcnt vmcnt(0)\n\t"
               "v_min3_u32 %1, v100, v101, v102\n\tv_min_u32_e32 %1, %1, v103\n\tv_cmp_eq_u32_e32 vcc, 0, %1\n\ts_nop 4\n\ts_cbranch_vccz 2f\n\t"
               "s_sleep 4\n\ts_sub_u32 %2, %2, 1\n\ts_cbranch_scc0 1b\n"
               "2:" : "=&{v[100:103]}"(f), "=&v"(m), "+s"(cnt) : "v"(voff), "s"(sb) : "memory", "vcc", "scc");
}
DI void swap_halves(u32x4& x, u32x4& y) {
  u32 a = x[2], b = x[3], c = y[0], d = y[1];
  asm volatile("v_swap_b32 %0, %2\n\tv_swap_b32 %1, %3" : "+v"(a), "+v"(b), "+v"(c), "+v"(d));
  x[2] = a; x[3] = b; y[0] = c; y[1] = d;
}
DI void flag_store(int w, u32 zero_off, u32 val, const char* sb) {
  asm volatile("s_cmp_lg_u32 %3, 0\n\ts_cbranch_scc1 1f\n\tglobal_store_dword %0, %1, %2 sc1\n1:" ::"v"(zero_off), "v"(val), "s"(sb), "s"(w) : "memory", "scc");
}
template <int OFF> DI void gload16(u32x4& dst, u32 voff, const char* sbase) {
  asm volatile("global_load_dwordx4 %0, %1, %2 offset:%3" : "=v"(dst) : "v"(voff), "s"(sbase), "n"(OFF) : "memory");
}
DI void gload8(u32x2& dst, u32 voff, const char* sbase) { asm volatile("global_load_dwordx2 %0, %1, %2" : "=v"(dst) : "v"(voff), "s"(sbase) : "memory"); }
template <int N, int VAL> DI void waitbar_if(int sel, u32x4& a, u32x4& b) {
  asm volatile("s_cmp_lg_u32 %2, %4\n\ts_cbranch_scc1 1f\n\ts_waitcnt vmcnt(%3)\n\ts_barrier\n1:" : "+v"(a), "+v"(b) : "s"(sel), "n"(N), "n"(VAL) : "memory", "scc");
}
template <int N> DI void wait_vm2(u32x4& a, u32x4& b) { asm volatile("s_waitcnt vmcnt(%2)" : "+v"(a), "+v"(b) : "n"(N) : "memory"); }
DI void dma16(const char* sbase, u32 voff, u32 lds_dst) {
  asm volatile("s_mov_b32 m0, %2\n\ts_nop 0\n\tglobal_load_lds_dwordx4 %0, %1" ::"v"(voff), "s"(sbase), "s"(lds_dst) : "memory");
}
template <int VAL> DI void dma16_if(int sel, const char* sbase, u32 voff, u32 lds_dst) {
  asm volatile("s_cmp_eq_u32 %3, %4\n\ts_mov_b32 m0, %2\n\ts_cselect_b64 exec, -1, 0\n\tglobal_load_lds_dwordx4 %0, %1\n\ts_mov_b64 exec, -1" ::"v"(voff), "s"(sbase), "s"(lds_dst), "s"(sel), "n"(VAL) : "memory", "scc");
}
typedef __amdgpu_buffer_rsrc_t rsrc_t;
DI rsrc_t mk_rsrc(const void* p, u32 bytes) { return __builtin_amdgcn_make_buffer_rsrc((void*)p, (short)0, (int)bytes, 0x00020000); }
template <int LOFF> DI void bdma(rsrc_t r, u32 voff, u32 soff, u32 ldsb) {
  asm volatile("s_add_u32 m0, %3, %4\n\ts_nop 0\n\tbuffer_load_dwordx4 %0, %1, %2 offen lds" ::"v"(voff), "s"(r), "s"(soff), "s"(ldsb), "n"(LOFF) : "memory", "scc");
}
template <int OFF> DI void bload(u32x4& d, rsrc_t r, u32 voff, u32 soff) {
  asm volatile("buffer_load_dwordx4 %0, %1, %2, %3 offen offset:%4" : "=v"(d) : "v"(voff), "s"(r), "s"(soff), "n"(OFF) : "memory");
}
template <int OFF> DI void bload_sc1(u32x4& d, rsrc_t r, u32 voff, u32 soff) {
  asm volatile("buffer_load_dwordx4 %0, %1, %2, %3 offen offset:%4 sc1" : "=v"(d) : "v"(voff), "s"(r), "s"(soff), "n"(OFF) : "memory");
}
DI void stat_loads(int fg, u32x4 (&x)[6], rsrc_t r, u32 voff, u32 soff, u32 soff2) {
  asm volatile("s_cmp_gt_u32 %10, 5\n\ts_cbranch_scc1 1f\n\t"
               "buffer_load_dwordx4 %0, %6, %7, %8 offen\n\tbuffer_load_dwordx4 %1, %6, %7, %8 offen offset:1024\n\t"
               "buffer_load_dwordx4 %2, %6, %7, %8 offen offset:2048\n\tbuffer_load_dwordx4 %3, %6, %7, %8 offen offset:3072\n\t"
               "buffer_load_dwordx4 %4, %6, %7, %9 offen\n\tbuffer_load_dwordx4 %5, %6, %7, %9 offen offset:1024\n1:"
               : "=&v"(x[0]), "=&v"(x[1]), "=&v"(x[2]), "=&v"(x[3]), "=&v"(x[4]), "=&v"(x[5])
               : "v"(voff), "s"(r), "s"(soff), "s"(soff2), "s"(fg) : "memory", "scc");
}
DI void code_touch(const char* base, u32 voff, u32 lds_dst) {
  asm volatile("s_mov_b32 m0, %2\n\ts_nop 0\n\tglobal_load_lds_dword %0, %1" ::"v"(voff), "s"(base), "s"(lds_dst) : "memory");
}
#define CODE_RANGE(base, len, endlabel) \
  asm volatile("s_getpc_b64 s[40:41]\n\ts_mov_b64 %0, s[40:41]\n\ts_mov_b32 %1, " endlabel "@rel32@lo-4" : "=s"(base), "=s"(len)::"s40", "s41")
DI ldsp opaque(ldsp p) { asm volatile("" : "+v"(p)); return p; }
DI u32 opq(u32 p) { asm volatile("" : "+v"(p)); return p; }
DI void pin(u32& p) { asm volatile("" : "+v"(p)); }
DI void pin6(u32x4 (&x)[6]) { asm volatile("" : "+v"(x[0]), "+v"(x[1]), "+v"(x[2]), "+v"(x[3]), "+v"(x[4]), "+v"(x[5])); }
DI void pinf(float& p) { asm volatile("" : "+v"(p)); }
DI float hide(float p) { asm("" : "+v"(p)); return p; }
DI float swap16_sum(float x) { const auto r = __builtin_amdgcn_permlane16_swap(__float_as_uint(x), __float_as_uint(x), false, false); return __uint_as_float(r[0]) + __uint_as_float(r[1]); }
DI float swap32_sum(float x) { const auto r = __builtin_amdgcn_permlane32_swap(__float_as_uint(x), __float_as_uint(x), false, false); return __uint_as_float(r[0]) + __uint_as_float(r[1]); }
#define STAT1(R, A, B) "v_lshlrev_b32_e32 %0, 16, " R "\n\tv_and_b32_e32 %1, %10, " R "\n\tv_add_f32_e32 " A ", " A ", %0\n\tv_fmac_f32_e32 " B ", %0, %0\n\tv_add_f32_e32 " A ", " A ", %1\n\tv_fmac_f32_e32 " B ", %1, %1\n\t"
DI void stat_acc(int fg, const u32x4& v, float (&s1)[2], float (&s2)[2]) {
  float x, y;
  asm volatile("s_cmp_gt_u32 %11, 5\n\ts_cbranch_scc1 1f\n\t" STAT1("%6", "%2", "%3") STAT1("%8", "%4", "%5") STAT1("%7", "%2", "%3") STAT1("%9", "%4", "%5") "\n1:"
               : "=&v"(x), "=&v"(y), "+v"(s1[0]), "+v"(s2[0]), "+v"(s1[1]), "+v"(s2[1]) : "v"(v[0]), "v"(v[1]), "v"(v[2]), "v"(v[3]), "s"(0xffff0000u), "s"(fg) : "scc");
}
DI void ld_sc1_x4(u32x4& d, u32 voff, const char* sb) { asm volatile("global_load_dwordx4 %0, %1, %2 sc1" : "=v"(d) : "v"(voff), "s"(sb) : "memory"); }
template <int OFF> DI void ld_sc1_x2(u32x2& d, u32 voff, const char* sb) { asm volatile("global_load_dwordx2 %0, %1, %2 offset:%3 sc1" : "=v"(d) : "v"(voff), "s"(sb), "n"(OFF) : "memory"); }
DI void wait0_x6(u32x4& a, u32x2& b) { asm volatile("s_waitcnt vmcnt(0)" : "+v"(a), "+v"(b)::"memory"); }
DI void st_sc1_x4(char* sb, u32 voff, const u32x4& v) { asm volatile("global_store_dwordx4 %0, %1, %2 sc1\n\ts_nop 1" ::"v"(voff), "v"(v), "s"(sb) : "memory"); }
DI void gstore16(char* sb, u32 voff, const u32x4& v) { asm volatile("global_store_dwordx4 %0, %1, %2\n\ts_nop 1" ::"v"(voff), "v"(v), "s"(sb) : "memory"); }
DI void gstore8(char* sb, u32 voff, const u32x2& v) { asm volatile("global_store_dwordx2 %0, %1, %2" ::"v"(voff), "v"(v), "s"(sb) : "memory"); }
DI void st_sc1_x2_if(int skip, const char* sb, u32 voff, const u32x2& v) {
  asm volatile("s_cmp_gt_u32 %3, 5\n\ts_cbranch_scc1 1f\n\tglobal_store_dwordx2 %0, %1, %2 sc1\n1:" ::"v"(voff), "v"(v), "s"(sb), "s"(skip) : "memory", "scc");
}
DI void st_sc1_x2(char* sb, u32 voff, const u32x2& v) { asm volatile("global_store_dwordx2 %0, %1, %2 sc1" ::"v"(voff), "v"(v), "s"(sb) : "memory"); }

#define STR(i)
#define ST(i)
#define ST_DECL
#define ST_FLUSH(dbg, wv)
#define ST_DRAIN()

DI void colsum_slice(const u16* __restrict__ wg, const u16* __restrict__ wu, float* __restrict__ cs, int sl, int l) {
  const int mat = sl & 1, qr = (sl >> 1) & 3, cb = sl >> 3;
  const u16* p = (mat ? wu : wg) + (size_t)(qr * 144 + (l >> 3)) * DF_ + cb * 64 + (l & 7) * 8;
  u32x4 x[18];
#pragma unroll
  for (int i = 0; i < 18; ++i) x[i] = *(const u32x4*)(p + (size_t)i * 8 * DF_);
  float a[8];
#pragma unroll
  for (int e = 0; e < 8; ++e) a[e] = 0.f;
#pragma unroll
  for (int i = 0; i < 18; ++i)
#pragma unroll
    for (int e = 0; e < 4; ++e) { a[2 * e] += bflo(x[i][e]); a[2 * e + 1] += bfhi(x[i][e]); }
#pragma unroll
  for (int e = 0; e < 8; ++e) { a[e] += __shfl_xor(a[e], 8); a[e] = swap32_sum(swap16_sum(a[e])); }
  if (l < 8) {
    float* d = cs + qr * 9216 + (cb * 32 + l * 4) * 4 + mat * 2;
#pragma unroll
    for (int e = 0; e < 4; ++e) { f32x2 o = {a[2 * e], a[2 * e + 1]}; *(f32x2*)(d + e * 4) = o; }
  }
}

constexpr int K0_GRID = 64 + 288;
template <bool NT> DI void dma16p(const char* sbase, u32 voff, u32 lds_dst) {
  if constexpr (NT) asm volatile("s_mov_b32 m0, %2\n\ts_nop 0\n\tglobal_load_lds_dwordx4 %0, %1 nt" ::"v"(voff), "s"(sbase), "s"(lds_dst) : "memory");
  else dma16(sbase, voff, lds_dst);
}
extern "C" __global__ __launch_bounds__(256) void k0_prep(const u16* __restrict__ q, const u16* __restrict__ k, const u16* __restrict__ v,
                                                          const u16* __restrict__ shk, const u16* __restrict__ ssm, char* __restrict__ ws) {
  __shared__ __attribute__((aligned(128))) char lds[(72 + 64 + 64 + 64) * 128];
  ST_DECL; ST(0);
  asm volatile("" ::"s"(q), "s"(k), "s"(v), "s"(shk), "s"(ssm), "s"(ws));
  const int tid = threadIdx.x, l = tid & 63;
  const int w = __builtin_amdgcn_readfirstlane(tid >> 6);
  if (blockIdx.x >= 64) {
    const int b = blockIdx.x - 64;
    const int r8 = b >> 3, tb = r8 < 32 ? r8 : (b & 7) * 4 + (r8 - 32), cb = r8 < 32 ? (b & 7) : 8;
    const ldsp LP = (ldsp)lds;
    const u32 L0 = (u32)(uintptr_t)LP;
    constexpr int KT = 0, VT = 72 * 128, QT = VT + 8192, OT = QT + 8192;
    const int col = cb * 128 + (l & 7) * 16, rl = l >> 3;
    {
      const int r0 = tb * 64 - 8 + w * 8 + rl;
      dma16p<true>((const char*)k, (u32)(max(r0, 0) * (D_ * 2) + col), L0 + KT + w * 1024);
      const u32 vo = (u32)((tb * 64 + w * 8 + rl) * (D_ * 2) + col);
      dma16p<true>((const char*)k + 24 * D_ * 2, vo, L0 + KT + (w + 4) * 1024);
      if (w == 0) dma16p<true>((const char*)k + 56 * D_ * 2, vo, L0 + KT + 8 * 1024);
      dma16p<true>((const char*)v, vo, L0 + VT + w * 1024);
      dma16p<true>((const char*)v + 32 * D_ * 2, vo, L0 + VT + (w + 4) * 1024);
    }
    const int sl = w * 16 + (l >> 4) * 4, cq = l & 15;
    u32x4 w0, w1, pf0, pf1;
    gload16<0>(w0, (u32)((cb * 64 + cq * 4) * 8), (const char*)shk);
    gload16<16>(w1, (u32)((cb * 64 + cq * 4) * 8), (const char*)shk);
    {
      const u32 vo = (u32)((tb * 64 + w * 8 + rl) * (D_ * 2) + col);
      dma16p<false>((const char*)q, vo, L0 + QT + w * 1024);
      dma16p<false>((const char*)q + 32 * D_ * 2, vo, L0 + QT + (w + 4) * 1024);
    }
    ST(3);
    wait_vm2<2>(w0, w1);
    BARRIER();
    ST(4);
    u32x2 kr[7], vr[4];
#pragma unroll
    for (int i = 0; i < 7; ++i) kr[i] = *(const LAS u32x2*)(LP + (KT + (sl + 5 + i) * 128 + cq * 8));
#pragma unroll
    for (int i = 0; i < 4; ++i) vr[i] = *(const LAS u32x2*)(LP + (VT + (sl + i) * 128 + cq * 8));
    const u32 po = (u32)((r8 < 32 ? (b & 7) * 64 + r8 * 2 : 512 + (b & 7) * 8 + (r8 - 32) * 2) * 4096 + tid * 16);
    gload16<0>(pf0, po, (const char*)ssm);
    gload16<0>(pf1, po + 4096, (const char*)ssm);
    if (tb == 0 && sl == 0) { kr[0] = (u32x2){0u, 0u}; kr[1] = kr[0]; kr[2] = kr[0]; }
#pragma unroll
    for (int c = 0; c < 4; ++c) {
      const u32 wa = (c < 2) ? w0[2 * (c & 1)] : w1[2 * (c & 1)], wb = (c < 2) ? w0[2 * (c & 1) + 1] : w1[2 * (c & 1) + 1];
      const float f0 = bflo(wa), f1 = bfhi(wa), f2 = bflo(wb), f3 = bfhi(wb);
      float kk[7], kvv[4];
#pragma unroll
      for (int i = 0; i < 7; ++i) kk[i] = (c & 1) ? bfhi(kr[i][c >> 1]) : bflo(kr[i][c >> 1]);
#pragma unroll
      for (int i = 0; i < 4; ++i) {
        const float ks = kk[i + 3] * f0 + kk[i + 2] * f1 + kk[i + 1] * f2 + kk[i] * f3;
        const float vv = (c & 1) ? bfhi(vr[i][c >> 1]) : bflo(vr[i][c >> 1]);
        kvv[i] = ks * vv;
      }
      u32x2 o = {pack2(kvv[3], kvv[2]), pack2(kvv[1], kvv[0])};
      *(LAS u32x2*)(LP + (OT + (cq * 4 + c) * 128 + (((sl >> 3) ^ (cq & 7)) * 16) + ((sl & 4) ? 0 : 8))) = o;
    }
    wait_vm<2>();
    LDS_BARRIER();
#pragma unroll
    for (int i = 0; i < 2; ++i) {
      const int ch = w * 16 + i * 8 + (l >> 3);
      const u32x4 o = *(const LAS u32x4*)(LP + (OT + (w * 16 + i * 8) * 128 + l * 16));
      gstore16(ws + OFF_KVT, (u32)((cb * 64 + ch) * 4096 + (tb * 8 + ((l & 7) ^ ((ch >> 2) & 7))) * 16), o);
    }
#pragma unroll
    for (int i = 0; i < 2; ++i) {
      const int gl = i * 8 + (l >> 3), t2 = w * 16 + (l & 7) * 2;
      const u32x2 a = *(const LAS u32x2*)(LP + (QT + t2 * 128 + gl * 8)), c = *(const LAS u32x2*)(LP + (QT + (t2 + 1) * 128 + gl * 8));
      u32x4 o = {a[0], a[1], c[0], c[1]};
      gstore16(ws + OFF_XP, (u32)(((cb * 16 + gl) * S_ + tb * 64 + t2) * 8), o);
    }
    if (b < 41) {
      const u32 f = b < 9 ? 0u : 0xffffffffu;
      u32x4 z = {f, f, f, f};
      gstore16(ws + OFF_FLG, (u32)(b * 4096 + tid * 16), z);
    }
    ST(5);
    asm volatile("s_waitcnt vmcnt(0)" : "+v"(pf0), "+v"(pf1)::"memory");
  } else {
    if (w == 0) {
      unsigned long long c0;
      asm volatile("s_getpc_b64 s[40:41]\n\ts_add_u32 s40, s40, k1_conv@rel32@lo+4\n\ts_addc_u32 s41, s41, k1_conv@rel32@hi+12\n\ts_mov_b64 %0, s[40:41]" : "=s"(c0)::"s40", "s41", "scc");
      code_touch((const char*)c0, (u32)(l * 128), (u32)(uintptr_t)(ldsp)lds);
    }
    const int row = blockIdx.x * 32 + w * 8 + (l >> 3);
    const u16* p = q + (size_t)row * D_ + (l & 7) * 8;
    u32x4 x[9];
#pragma unroll
    for (int i = 0; i < 9; ++i) x[i] = *(const u32x4*)(p + i * 64);
    float sum = 0.f;
#pragma unroll
    for (int i = 0; i < 9; ++i)
#pragma unroll
      for (int j = 0; j < 4; ++j) sum += bflo(x[i][j]) + bfhi(x[i][j]);
    sum += __shfl_xor(sum, 1); sum += __shfl_xor(sum, 2); sum += __shfl_xor(sum, 4);
    const float mu = sum * (1.f / D_);
    float sq = 0.f;
#pragma unroll
    for (int i = 0; i < 9; ++i)
#pragma unroll
      for (int j = 0; j < 4; ++j) { const float a = bflo(x[i][j]) - mu, c = bfhi(x[i][j]) - mu; sq += a * a + c * c; }
    sq += __shfl_xor(sq, 1); sq += __shfl_xor(sq, 2); sq += __shfl_xor(sq, 4);
    if ((l & 7) == 0) { f32x2 o = {mu, rsqrtf(sq * (1.f / D_) + 1e-5f)}; ((f32x2*)(ws + OFF_ST1))[row] = o; }
  }
  ST(1); ST_DRAIN(); ST(2);
  ST_FLUSH((u32*)(ws + (202u << 20)), (blockIdx.x >= 64 ? blockIdx.x - 64 : blockIdx.x + 288) * 4 + w);
}

constexpr int K1_UPLANE = 2304, K1_UCH = 4608, K1_TOFF = 4 * K1_UCH, K1_TCOPY = 4160, K1_TCH = 4 * K1_TCOPY;
constexpr int K1_YOFF = K1_TOFF + 4 * K1_TCH, K1_YBUF = 8192;
constexpr int K1_LDS = K1_YOFF + 8 * K1_YBUF;
constexpr int K1_GRID = 256;

#define HSTORE gstore16
extern "C" __global__ __launch_bounds__(512) void k1_conv(const u16* __restrict__ ssm, const u16* __restrict__ ssmD, const u16* __restrict__ wg,
                                                          const u16* __restrict__ wu, char* __restrict__ ws) {
  extern __shared__ __attribute__((aligned(128))) char lds[];
  ST_DECL; ST(0);
  asm volatile("" ::"s"(ssm), "s"(ssmD), "s"(wg), "s"(wu), "s"(ws));
  const int tid = threadIdx.x, l = tid & 63;
  const int w = __builtin_amdgcn_readfirstlane(tid >> 6);
  const int b = blockIdx.x;
  const ldsp LP = (ldsp)lds;
  const u32 L0 = (u32)(uintptr_t)LP;
  if (b >= 144) {
    if (w == 7) {
      unsigned long long c0; u32 cl;
      asm volatile("s_getpc_b64 s[40:41]\n\ts_add_u32 s40, s40, ffn_fused@rel32@lo+4\n\ts_addc_u32 s41, s41, ffn_fused@rel32@hi+12\n\ts_mov_b64 %0, s[40:41]\n\ts_mov_b32 %1, .Lffn_code_end-ffn_fused"
                   : "=s"(c0), "=s"(cl)::"s40", "s41", "scc");
#pragma unroll
      for (int i = 0; i < 5; ++i) code_touch((const char*)c0, min((u32)(l * 128 + i * 8192), cl - 4u), L0);
    }
    for (int i = 0; i < SIDE_DELAY; ++i) __builtin_amdgcn_s_sleep(8);
    const int j = ((b - 144) >> 3) + 14 * w, jj = j * 2 + ((b >> 2) & 1), cbl = jj >> 3;
    if (j < 72) colsum_slice(wg, wu, (float*)(ws + OFF_CS), (cbl < 16 ? (b & 3) * 16 + cbl : 64 + (b & 3) * 2 + (cbl - 16)) * 8 + (jj & 7), l);
  } else {
    const u16* __restrict__ kvt = (const u16*)(ws + OFF_KVT);
    const u16* __restrict__ xp = (const u16*)(ws + OFF_XP);
    const f32x2* __restrict__ st1 = (const f32x2*)(ws + OFF_ST1);
    const int g = (b >> 3) < 16 ? (b & 7) * 16 + (b >> 3) : 128 + (b & 7) * 2 + ((b >> 3) - 16), ch = w & 3, hf_ = w >> 2;
    const int gch = g * 4 + ch;
    {
      const char* src = (const char*)(kvt + (size_t)gch * S_);
      const u32 vo = (2 * l + hf_) * 16;
      const u32 dst = L0 + ch * K1_UCH + hf_ * K1_UPLANE + 256;
      dma16(src, vo, dst);
      dma16(src + 2048, vo, dst + 1024);
    }
    const u16* krow = ssm + (size_t)gch * S_;
    u32x4 wv[2]; u32x2 wn[2];
  #pragma unroll
    for (int it = 0; it < 2; ++it) {
      const int idx = hf_ * 128 + it * 64 + l;
      wv[it] = *(const u32x4*)(krow + (idx > 0 ? idx - 1 : 0) * 8);
      wn[it] = *(const u32x2*)(krow + idx * 8);
    }
    const u32x4 wl = *(const u32x4*)(krow + opq(255 * 8));
    const u32 dv1 = ssmD[opq(gch)];
    SCHED0();
    u32x2 xr[4]; f32x2 st[4];
  #pragma unroll
    for (int i = 0; i < 4; ++i) { const int s = (tid >> 4) * 32 + (tid & 15) + (i & 1) * 16 + (i >> 1) * 1024; xr[i] = *(const u32x2*)(xp + ((size_t)g * S_ + s) * 4); st[i] = st1[s]; }
    SCHED0();
    const u32x4 z4 = {0u, 0u, 0u, 0u};
    if (l < 16) *(LAS u32x4*)(LP + (ch * K1_UCH + hf_ * K1_UPLANE + l * 16)) = z4;
    const ldsp tb = LP + (K1_TOFF + ch * K1_TCH);
    auto put = [&](int pos  , u32x4 a, u32x2 n) {
      const u32 o0 = __builtin_amdgcn_alignbit(a[1], a[0], 16), o1 = __builtin_amdgcn_alignbit(a[2], a[1], 16), o2 = __builtin_amdgcn_alignbit(a[3], a[2], 16),
                o3 = __builtin_amdgcn_alignbit(n[0], a[3], 16), o4 = __builtin_amdgcn_alignbit(n[1], n[0], 16);
      u32x4 c1 = {o0, o1, o2, o3}, c2 = {a[1], a[2], a[3], n[0]}, c3 = {o1, o2, o3, o4};
      *(LAS u32x4*)(tb + pos * 16) = a;
      *(LAS u32x4*)(tb + (K1_TCOPY + pos * 16)) = c1;
      *(LAS u32x4*)(tb + (2 * K1_TCOPY + pos * 16)) = c2;
      *(LAS u32x4*)(tb + (3 * K1_TCOPY + pos * 16)) = c3;
    };
    if (hf_ == 0 && l == 0) wv[0] = z4;
  #pragma unroll
    for (int it = 0; it < 2; ++it) put(hf_ * 128 + it * 64 + l + 1, wv[it], wn[it]);
    if (hf_ == 1 && l < 2) { u32x2 z2 = {0u, 0u}; put(l ? 257 : 0, l ? wl : z4, z2); }
    ST(1);
    wait_vm<8>();
    LDS_BARRIER();
    ST(2);
    const int a_ = l & 15, q_ = l >> 4;
    const int cpy = (1 + a_) & 3;
    const ldsp taddr = LP + (K1_TOFF + ch * K1_TCH + cpy * K1_TCOPY + 2 * (1 + a_ + 8 * q_ - cpy) + hf_ * 256);
    const ldsp uaddr = LP + (ch * K1_UCH + ((q_ + 1) & 1) * K1_UPLANE + (16 + a_ - (q_ >> 1) - 8 * hf_ - 6) * 16);
    f32x4 acc[8];
    u32x4 dg;
    {
      const int j = 15 - a_ - 8 * q_;
      const u32 dval = hf_ ? 0u : dv1;
  #pragma unroll
      for (int e = 0; e < 4; ++e) dg[e] = (j == 2 * e) ? dval : ((j == 2 * e + 1) ? (dval << 16) : 0u);
    }
    u32x4 U[2][8], T[2][8];
    auto ldU = [&](int e, int j) { return *(const LAS u32x4*)(uaddr + ((3 - e) * 32 + 256 * j)); };
    auto ldT = [&](int e, int m) {
      const u32x2 lo = *(volatile const LAS u32x2*)(taddr + (64 * e + 512 * m)), hi = *(volatile const LAS u32x2*)(taddr + (8 + 64 * e + 512 * m));
      u32x4 t = {lo[0], lo[1], hi[0], hi[1]};
      return t;
    };
#pragma unroll
    for (int j = 0; j < 8; ++j) U[0][j] = ldU(0, j);
#pragma unroll
    for (int j = 0; j < 8; ++j) T[0][j] = ldT(0, j);
    asm volatile("s_nop 1" : "+v"(dg));
    SCHED0();
#pragma unroll
    for (int n = 0; n < 8; ++n) mma<true>(acc[n], dg, U[0][n]);
    SCHED0();
    float xs[16], xn[16];
    static_for<0, 144>([&](auto ic) {
      constexpr int e = ic.value / 36, i = ic.value % 36;
      constexpr auto mn = [] { int k = 0; for (int m = 0; m < 8; ++m) for (int n = m; n < 8; ++n) { if (k == i) return m * 8 + n; ++k; } return 0; }();
      constexpr int m = mn >> 3, n = mn & 7;
      if constexpr (e < 3 && i < 32 && (i & 1) == 0) {
        if constexpr ((i & 2) == 0) U[(e + 1) & 1][i >> 2] = ldU(e + 1, i >> 2); else T[(e + 1) & 1][i >> 2] = ldT(e + 1, i >> 2);
      }
      mma<false>(acc[n], T[e & 1][m], U[e & 1][n - m]);
      if constexpr (e == 3 && i < 16) {
        constexpr int ti = i >> 2, c = i & 3;
        xs[i] = (c & 1) ? bfhi(xr[ti][c >> 1]) : bflo(xr[ti][c >> 1]);
        xn[i] = hide(xs[i] - st[ti][0]) * st[ti][1];
        pinf(xs[i]); pinf(xn[i]);
      }
      SCHED0();
    });
    MFMA_DRAIN(); SCHED0();
    ST(3);
    {
      const ldsp yb = LP + (K1_YOFF + (hf_ * 4 + ch) * K1_YBUF + ((4 * a_ + q_) ^ ((a_ >> 1) & 3) ^ ((a_ & 2) << 1)) * 16);
  #pragma unroll
      for (int n = 0; n < 8; ++n) *(LAS f32x4*)(yb + n * 1024) = acc[n];
    }
    LDS_BARRIER();
    ST(4);
    {
      float y[4][8];
#pragma unroll
      for (int i = 0; i < 4; ++i) {
        const int s = (tid >> 4) * 32 + (tid & 15) + (i & 1) * 16, sl = s >> 2;
        const ldsp yb = LP + (K1_YOFF + (sl ^ ((sl >> 3) & 3) ^ ((sl >> 1) & 4)) * 16 + (s & 3) * 4 + (i >> 1) * 4096);
#pragma unroll
        for (int c = 0; c < 8; ++c) y[i][c] = *(const LAS float*)(yb + c * K1_YBUF);
      }
#pragma unroll
      for (int j = 0; j < 2; ++j) {
        u32x4 o;
#pragma unroll
        for (int e = 0; e < 2; ++e) {
          const int i = 2 * j + e;
          float hv[4];
#pragma unroll
          for (int c = 0; c < 4; ++c) hv[c] = fmaf(xn[4 * i + c], hide(y[i][c] + y[i][4 + c]), xs[4 * i + c]);
          o[2 * e] = pack2(hv[0], hv[1]); o[2 * e + 1] = pack2(hv[2], hv[3]);
        }
        HSTORE(ws + OFF_HF + (size_t)j * 32 * 36864 + (g >> 3) * 2048 + (g & 1) * 1024 + ((g >> 1) & 3) * 256, (u32)((tid >> 4) * 36864 + (tid & 15) * 16), o);
      }
    }
    ST(5);
  }
  ST_DRAIN(); ST(6);
  ST_FLUSH((u32*)(ws + (203u << 20)), b * 8 + w);
}

constexpr int G1_QE = 147456;
constexpr int G1_QL = 14 * 8192;
constexpr int G1_CS = G1_QE + 8192;
constexpr int G1_CP = G1_CS + 2048;
constexpr int FF_LDS = G1_CP + 5120 + 256;
constexpr int PF_ISS0 = 86;
constexpr int STG = 18432, NST = 8;
constexpr int G1_D = 4, G1_DW = 6, HD = 5, HB = HD + 1;
constexpr int p1_ops(int x, bool even_only) { return (x + G1_DW < 18 ? 1 : 0) + (x + G1_D < 18 ? (even_only ? 1 : 2) : 0) + (x == 18 - G1_D ? 3 : 0); }
constexpr int p1_wait(int g) {
  int n = 0;
  if (g + 1 < G1_D) n += 3 * (G1_D - (g + 2)) + (G1_DW - G1_D);
  for (int x = (g + 2 - G1_D > 0 ? g + 2 - G1_D : 0); x < g; ++x) n += p1_ops(x, false);
  return n + p1_ops(g, true);
}
constexpr bool p1_bar(int g) { return g < G1_D - 1 || (g - (G1_D - 1)) % (G1_DW - G1_D + 1) == 0; }
constexpr float GC = -2.3022081983f;
constexpr int U2 = 216;
constexpr int g1_q_issue(int half, int r) { return 8 + 6 * r; }
constexpr int g1_b_issue(int half, int j) { return (j < 14 ? 29 + 3 * j : 73 + 3 * (j - 14)); }
constexpr int G1_E0 = 9;
constexpr int epi_start(int i) { return i < 8 ? G1_E0 + 26 * i : U2 + 2; }
static_assert(G1_E0 + 26 * 7 + 24 < 216, "epilogue stream");
constexpr bool epi_stores(int i) { return i == 1 || i == 3 || i == 4 || i == 6 || i == 8; }
constexpr int w_issue(int n, int k) {
  return n < 4 ? 191 + 4 * n + 2 * k : n < 6 ? 209 + 4 * (n - 4) + 2 * k : ((n & 1) || (G2_BAR_ALL && n > 6)) ? U2 + 9 * (n - 6) + 4 * k : U2 + 9 * (n - 6) + WE0 + WE1 * k;
}
constexpr int h_issue(int n, int k) { return n < 5 ? 192 + 4 * n + 2 * k : U2 + 9 * (n - (n < 10 ? HD : 4)) + 2 + 3 * k; }
constexpr int dq_issue(int r) { return r == 0 ? 183 : r < 4 ? U2 + 9 * (4 * r - 3) + 1 : U2 + 9 * 11 + 1; }
constexpr int poll_iss(int p) { return p == 0 ? 180 : p == 1 ? U2 + 9 * 2 + 3 : U2 + 9 * 7 + 3; }
constexpr int poll_chk(int p) { return p == 0 ? 191 : p == 1 ? U2 + 9 * 6 + 1 : U2 + 9 * 10 + 1; }
constexpr int RES_ISS = U2 + 9 * 16 + 7;
template <class F> constexpr int find_at(int n, int u, F f) { for (int i = 0; i < n; ++i) if (f(i) == u) return i; return -1; }
constexpr int cnt_ops(int half, int k0, int k1) {
  int n = 0;
  auto in = [&](int key) { return key > k0 && key < k1 ? 1 : 0; };
  for (int r = 0; r < 4; ++r) n += in(4 * g1_q_issue(half, r) + 2);
  for (int j = 0; j < 18; ++j) n += in(4 * g1_b_issue(half, j) + 2);
  for (int i = 0; i < 8; ++i) if (epi_stores(i)) n += in(4 * (epi_start(i) + 18)) + in(4 * (epi_start(i) + 24));
  n += in(4 * (U2 + 11)) + in(4 * (U2 + 14));
  for (int s = 0; s < 18; ++s) for (int k = 0; k < 2; ++k) n += in(4 * w_issue(s, k) + 2) + in(4 * h_issue(s, k) + 2);
  for (int p = 0; p < 3; ++p) n += in(4 * poll_iss(p) + 2);
  n += 3 * in(4 * RES_ISS + 2);
  for (int r = 0; r < 5; ++r) n += in(4 * (PF_ISS0 + 4 * r) + 2);
  for (int r = 0; r < 5; ++r) n += in(4 * dq_issue(r) + 2);
  return n;
}
constexpr int wait_n(int k0a, int k0b, int k1) { return cmin(cnt_ops(0, k0a, k1), cnt_ops(1, k0b, k1)); }
constexpr int wait_q(int r, int tw) { return wait_n(4 * g1_q_issue(0, r) + 2, 4 * g1_q_issue(1, r) + 2, 4 * tw + 1); }
constexpr int wait_b(int j, int tw) { return wait_n(4 * g1_b_issue(0, j) + 2, 4 * g1_b_issue(1, j) + 2, 4 * tw + 1); }
constexpr int wait_k(int k0, int k1) { return wait_n(k0, k0, k1); }
constexpr int cmax(int a, int b) { return a > b ? a : b; }
static_assert(w_issue(1, 1) > epi_start(6) + 24 && h_issue(0, 0) > poll_chk(0) && h_issue(10, 0) > poll_chk(1) && h_issue(14, 0) > poll_chk(2), "schedule");
constexpr int kk_of(int s) { return s < 8 ? 9 * ((s >> 1) & 1) + 4 * (s & 1) + (s >> 2) : s < 10 ? 9 * (s - 8) + 8 : 9 * (((s - 10) >> 1) & 1) + 4 * (s & 1) + 2 + ((s - 10) >> 2); }
struct Acc { f32x4 a[2][2]; };

extern "C" __global__ __launch_bounds__(512, 2) void ffn_fused(const u16* __restrict__ wg, const u16* __restrict__ wu, const u16* __restrict__ wd,
                                                               char* __restrict__ ws, u16* __restrict__ out) {
  extern __shared__ __attribute__((aligned(128))) char lds[];
  ST_DECL; ST(0);
  asm volatile("" ::"s"(wg), "s"(wu), "s"(wd), "s"(ws), "s"(out));
  const int tid = threadIdx.x, l = tid & 63;
  const int w = __builtin_amdgcn_readfirstlane(tid >> 6);
  const int b = blockIdx.x;
  const int fg = (b & 3) * 8 + ((b >> 3) & 7), tg = ((b >> 2) & 1) * 4 + (b >> 6);
  const int tt = tg * 8 + w;
  const ldsp LP = (ldsp)lds;
  const u32 L0 = (u32)(uintptr_t)LP;
  constexpr u32 RS = DF_ * 2, GS = 32 * RS;
  const int wblk = w & 1, whr = (w >> 1) & 1, whalf = w >> 2;
  const rsrc_t rW = mk_rsrc((w & 1) ? wu : wg, D_ * DF_ * 2), rS = mk_rsrc(ws, (u32)WS_USED), rD = mk_rsrc(wd, D_ * DF_ * 2);
  const u32 ab_voff = (u32)((16 * whalf + 8 * whr + (l >> 3)) * RS + ((((l & 7) >> 1) ^ (((l >> 4) & 1) | (whr << 1))) * 32) + (l & 1) * 16 + fg * 256);
  const u32 ab_dst = L0 + whalf * 4096 + wblk * 2048 + whr * 1024;
  auto mk_qvoff = [&](u32 l2) { return (u32)(((l2 >> 1) ^ ((l2 >> 2) & 4)) * RS + (l2 & 1) * 16 + (w >> 1) * GS + 8192 + fg * 32); };
  u32x4 h[2][18];
  const u32 lo16 = l * 16;
  const u32 h_voff = lo16 + (u32)OFF_HF + tt * 36864;
  auto issue_dma = [&](auto ic) { constexpr int g = ic.value; bdma<g * 8192>(rW, ab_voff, g * GS, ab_dst); };
  auto issue_h = [&](auto ic, auto tc) { constexpr int g = ic.value, tl = tc.value; bload<(g & 1) * 2048 + tl * 1024>(h[tl][g], rS, h_voff, (g >> 1) * 4096); };
  static_for<0, G1_D>([&](auto ic) { issue_dma(ic); issue_h(ic, IC<0>{}); issue_h(ic, IC<1>{}); });
  static_for<G1_D, G1_DW>(issue_dma);
  const char* code0; u32 codelen;
  CODE_RANGE(code0, codelen, ".Lffn_code_end");
  u32x4 sx[6];
  stat_loads(fg, sx, rS, h_voff, fg * 6144, fg * 6144 + 4096);
  ST(1);
  const u32 lw = L0 + w * 1024;
  const int rq = l >> 4, rr = (l >> 2) & 3, rp = l & 3;
  const u32 rb0 = L0 + (rq >> 1) * 4096 + (rq & 1) * 1024 + rr * 128 + rp * 8 + (((rr >> 1) | ((rq & 1) << 1)) * 32);
  auto abase = [&](int c, int off) { return opaque((ldsp)(uintptr_t)((opq(rb0) ^ (u32)(c * 32)) + (u32)off)); };
  Acc acc[4];
  const char* stb = ws + OFF_ST2;
  u32x4 sg4; u32x2 sg2;
  {
    float s1[2] = {0.f, 0.f}, s2[2] = {0.f, 0.f};
    ldsp pb[3];
    Frag pf[2][3];
    auto fetch1 = [&](auto ic, int c, int hi) {
      constexpr int s = ic.value;
      if constexpr ((s & 15) == 0) { if (hi == 0) pb[c] = abase(c, s * 4096); }
      constexpr int o = ((s & 15) >> 1) * 8192 + (s & 1) * 2048;
      if (hi) pf[s & 1][c].hi = tr_half(pb[c] + (o + 512)); else pf[s & 1][c].lo = tr_half(pb[c] + o);
    };
    wait_vm2<3 * (G1_D - 1) + G1_DW - G1_D>(h[0][0], h[1][0]);
    BARRIER();
    swap_halves(h[0][0], h[1][0]);
#pragma unroll
    for (int c = 0; c < 3; ++c) { fetch1(IC<0>{}, c, 0); fetch1(IC<0>{}, c, 1); }
    static_for<0, 36>([&](auto ic) {
      constexpr int s = ic.value, g = s >> 1, mt = s & 1;
      constexpr int SQ = 2 * (18 - G1_D);
      auto mm = [&](int c) {
#pragma unroll
        for (int tl = 0; tl < 2; ++tl) {
          mma<g == 0>(acc[c].a[mt][tl], frag_of(pf[s & 1][c]), h[tl][g]);
          SCHED0();
          if constexpr (s < 35) fetch1(IC<s + 1>{}, c, tl);
          SCHED0();
        }
      };
      if constexpr (mt == 1 && s < 35) {
        wait_vm2<p1_wait(g)>(h[0][g + 1], h[1][g + 1]);
        if constexpr (p1_bar(g)) BARRIER();
      }
      SCHED0();
      mm(0);
      if constexpr (mt == 0 && g + G1_DW < 18) issue_dma(IC<g + G1_DW>{});
      if constexpr (mt == 1 && g + G1_D < 18) issue_h(IC<g + G1_D>{}, IC<1>{});
      if constexpr (s == SQ) {
        const int ii = min(tid, 287), qd = ii / 72, r = ii - qd * 72;
        if (w < 5) dma16(ws + OFF_CS, (u32)(qd * 2304 + (r < 64 ? fg * 64 + r : 2048 + fg * 8 + (r - 64))) * 16u, L0 + G1_CP + w * 1024);
        bdma<G1_QE>(rW, mk_qvoff(l), 0, lw);
      }
      SCHED0();
      mm(1);
      if constexpr (g + G1_D < 18 && mt == 0) issue_h(IC<g + G1_D>{}, IC<0>{});
      if constexpr (s == SQ) { const u32 so = tt * 2048 + ((rq >> 1) * 16 + (l & 15)) * 64 + (rq & 1) * 24; ld_sc1_x4(sg4, so, stb); ld_sc1_x2<16>(sg2, so, stb); }
      if constexpr (mt == 1 && s < 35) swap_halves(h[0][g + 1], h[1][g + 1]);
      if constexpr (s >= 8 && s < 14) { if constexpr (s == 8) pin6(sx); stat_acc(fg, sx[s - 8], s1, s2); }
      SCHED0();
      mm(2);
      if constexpr (s == 14) {
        const float a0 = swap32_sum(swap16_sum(s1[0])), c0 = swap32_sum(swap16_sum(s2[0]));
        const float a1 = swap32_sum(swap16_sum(s1[1])), c1 = swap32_sum(swap16_sum(s2[1]));
        u32x2 gr = {__float_as_uint((l & 16) ? a1 : a0), __float_as_uint((l & 16) ? c1 : c0)};
        st_sc1_x2_if(fg, stb, (u32)(tt * 2048 + fg * 8 + (l & 31) * 64), gr);
      }
      SCHED0();
      if constexpr (s % 12 == 11) { ST(2 + s / 12); }
    });
  }
  STR(28);
  Frag wf[4];
  ldsp fb = nullptr, fb2 = nullptr;
  auto fetch = [&](auto ic, int hi) {
    constexpr int i = ic.value, p = i / 36, s = i % 36;
    if constexpr (p != 1) {
      constexpr int c = (p == 0) ? 3 : p - 2;
      constexpr int pos = (p == 0) ? (s + 28) % 36 : s;
      if constexpr (s == 0 || (pos & 15) == 0) { if (!hi) fb = abase(c, (pos & ~15) * 4096); }
      constexpr int o = ((pos & 15) >> 1) * 8192 + (pos & 1) * 2048;
      if (hi) wf[i % 3].hi = tr_half(fb + (o + 512)); else wf[i % 3].lo = tr_half(fb + o);
    } else {
      if constexpr (s == 0 || s == 8) {
        if (!hi) {
          const u32 l2 = opq(tid);
          const int qo = (l2 >> 4) & 1;
          const int qb = (8 * ((l2 >> 4) & 3) + ((l2 >> 2) & 3)) * 32 + (l2 & 3) * 8 + (s == 0 ? G1_QE : G1_QL);
          fb = opaque(LP + (qb + qo * 128)); fb2 = opaque(LP + (qb + (1 - qo) * 128));
        }
      }
      constexpr int o = (s < 8 ? s : s - 8) * 1024;
      if (hi) wf[i % 3].hi = tr_half(fb2 + o); else wf[i % 3].lo = tr_half(fb + o);
    }
  };
  fetch(IC<0>{}, 0); fetch(IC<0>{}, 1); fetch(IC<1>{}, 0); fetch(IC<1>{}, 1);
  SCHED0();
  wait0_x6(sg4, sg2);
#pragma unroll 1
  for (int it = 0; it < (1 << 16); ++it) {
    const bool bad = (sg4[0] & sg4[1]) == 0xffffffffu || (sg4[2] & sg4[3]) == 0xffffffffu || (sg2[0] & sg2[1]) == 0xffffffffu;
    if (!__any(bad)) break;
    __builtin_amdgcn_s_sleep(2);
    const u32 so = tt * 2048 + ((rq >> 1) * 16 + (l & 15)) * 64 + (rq & 1) * 24;
    ld_sc1_x4(sg4, so, stb); ld_sc1_x2<16>(sg2, so, stb);
    wait0_x6(sg4, sg2);
  }
  float nmu[2], k1[2], k2[2], kd[2];
  {
    const float t1 = swap16_sum(__uint_as_float(sg4[0]) + __uint_as_float(sg4[2]) + __uint_as_float(sg2[0]));
    const float t2 = swap16_sum(__uint_as_float(sg4[1]) + __uint_as_float(sg4[3]) + __uint_as_float(sg2[1]));
    const float mu = hide(t1 * (1.f / D_));
    const float var = fmaxf(t2 * (1.f / D_) - mu * mu, 0.f) + 1e-5f;
    const auto m2 = __builtin_amdgcn_permlane32_swap(__float_as_uint(-mu), __float_as_uint(-mu), false, false);
    const auto v2 = __builtin_amdgcn_permlane32_swap(__float_as_uint(var), __float_as_uint(var), false, false);
#pragma unroll
    for (int tl = 0; tl < 2; ++tl) {
      kd[tl] = __uint_as_float(v2[tl]);
      const float r = __builtin_amdgcn_rsqf(kd[tl]);
      nmu[tl] = __uint_as_float(m2[tl]);
      k1[tl] = hide(GC * r);
      k2[tl] = hide(k1[tl] * 0.044715f * r * r);
    }
  }
  char* hdst = ws + OFF_HH + ((size_t)tt * 288 + (fg >> 1) * 9 + (fg & 1) * 4) * 1024;
  const ldsp csb = opaque(LP + (G1_CS + rq * 32));
  f32x4 cc;
  auto ldcs = [&](int lc, int hfx) { cc = *(const LAS f32x4*)(csb + (lc * 128 + hfx * 16)); };
  u32x4 eo[2];
  u32x2 eq[2];
  float m0, m1, tu0, tu1, y0, y1;
  auto emit = [&](int lc, int i, u32 pk) {
    const int tl = i & 1, hx = i >> 1;
    if (lc == 8) {
      eq[tl][hx] = pk;
      if (hx) st_sc1_x2(hdst + (size_t)tl * 147456 + (8 - (fg & 1) * 4) * 1024 + (fg & 1) * 8, lo16, eq[tl]);
    } else {
      eo[tl][(lc & 1) * 2 + hx] = pk;
      if ((lc & 1) && hx) st_sc1_x4(hdst + (size_t)tl * 147456 + (lc >> 1) * 1024, lo16, eo[tl]);
    }
  };
  auto epi = [&](const Acc& a, int lc, int st, int nlc, int part) {
    const int i = st / 6, sg = st % 6, tl = i & 1, e = (i >> 1) * 2;
    if (sg == 0) {
      if (part == 0) { if (i > 0) { u32 pk = pack2(tu0, tu1 * y1); pin(pk); emit(lc, i - 1, pk); } }
      else if (i < 4) { m0 = fmaf(nmu[tl], cc[0], a.a[0][tl][e]); m1 = fmaf(nmu[tl], cc[1], a.a[0][tl][e + 1]); pinf(m0); pinf(m1); }
    } else if (sg == 1) {
      if (part == 0) { tu0 = fmaf(nmu[tl], cc[2], a.a[1][tl][e]); tu1 = fmaf(nmu[tl], cc[3], a.a[1][tl][e + 1]); pinf(tu0); pinf(tu1); }
      else {
        y0 = hide(m0 * m0); y1 = m1 * m1; pinf(y0); pinf(y1);
        if (i == 1) ldcs(lc, 1);
        if (i == 3 && nlc >= 0) ldcs(nlc, 0);
      }
    } else if (sg == 2) {
      if (part == 0) { tu0 = hide(tu0 * m0); tu1 = tu1 * m1; pinf(tu0); pinf(tu1); }
      else { y0 = hide(fmaf(y0, k2[tl], k1[tl])); y1 = fmaf(y1, k2[tl], k1[tl]); pinf(y0); pinf(y1); }
    } else if (sg == 3) {
      if (part == 0) { y0 = hide(y0 * m0); y1 = y1 * m1; pinf(y0); pinf(y1); }
      else { y0 = __builtin_amdgcn_exp2f(y0); pinf(y0); }
    } else if (sg == 4) {
      if (part == 0) { y1 = __builtin_amdgcn_exp2f(y1); pinf(y1); }
      else { y0 = fmaf(y0, kd[tl], kd[tl]); pinf(y0); }
    } else {
      if (part == 0) { y0 = __builtin_amdgcn_rcpf(y0); y1 = fmaf(y1, kd[tl], kd[tl]); pinf(y0); pinf(y1); }
      else { y1 = __builtin_amdgcn_rcpf(y1); tu0 = tu0 * y0; pinf(y1); pinf(tu0); }
    }
  };
  const int r5 = b >> 3;
  const int ks = b & 3, tg2 = ((b >> 2) & 1) * 8 + (r5 & 7), dt = r5 >> 3;
  const int tq = w & 3, kh = w >> 2;
  constexpr u32 RD = D_ * 2;
  const u32 d_dst = L0 + kh * 9216 + tq * 2048;
  const int mb = ks * 4 + kh * 2;
  const u32 rowA = mb * 256 * RD, rowQ = (4096 + mb * 32) * RD;
  auto row0 = [&](int kk) { const int q = kk / 9, i = kk - 9 * q; return i < 8 ? rowA + (q * 256 + i * 32) * RD : rowQ + q * 32 * RD; };
  u32x4 hb[HB][2];
  u32 hv0 = 0, hv1 = 0;
  const char* myflag = ws + OFF_FLG + 16384 + b * 64;
  const char* pflags = ws + OFF_FLG + 16384 + ((b & 7) + kh * 32 + ((r5 & 7) >> 1) * 64) * 64;
  u32 d_voff = 0, dq_voff = 0, p_voff = 0, pv = 0, q_voff = 0;
  f32x4 ac2[9][2];
  ldsp pb[5];
  u32 rb2 = 0, rbq = 0;
  auto fetch2 = [&](auto ic, int hi) {
    constexpr int i = ic.value, s = i / 9, c = i % 9, stg = s % NST;
    if constexpr (stg % 3 == 0 && c < 4) { if (!hi) pb[c] = opaque((ldsp)(uintptr_t)((opq(rb2) ^ (u32)(c * 32)) + (u32)(stg * STG))); }
    if constexpr (c == 8 && (stg % 3 == 0 || s == 4)) { if (!hi) pb[4] = opaque((ldsp)(uintptr_t)(opq(rbq) + (s < 4 ? (u32)(G1_QE - 8192) - kh * (9216 - 1024) : (u32)((stg - stg % 3) * STG)))); }
    constexpr int o = c < 8 ? (stg % 3) * STG + (c >> 2) * 1024 : (s < 4 ? s * 2048 : (stg % 3) * STG);
    if (hi) wf[i & 3].hi = tr_half(pb[c < 8 ? (c & 3) : 4] + (o + (c < 8 ? 4096 : 512))); else wf[i & 3].lo = tr_half(pb[c < 8 ? (c & 3) : 4] + o);
  };
  auto issue2 = [&](auto uc) {
    constexpr int u = uc.value;
    constexpr int nw0 = find_at(18, u, [](int n) { return w_issue(n, 0); }), nw1 = find_at(18, u, [](int n) { return w_issue(n, 1); });
    constexpr int nh0 = find_at(18, u, [](int n) { return h_issue(n, 0); }), nh1 = find_at(18, u, [](int n) { return h_issue(n, 1); });
    constexpr int r = find_at(5, u, [](int n) { return dq_issue(n); });
    constexpr int pi = find_at(3, u, [](int n) { return poll_iss(n); }), pc = find_at(3, u, [](int n) { return poll_chk(n); });
    if constexpr (nw0 >= 0) bdma<(nw0 % NST) * STG>(rD, d_voff, row0(kk_of(nw0)), d_dst);
    if constexpr (nw1 >= 0) bdma<(nw1 % NST) * STG + 1024>(rD, d_voff, row0(kk_of(nw1)) + 128, d_dst);
    if constexpr (nh0 >= 0) HLOAD<(kk_of(nh0) & 3) * 1024>(hb[nh0 % HB][0], rS, hv0, (kk_of(nh0) >> 2) * 4096);
    if constexpr (nh1 >= 0) HLOAD<(kk_of(nh1) & 3) * 1024>(hb[nh1 % HB][1], rS, hv1, (kk_of(nh1) >> 2) * 4096);
    if constexpr (r >= 0) {
      const int tsel = r == 4 ? (tq & 1) : tq;
      const u32 ra = row0(kk_of(4 * r)), rb = row0(kk_of(4 * r + 1)), rc_ = row0(kk_of(r == 4 ? 16 : 4 * r + 2)), rd = row0(kk_of(r == 4 ? 17 : 4 * r + 3));
      const u32 rsel = (tsel & 2) ? ((tsel & 1) ? rd : rc_) : ((tsel & 1) ? rb : ra);
      bdma<0>(rD, dq_voff, rsel, r == 0 ? L0 + G1_QE + (tq * 2 + kh) * 1024 : L0 + (((4 * r) % NST) + tsel) * STG + kh * 9216 + 8192);
    }
    if constexpr (pi >= 0) poll_issue(pv, p_voff, pflags);
    if constexpr (pc >= 0) poll_check<wait_k(4 * poll_iss(pc) + 2, 4 * u + 2), pc + 1>(pv, p_voff, pflags);
  };
  static_for<0, 216>([&](auto ic) {
    constexpr int t = ic.value, p = t / 36, s = t % 36;
    constexpr int slot = (p + 3) & 3;
    constexpr int hg = (p == 0) ? ((s >> 1) + 14) % 18 : (s >> 1);
    constexpr int ci = (t - G1_E0) / 26, st = (t - G1_E0) % 26;
    constexpr int lc = ci < 4 ? ci : (ci == 4 ? 8 : ci - 1), nlc = ci < 3 ? ci + 1 : (ci == 3 ? 8 : ci);
#pragma unroll
    for (int tl = 0; tl < 2; ++tl) {
      mma<(s < 2)>(acc[slot].a[s & 1][tl], frag_of(wf[t % 3]), h[tl][hg]);
      SCHED0();
      if constexpr (t + 2 < 216) fetch(IC<t + 2>{}, tl);
      SCHED0();
      if constexpr (t >= G1_E0 && ci < 8 && st <= 24) epi(acc[ci & 3], lc, st, nlc, tl);
      SCHED0();
    }
    if constexpr (t == 2) {
      const u32 pi = min(opq(tid), 71u);
      const ldsp cp = LP + (G1_CP + pi * 16);
      const f32x4 r = (*(const LAS f32x4*)cp + *(const LAS f32x4*)(cp + 1152)) + (*(const LAS f32x4*)(cp + 2304) + *(const LAS f32x4*)(cp + 3456));
      *(LAS f32x4*)(LP + (G1_CS + pi * 16)) = r;
    }
    if constexpr (t == G1_E0 - 1) ldcs(0, 0);
    if constexpr (t == 1 || t == 7 || t == 28 || t == 72 || t == 190) { SCHED0(); BARRIER(); }
    if constexpr (t == 41) { wait_vm<wait_q(1, 41)>(); BARRIER(); }
    if constexpr (t == 57) { wait_vm<wait_q(3, 57)>(); BARRIER(); }
    if constexpr (t == 69) { wait_vm<wait_b(9, 69)>(); BARRIER(); }
    if constexpr (t == 89) { wait_vm<wait_b(14, 89)>(); BARRIER(); }
    if constexpr (t == 99) { wait_vm<wait_b(17, 99)>(); BARRIER(); }
    if constexpr (t == 160) { wait_vm<wait_k(4 * (epi_start(4) + 24), 4 * t + 1)>(); BARRIER(); flag_store(w, opq(0u), opq(1u), myflag); }
    if constexpr (t == 198) { SCHED0(); BARRIER(); }
    if constexpr (t == 208) { SCHED0(); wait_vm<wait_k(4 * w_issue(1, 1) + 2, 4 * t + 1)>(); BARRIER(); flag_store(w, opq(0u), opq(2u), myflag); }
    if constexpr (t == 7) q_voff = mk_qvoff(opq(tid) & 63);
    if constexpr (t == 179) {
      const u32 l2 = opq(tid) & 63;
      d_voff = (u32)((8 * tq + (l2 >> 3)) * RD + ((((l2 & 7) >> 1) ^ ((l2 >> 4) & 3)) * 32) + (l2 & 1) * 16 + dt * 256);
      dq_voff = (u32)((l2 >> 1) * RD + (l2 & 1) * 16 + 1024 + dt * 32);
      p_voff = (l2 & 3) * 512;
      hv0 = l2 * 16 + (u32)OFF_HH + ((tg2 * 8 + tq * 2 + kh) * 144 + mb * 9) * 1024;
      hv1 = l2 * 16 + (u32)OFF_HH + ((tg2 * 8 + tq * 2 + (kh ^ 1)) * 144 + mb * 9) * 1024;
      pin(d_voff); pin(dq_voff); pin(p_voff); pin(hv0); pin(hv1);
    }
    {
      constexpr int r = find_at(4, t, [](int n) { return g1_q_issue(0, n); }), j = find_at(18, t, [](int n) { return g1_b_issue(0, n); });
      if constexpr (r >= 0) bdma<G1_QL + r * 8192>(rW, q_voff, (4 + 4 * r) * GS, lw);
      if constexpr (j >= 0) bdma<j * 8192>(rW, ab_voff, j * GS + 128, ab_dst);
    }
    if constexpr (t >= PF_ISS0 && t < PF_ISS0 + 20 && (t - PF_ISS0) % 4 == 0) {
      const int piece = ((b >> 3) * 5 + (t - PF_ISS0) / 4) * 8 + w;
      const int byte = piece * 1024;
      bdma<G1_QE>(rD, lo16, (u32)(byte < 1024 * 1152 ? (b & 3) * 1024 * 1152 + byte : 4096 * 1152 + (b & 3) * 128 * 1152 + (byte - 1024 * 1152)), lw);
    }
    if constexpr (t == 3 || t == 102) code_touch(code0, min(opq((u32)tid) * 64u + (t == 3 ? 8192u : 18432u), codelen - 4u), L0 + FF_LDS - 256);
    if constexpr (t >= 180) issue2(ic);
    SCHED0();
    if constexpr (t % 24 == 23) { ST(5 + t / 24); }
  });
  STR(29);
  {
    const u32 l2 = opq(tid) & 63;
    const u32 q2 = l2 >> 4, r2_ = (l2 >> 2) & 3, p2 = l2 & 3;
    rb2 = L0 + kh * 9216 + (q2 >> 1) * 2048 + (4 * (q2 & 1) + r2_) * 128 + ((2 * (q2 & 1) + (r2_ >> 1)) * 32) + p2 * 8;
    rbq = L0 + kh * 9216 + 8192 + (4 * q2 + r2_) * 32 + p2 * 8;
  }
  const int ft = tg2 * 8 + ks * 2 + kh;
  const int dcol = dt * 128 + tq * 32 + 4 * rq;
  const int qcol = 512 + dt * 16 + 4 * rq;
  u32x2 r0, r1, r2;
  static_for<0, 3>([&](auto ic) { fetch2(ic, 0); fetch2(ic, 1); });
  wait_vm2<wait_k(4 * h_issue(0, 1) + 2, 4 * U2 - 1)>(hb[0][0], hb[0][1]);
  static_for<0, 162>([&](auto ic) {
    constexpr int i = ic.value, s = i / 9, c = i % 9, u = U2 + i;
    if constexpr (c == ((s & 1) ? 6 : 4) && s < 17) {
      constexpr int s2 = cmin(s + 2, 17);
      constexpr int tgt = (s & 1) ? 4 * h_issue(s + 1, 1) + 2
                                  : cmax(cmax(cmax(4 * h_issue(s + 1, 1) + 2, 4 * w_issue(s2, 1) + 2), 4 * dq_issue((s + 1) >> 2) + 2), s == 4 ? 4 * (U2 + 14) : 0);
      wait_vm2<wait_k(tgt, 4 * u - 1)>(hb[(s + 1) % HB][0], hb[(s + 1) % HB][1]);
      if constexpr ((s & 1) == 0 || G2_BAR_ALL) BARRIER();
      if constexpr (s == 4) flag_store(w, opq(0u), opq(3u), myflag);
    }
    SCHED0();
#pragma unroll
    for (int tl = 0; tl < 2; ++tl) {
      mma<s == 0>(ac2[c][tl], frag_of(wf[i & 3]), hb[s % HB][tl]);
      SCHED0();
      if constexpr (i + 3 < 162) fetch2(IC<i + 3>{}, tl);
      SCHED0();
      if constexpr (i >= 2 && i <= 14) { if (i < 14 || tl == 0) { epi(acc[0], 7, 2 * (i - 2) + tl, -1, 0); epi(acc[0], 7, 2 * (i - 2) + tl, -1, 1); } }
      SCHED0();
    }
    issue2(IC<u>{});
    if constexpr (u == RES_ISS) {
      const char* hb_ = ws + OFF_HF + (size_t)(ft >> 1) * 36864 + (ft & 1) * 8;
      auto po = [&](int d) { return (u32)((d >> 5) * 2048 + ((d >> 2) & 1) * 1024 + ((d >> 3) & 3) * 256 + (l & 15) * 16); };
      gload8(r0, po(dcol), hb_); gload8(r1, po(dcol + 16), hb_); gload8(r2, po(qcol), hb_);
    }
    SCHED0();
    if constexpr (c == 8 && s % 3 == 2) { ST(14 + s / 3); }
  });
  MFMA_DRAIN(); SCHED0();
  {
    const ldsp xw = LP + (2 * STG + w * 9216 + l * 16);
#pragma unroll
    for (int c = 0; c < 9; ++c) *(LAS f32x4*)(xw + c * 1024) = ac2[c][1];
  }
  LDS_BARRIER();
  f32x4 sm[9];
  {
    const ldsp xr = LP + (2 * STG + (w ^ 4) * 9216 + l * 16);
#pragma unroll
    for (int c = 0; c < 9; ++c) sm[c] = ac2[c][0] + *(const LAS f32x4*)(xr + c * 1024);
  }
  ST(20);
  char* part = ws + OFF_PART;
  if (tq != ks) {
    char* pdst = part + ((((size_t)(tg2 * 4 + dt) * 4 + tq) * 4 + ks) * 2 + kh) * 4608;
#pragma unroll
    for (int pc = 0; pc < 4; ++pc) {
      u32x4 v = {pack2(sm[2 * pc][0], sm[2 * pc][1]), pack2(sm[2 * pc][2], sm[2 * pc][3]), pack2(sm[2 * pc + 1][0], sm[2 * pc + 1][1]), pack2(sm[2 * pc + 1][2], sm[2 * pc + 1][3])};
      st_sc1_x4(pdst + pc * 1024, lo16, v);
    }
    u32x2 v2 = {pack2(sm[8][0], sm[8][1]), pack2(sm[8][2], sm[8][3])};
    st_sc1_x2(pdst + 4096, l * 8, v2);
  } else {
    const ldsp ow = LP + (kh * 9216 + l * 16);
#pragma unroll
    for (int c = 0; c < 9; ++c) *(LAS f32x4*)(ow + c * 1024) = sm[c];
  }
  asm volatile("s_waitcnt vmcnt(0)" : "+v"(r0), "+v"(r1), "+v"(r2)::"memory");
  LDS_BARRIER();
  ST(21);
  const char* fl = ws + OFF_FLG + (tg2 * 4 + dt) * 256;
  flag_store(w, opq(0u), opq(1u), fl + ks * 64);
  const ldsp orp = LP + (kh * 9216 + l * 16);
  f32x4 o0 = *(const LAS f32x4*)(orp + tq * 2048), o1 = *(const LAS f32x4*)(orp + (tq * 2048 + 1024)), o2 = *(const LAS f32x4*)(orp + 8192);
  o0[0] += bflo(r0[0]); o0[1] += bfhi(r0[0]); o0[2] += bflo(r0[1]); o0[3] += bfhi(r0[1]);
  o1[0] += bflo(r1[0]); o1[1] += bfhi(r1[0]); o1[2] += bflo(r1[1]); o1[3] += bfhi(r1[1]);
  o2[0] += bflo(r2[0]); o2[1] += bfhi(r2[0]); o2[2] += bflo(r2[1]); o2[3] += bfhi(r2[1]);
  {
    const u32 fo = (l & 3) * 64;
    poll_issue(pv, fo, fl);
    poll_check<0, 1>(pv, fo, fl);
  }
  ST(22);
  {
    const char* psrc = part + (((size_t)(tg2 * 4 + dt) * 4 + ks) * 4 * 2 + kh) * 4608;
    u32x4 v[3]; u32x2 vq[3];
#pragma unroll
    for (int j = 0; j < 3; ++j) {
      const int src = (ks + 1 + j) & 3;
      ld_sc1_x4(v[j], lo16 + tq * 1024, psrc + src * 9216);
      ld_sc1_x2<0>(vq[j], l * 8 + 4096, psrc + src * 9216);
    }
    asm volatile("s_waitcnt vmcnt(0)" : "+v"(v[0]), "+v"(v[1]), "+v"(v[2]), "+v"(vq[0]), "+v"(vq[1]), "+v"(vq[2])::"memory");
#pragma unroll
    for (int j = 0; j < 3; ++j) {
      o0[0] += bflo(v[j][0]); o0[1] += bfhi(v[j][0]); o0[2] += bflo(v[j][1]); o0[3] += bfhi(v[j][1]);
      o1[0] += bflo(v[j][2]); o1[1] += bfhi(v[j][2]); o1[2] += bflo(v[j][3]); o1[3] += bfhi(v[j][3]);
      o2[0] += bflo(vq[j][0]); o2[1] += bfhi(vq[j][0]); o2[2] += bflo(vq[j][1]); o2[3] += bfhi(vq[j][1]);
    }
    u16* op = out + (size_t)(ft * 16 + (l & 15)) * D_;
    u32x2 oa = {pack2(o0[0], o0[1]), pack2(o0[2], o0[3])}, ob = {pack2(o1[0], o1[1]), pack2(o1[2], o1[3])}, oc = {pack2(o2[0], o2[1]), pack2(o2[2], o2[3])};
    *(u32x2*)(op + dcol) = oa; *(u32x2*)(op + dcol + 16) = ob;
    if (tq == 0) *(u32x2*)(op + qcol) = oc;
  }
  asm volatile(".Lffn_code_end:");
  ST(23); ST_DRAIN(); ST(24);
  ST_FLUSH((u32*)(ws + (200u << 20)), b * 8 + w);
}

extern "C" void kernel_launch(void* const* d_in, const int* in_sizes, int n_in, void* d_out, int out_size, void* d_ws, size_t ws_size,
                              hipStream_t stream) {
  const u16* q = (const u16*)d_in[0]; const u16* k = (const u16*)d_in[1]; const u16* v = (const u16*)d_in[2];
  const u16* shk = (const u16*)d_in[3]; const u16* ssm = (const u16*)d_in[4]; const u16* ssmD = (const u16*)d_in[5];
  const u16* wg = (const u16*)d_in[7]; const u16* wu = (const u16*)d_in[8]; const u16* wd = (const u16*)d_in[9];
  char* ws = (char*)d_ws;
  (void)hipFuncSetAttribute((const void*)k1_conv, hipFuncAttributeMaxDynamicSharedMemorySize, K1_LDS);
  (void)hipFuncSetAttribute((const void*)ffn_fused, hipFuncAttributeMaxDynamicSharedMemorySize, FF_LDS);
  k0_prep<<<K0_GRID, 256, 0, stream>>>(q, k, v, shk, ssm, ws);
  k1_conv<<<K1_GRID, 512, K1_LDS, stream>>>(ssm, ssmD, wg, wu, ws);
  ffn_fused<<<256, 512, FF_LDS, stream>>>(wg, wu, wd, ws, (u16*)d_out);
}
```

```cpp
#include <hip/hip_runtime.h>
#include <hip/hip_bf16.h>
#include <stdint.h>
#include <utility>
#define G2_BAR_ALL 0
#define WE0 7
#define WE1 1
#define HD2 4
#define LASTX 2
#define HH_PLAIN 0
#define HLOAD bload_sc1
#define NT0 3
#define LDK(p) ((NT0 & 1) ? __builtin_nontemporal_load(p) : *(p))
#define LDV(p) ((NT0 & 2) ? __builtin_nontemporal_load(p) : *(p))
#define LDQ(p) ((NT0 & 4) ? __builtin_nontemporal_load(p) : *(p))
#define SIDE_DELAY 5

typedef unsigned short u16;
typedef unsigned int u32;
typedef __attribute__((ext_vector_type(2))) unsigned u32x2;
typedef __attribute__((ext_vector_type(4))) unsigned u32x4;
typedef __attribute__((ext_vector_type(2))) float f32x2;
typedef __attribute__((ext_vector_type(4))) float f32x4;
typedef __attribute__((ext_vector_type(8))) __bf16 bf16x8;
typedef __attribute__((ext_vector_type(2))) __bf16 bf16x2;
typedef __attribute__((ext_vector_type(4))) short s16x4;
#define GAS __attribute__((address_space(1)))
#define LAS __attribute__((address_space(3)))
#define DI __device__ __forceinline__
typedef LAS char* ldsp;

constexpr int S_ = 2048, D_ = 576, DF_ = 4608;
constexpr size_t OFF_KVT = 0;
constexpr size_t OFF_XP  = OFF_KVT + (size_t)D_ * S_ * 2;
constexpr size_t OFF_HF  = OFF_XP + (size_t)D_ * S_ * 2;
constexpr size_t OFF_ST1 = OFF_HF + (size_t)D_ * S_ * 2;
constexpr size_t OFF_CS  = OFF_ST1 + 2048 * 8;
constexpr size_t OFF_FLG = OFF_CS + 4 * 2 * 4608 * 4;
constexpr size_t OFF_ST2 = OFF_FLG + 36864;
constexpr size_t OFF_HH  = OFF_ST2 + 131072;
constexpr size_t OFF_PART = OFF_HH + (size_t)S_ * DF_ * 2;
constexpr size_t WS_USED = OFF_PART + 64 * 4 * 4 * 2 * 4608;

DI float bflo(u32 w) { return __uint_as_float(w << 16); }
DI float bfhi(u32 w) { return __uint_as_float(w & 0xffff0000u); }
DI u32 pack2(float a, float b) { bf16x2 p; p[0] = (__bf16)a; p[1] = (__bf16)b; return __builtin_bit_cast(u32, p); }

template <int I> struct IC { static constexpr int value = I; };
template <int... I> struct Seq {};
template <int B, class F, int... I> DI void static_for_impl(F&& f, std::integer_sequence<int, I...>) { (f(IC<B + I>{}), ...); }
template <int B, int E, class F> DI void static_for(F&& f) { static_for_impl<B>(f, std::make_integer_sequence<int, E - B>{}); }
template <int N> DI void wait_vm() { asm volatile("s_waitcnt vmcnt(%0)" ::"n"(N) : "memory"); }
#define BARRIER() do { asm volatile("" ::: "memory"); __builtin_amdgcn_s_barrier(); asm volatile("" ::: "memory"); } while (0)
#define LDS_BARRIER() do { asm volatile("s_waitcnt lgkmcnt(0)" ::: "memory"); __builtin_amdgcn_s_barrier(); asm volatile("" ::: "memory"); } while (0)
#define SCHED0() __builtin_amdgcn_sched_barrier(0)
constexpr int cmin(int a, int b) { return a < b ? a : b; }

DI u32x4 tr_frag(ldsp a0, ldsp a1) {
  const s16x4 lo = __builtin_amdgcn_ds_read_tr16_b64_v4i16((LAS s16x4*)a0);
  const s16x4 hi = __builtin_amdgcn_ds_read_tr16_b64_v4i16((LAS s16x4*)a1);
  return __builtin_bit_cast(u32x4, __builtin_shufflevector(lo, hi, 0, 1, 2, 3, 4, 5, 6, 7));
}
struct Frag { s16x4 lo, hi; };
DI s16x4 tr_half(ldsp a) { return __builtin_amdgcn_ds_read_tr16_b64_v4i16((LAS s16x4*)a); }
DI u32x4 frag_of(const Frag& f) { return __builtin_bit_cast(u32x4, __builtin_shufflevector(f.lo, f.hi, 0, 1, 2, 3, 4, 5, 6, 7)); }
DI f32x4 mfma16(const u32x4& a, const u32x4& b, const f32x4& c) {
  return __builtin_amdgcn_mfma_f32_16x16x32_bf16(__builtin_bit_cast(bf16x8, a), __builtin_bit_cast(bf16x8, b), c, 0, 0, 0);
}
template <bool FIRST> DI void mma(f32x4& c, const u32x4& a, const u32x4& b) {
  if constexpr (FIRST) asm volatile("v_mfma_f32_16x16x32_bf16 %0, %1, %2, 0" : "=&v"(c) : "v"(a), "v"(b));
  else asm volatile("v_mfma_f32_16x16x32_bf16 %0, %1, %2, %0" : "+v"(c) : "v"(a), "v"(b));
}
#define MFMA_DRAIN() asm volatile("s_nop 15" ::: "memory")
template <int OFF> DI void gload16_sc1(u32x4& dst, u32 voff, const char* sbase) {
  asm volatile("global_load_dwordx4 %0, %1, %2 offset:%3 sc1" : "=v"(dst) : "v"(voff), "s"(sbase), "n"(OFF) : "memory");
}
DI void poll_issue(u32& f, u32 voff, const char* sb) { asm volatile("global_load_dword %0, %1, %2 sc1" : "=v"(f) : "v"(voff), "s"(sb) : "memory"); }
template <int N, int PH> DI void poll_check(u32& f, u32 voff, const char* sb) {
  u32 cnt = 1u << 18;
  asm volatile("s_waitcnt vmcnt(%4)\n"
               "1:\n\tv_cmp_gt_u32_e32 vcc, %5, %0\n\ts_nop 4\n\ts_cbranch_vccz 2f\n\t"
               "s_sleep 1\n\tglobal_load_dword %0, %2, %3 sc1\n\ts_waitcnt vmcnt(0)\n\t"
               "s_sub_u32 %1, %1, 1\n\ts_cbranch_scc0 1b\n"
               "2:" : "+v"(f), "+s"(cnt) : "v"(voff), "s"(sb), "n"(N), "n"(PH) : "memory", "vcc", "scc");
}
DI void poll4(u32 voff, const char* sb) {
  u32 cnt = 1u << 18;
  u32x4 f; u32 m;
  asm volatile("1:\n\tglobal_load_dwordx4 %0, %3, %4 sc1\n\ts_waitcnt vmcnt(0)\n\t"
               "v_min3_u32 %1, v100, v101, v102\n\tv_min_u32_e32 %1, %1, v103\n\tv_cmp_eq_u32_e32 vcc, 0, %1\n\ts_nop 4\n\ts_cbranch_vccz 2f\n\t"
               "s_sleep 4\n\ts_sub_u32 %2, %2, 1\n\ts_cbranch_scc0 1b\n"
               "2:" : "=&{v[100:103]}"(f), "=&v"(m), "+s"(cnt) : "v"(voff), "s"(sb) : "memory", "vcc", "scc");
}
DI void swap_halves(u32x4& x, u32x4& y) {
  u32 a = x[2], b = x[3], c = y[0], d = y[1];
  asm volatile("v_swap_b32 %0, %2\n\tv_swap_b32 %1, %3" : "+v"(a), "+v"(b), "+v"(c), "+v"(d));
  x[2] = a; x[3] = b; y[0] = c; y[1] = d;
}
DI void flag_store(int w, u32 zero_off, u32 val, const char* sb) {
  asm volatile("s_cmp_lg_u32 %3, 0\n\ts_cbranch_scc1 1f\n\tglobal_store_dword %0, %1, %2 sc1\n1:" ::"v"(zero_off), "v"(val), "s"(sb), "s"(w) : "memory", "scc");
}
template <int OFF> DI void gload16(u32x4& dst, u32 voff, const char* sbase) {
  asm volatile("global_load_dwordx4 %0, %1, %2 offset:%3" : "=v"(dst) : "v"(voff), "s"(sbase), "n"(OFF) : "memory");
}
DI void gload8(u32x2& dst, u32 voff, const char* sbase) { asm volatile("global_load_dwordx2 %0, %1, %2" : "=v"(dst) : "v"(voff), "s"(sbase) : "memory"); }
template <int N, int VAL> DI void waitbar_if(int sel, u32x4& a, u32x4& b) {
  asm volatile("s_cmp_lg_u32 %2, %4\n\ts_cbranch_scc1 1f\n\ts_waitcnt vmcnt(%3)\n\ts_barrier\n1:" : "+v"(a), "+v"(b) : "s"(sel), "n"(N), "n"(VAL) : "memory", "scc");
}
template <int N> DI void wait_vm2(u32x4& a, u32x4& b) { asm volatile("s_waitcnt vmcnt(%2)" : "+v"(a), "+v"(b) : "n"(N) : "memory"); }
DI void dma16(const char* sbase, u32 voff, u32 lds_dst) {
  asm volatile("s_mov_b32 m0, %2\n\ts_nop 0\n\tglobal_load_lds_dwordx4 %0, %1" ::"v"(voff), "s"(sbase), "s"(lds_dst) : "memory");
}
template <int VAL> DI void dma16_if(int sel, const char* sbase, u32 voff, u32 lds_dst) {
  asm volatile("s_cmp_eq_u32 %3, %4\n\ts_mov_b32 m0, %2\n\ts_cselect_b64 exec, -1, 0\n\tglobal_load_lds_dwordx4 %0, %1\n\ts_mov_b64 exec, -1" ::"v"(voff), "s"(sbase), "s"(lds_dst), "s"(sel), "n"(VAL) : "memory", "scc");
}
typedef __amdgpu_buffer_rsrc_t rsrc_t;
DI rsrc_t mk_rsrc(const void* p, u32 bytes) { return __builtin_amdgcn_make_buffer_rsrc((void*)p, (short)0, (int)bytes, 0x00020000); }
template <int LOFF> DI void bdma(rsrc_t r, u32 voff, u32 soff, u32 ldsb) {
  asm volatile("s_add_u32 m0, %3, %4\n\ts_nop 0\n\tbuffer_load_dwordx4 %0, %1, %2 offen lds" ::"v"(voff), "s"(r), "s"(soff), "s"(ldsb), "n"(LOFF) : "memory", "scc");
}
template <int LOFF, u32 SOFF> DI void bdma_i(rsrc_t r, u32 voff, u32 ldsb) {
  u32 so;
  asm volatile("s_add_u32 m0, %3, %4\n\ts_mov_b32 %0, %5\n\tbuffer_load_dwordx4 %1, %2, %0 offen lds" : "=&s"(so) : "v"(voff), "s"(r), "s"(ldsb), "n"(LOFF), "n"(SOFF) : "memory", "scc");
}
template <int LOFF, u32 SOFF> DI void bdma_a(rsrc_t r, u32 voff, u32 sbase, u32 ldsb) {
  u32 so;
  asm volatile("s_add_u32 m0, %3, %4\n\ts_add_u32 %0, %6, %5\n\tbuffer_load_dwordx4 %1, %2, %0 offen lds" : "=&s"(so) : "v"(voff), "s"(r), "s"(ldsb), "n"(LOFF), "n"(SOFF), "s"(sbase) : "memory", "scc");
}
template <int X> DI ldsp lds_base_t(u32 rb, u32 off) {
  u32 r;
  if constexpr (X == 96) { asm volatile("v_xor_b32_e32 %0, 0x60, %1\n\tv_add_u32_e32 %0, %2, %0" : "=&v"(r) : "v"(rb), "s"(off)); }
  else asm volatile("v_xad_u32 %0, %1, %2, %3" : "=v"(r) : "v"(rb), "n"(X), "s"(off));
  return (ldsp)(uintptr_t)r;
}
DI ldsp lds_base(u32 rb, int x, u32 off) { return x == 0 ? lds_base_t<0>(rb, off) : x == 32 ? lds_base_t<32>(rb, off) : x == 64 ? lds_base_t<64>(rb, off) : lds_base_t<96>(rb, off); }
DI void touch(const Frag& f) { asm volatile("" ::"v"(f.lo), "v"(f.hi)); }
template <int OFF> DI void bload(u32x4& d, rsrc_t r, u32 voff, u32 soff) {
  asm volatile("buffer_load_dwordx4 %0, %1, %2, %3 offen offset:%4" : "=v"(d) : "v"(voff), "s"(r), "s"(soff), "n"(OFF) : "memory");
}
template <int OFF> DI void bload_sc1(u32x4& d, rsrc_t r, u32 voff, u32 soff) {
  asm volatile("buffer_load_dwordx4 %0, %1, %2, %3 offen offset:%4 sc1" : "=v"(d) : "v"(voff), "s"(r), "s"(soff), "n"(OFF) : "memory");
}
DI void stat_loads(int fg, u32x4 (&x)[6], rsrc_t r, u32 voff, u32 soff, u32 soff2) {
  asm volatile("s_cmp_gt_u32 %10, 5\n\ts_cbranch_scc1 1f\n\t"
               "buffer_load_dwordx4 %0, %6, %7, %8 offen\n\tbuffer_load_dwordx4 %1, %6, %7, %8 offen offset:1024\n\t"
               "buffer_load_dwordx4 %2, %6, %7, %8 offen offset:2048\n\tbuffer_load_dwordx4 %3, %6, %7, %8 offen offset:3072\n\t"
               "buffer_load_dwordx4 %4, %6, %7, %9 offen\n\tbuffer_load_dwordx4 %5, %6, %7, %9 offen offset:1024\n1:"
               : "=&v"(x[0]), "=&v"(x[1]), "=&v"(x[2]), "=&v"(x[3]), "=&v"(x[4]), "=&v"(x[5])
               : "v"(voff), "s"(r), "s"(soff), "s"(soff2), "s"(fg) : "memory", "scc");
}
DI void code_touch(const char* base, u32 voff, u32 lds_dst) {
  asm volatile("s_mov_b32 m0, %2\n\ts_nop 0\n\tglobal_load_lds_dword %0, %1" ::"v"(voff), "s"(base), "s"(lds_dst) : "memory");
}
#define CODE_RANGE(base, len, endlabel) \
  asm volatile("s_getpc_b64 s[40:41]\n\ts_mov_b64 %0, s[40:41]\n\ts_mov_b32 %1, " endlabel "@rel32@lo-4" : "=s"(base), "=s"(len)::"s40", "s41")
DI ldsp opaque(ldsp p) { asm volatile("" : "+v"(p)); return p; }
DI u32 opq(u32 p) { asm volatile("" : "+v"(p)); return p; }
DI void pin(u32& p) { asm volatile("" : "+v"(p)); }
DI void pin6(u32x4 (&x)[6]) { asm volatile("" : "+v"(x[0]), "+v"(x[1]), "+v"(x[2]), "+v"(x[3]), "+v"(x[4]), "+v"(x[5])); }
DI void pinf(float& p) { asm volatile("" : "+v"(p)); }
DI float hide(float p) { asm("" : "+v"(p)); return p; }
DI float swap16_sum(float x) { const auto r = __builtin_amdgcn_permlane16_swap(__float_as_uint(x), __float_as_uint(x), false, false); return __uint_as_float(r[0]) + __uint_as_float(r[1]); }
DI float swap32_sum(float x) { const auto r = __builtin_amdgcn_permlane32_swap(__float_as_uint(x), __float_as_uint(x), false, false); return __uint_as_float(r[0]) + __uint_as_float(r[1]); }
#define STAT1(R, A, B) "v_lshlrev_b32_e32 %0, 16, " R "\n\tv_and_b32_e32 %1, %10, " R "\n\tv_add_f32_e32 " A ", " A ", %0\n\tv_fmac_f32_e32 " B ", %0, %0\n\tv_add_f32_e32 " A ", " A ", %1\n\tv_fmac_f32_e32 " B ", %1, %1\n\t"
DI void stat_acc(int fg, const u32x4& v, float (&s1)[2], float (&s2)[2]) {
  float x, y;
  asm volatile("s_cmp_gt_u32 %11, 5\n\ts_cbranch_scc1 1f\n\t" STAT1("%6", "%2", "%3") STAT1("%8", "%4", "%5") STAT1("%7", "%2", "%3") STAT1("%9", "%4", "%5") "\n1:"
               : "=&v"(x), "=&v"(y), "+v"(s1[0]), "+v"(s2[0]), "+v"(s1[1]), "+v"(s2[1]) : "v"(v[0]), "v"(v[1]), "v"(v[2]), "v"(v[3]), "s"(0xffff0000u), "s"(fg) : "scc");
}
DI void ld_sc1_x4(u32x4& d, u32 voff, const char* sb) { asm volatile("global_load_dwordx4 %0, %1, %2 sc1" : "=v"(d) : "v"(voff), "s"(sb) : "memory"); }
template <int OFF> DI void ld_sc1_x2(u32x2& d, u32 voff, const char* sb) { asm volatile("global_load_dwordx2 %0, %1, %2 offset:%3 sc1" : "=v"(d) : "v"(voff), "s"(sb), "n"(OFF) : "memory"); }
DI void wait0_x6(u32x4& a, u32x2& b) { asm volatile("s_waitcnt vmcnt(0)" : "+v"(a), "+v"(b)::"memory"); }
DI void st_sc1_x4(char* sb, u32 voff, const u32x4& v) { asm volatile("global_store_dwordx4 %0, %1, %2 sc1\n\ts_nop 1" ::"v"(voff), "v"(v), "s"(sb) : "memory"); }
DI void gstore16(char* sb, u32 voff, const u32x4& v) { asm volatile("global_store_dwordx4 %0, %1, %2\n\ts_nop 1" ::"v"(voff), "v"(v), "s"(sb) : "memory"); }
DI void gstore8(char* sb, u32 voff, const u32x2& v) { asm volatile("global_store_dwordx2 %0, %1, %2" ::"v"(voff), "v"(v), "s"(sb) : "memory"); }
DI void st_sc1_x2_if(int skip, const char* sb, u32 voff, const u32x2& v) {
  asm volatile("s_cmp_gt_u32 %3, 5\n\ts_cbranch_scc1 1f\n\tglobal_store_dwordx2 %0, %1, %2 sc1\n1:" ::"v"(voff), "v"(v), "s"(sb), "s"(skip) : "memory", "scc");
}
DI void st_sc1_x2(char* sb, u32 voff, const u32x2& v) { asm volatile("global_store_dwordx2 %0, %1, %2 sc1" ::"v"(voff), "v"(v), "s"(sb) : "memory"); }

#define STR(i)
#define ST(i)
#define ST_DECL
#define ST_FLUSH(dbg, wv)
#define ST_DRAIN()

DI void colsum_slice(const u16* __restrict__ wg, const u16* __restrict__ wu, float* __restrict__ cs, int sl, int l) {
  const int mat = sl & 1, qr = (sl >> 1) & 3, cb = sl >> 3;
  const u16* p = (mat ? wu : wg) + (size_t)(qr * 144 + (l >> 3)) * DF_ + cb * 64 + (l & 7) * 8;
  u32x4 x[18];
#pragma unroll
  for (int i = 0; i < 18; ++i) x[i] = *(const u32x4*)(p + (size_t)i * 8 * DF_);
  float a[8];
#pragma unroll
  for (int e = 0; e < 8; ++e) a[e] = 0.f;
#pragma unroll
  for (int i = 0; i < 18; ++i)
#pragma unroll
    for (int e = 0; e < 4; ++e) { a[2 * e] += bflo(x[i][e]); a[2 * e + 1] += bfhi(x[i][e]); }
#pragma unroll
  for (int e = 0; e < 8; ++e) { a[e] += __shfl_xor(a[e], 8); a[e] = swap32_sum(swap16_sum(a[e])); }
  if (l < 8) {
    float* d = cs + qr * 9216 + (cb * 32 + l * 4) * 4 + mat * 2;
#pragma unroll
    for (int e = 0; e < 4; ++e) { f32x2 o = {a[2 * e], a[2 * e + 1]}; *(f32x2*)(d + e * 4) = o; }
  }
}

DI void colsum_label(const u16* __restrict__ wg, const u16* __restrict__ wu, float* __restrict__ cs, int b, int j, int l) {
  const int jj = j * 2 + ((b >> 2) & 1), cbl = jj >> 3;
  colsum_slice(wg, wu, cs, (cbl < 16 ? (b & 3) * 16 + cbl : 64 + (b & 3) * 2 + (cbl - 16)) * 8 + (jj & 7), l);
}

#define K0_CS 64
constexpr int K0_GRID = 64 + 288 + K0_CS;
template <bool NT> DI void dma16p(const char* sbase, u32 voff, u32 lds_dst) {
  if constexpr (NT) asm volatile("s_mov_b32 m0, %2\n\ts_nop 0\n\tglobal_load_lds_dwordx4 %0, %1 nt" ::"v"(voff), "s"(sbase), "s"(lds_dst) : "memory");
  else dma16(sbase, voff, lds_dst);
}
extern "C" __global__ __launch_bounds__(256) void k0_prep(const u16* __restrict__ q, const u16* __restrict__ k, const u16* __restrict__ v,
                                                          const u16* __restrict__ shk, const u16* __restrict__ ssm, const u16* __restrict__ wg,
                                                          const u16* __restrict__ wu, char* __restrict__ ws) {
  __shared__ __attribute__((aligned(128))) char lds[(72 + 64 + 64 + 64) * 128];
  ST_DECL; ST(0);
  asm volatile("" ::"s"(q), "s"(k), "s"(v), "s"(shk), "s"(ssm), "s"(wg), "s"(wu), "s"(ws));
  const int tid = threadIdx.x, l = tid & 63;
  const int w = __builtin_amdgcn_readfirstlane(tid >> 6);
  if (blockIdx.x >= 352) {
    if (w < 2) colsum_label(wg, wu, (float*)(ws + OFF_CS), blockIdx.x, 72 - K0_CS / 4 + ((blockIdx.x - 352) >> 3) * 2 + w, l);
    return;
  }
  if (blockIdx.x >= 64) {
    const int b = blockIdx.x - 64;
    const int r8 = b >> 3, tb = r8 < 32 ? r8 : (b & 7) * 4 + (r8 - 32), cb = r8 < 32 ? (b & 7) : 8;
    const ldsp LP = (ldsp)lds;
    const u32 L0 = (u32)(uintptr_t)LP;
    constexpr int KT = 0, VT = 72 * 128, QT = VT + 8192, OT = QT + 8192;
    const int col = cb * 128 + (l & 7) * 16, rl = l >> 3;
    {
      const int r0 = tb * 64 - 8 + w * 8 + rl;
      dma16p<true>((const char*)k, (u32)(max(r0, 0) * (D_ * 2) + col), L0 + KT + w * 1024);
      const u32 vo = (u32)((tb * 64 + w * 8 + rl) * (D_ * 2) + col);
      dma16p<true>((const char*)k + 24 * D_ * 2, vo, L0 + KT + (w + 4) * 1024);
      if (w == 0) dma16p<true>((const char*)k + 56 * D_ * 2, vo, L0 + KT + 8 * 1024);
      dma16p<true>((const char*)v, vo, L0 + VT + w * 1024);
      dma16p<true>((const char*)v + 32 * D_ * 2, vo, L0 + VT + (w + 4) * 1024);
    }
    const int sl = w * 16 + (l >> 4) * 4, cq = l & 15;
    u32x4 w0, w1, pf0, pf1;
    gload16<0>(w0, (u32)((cb * 64 + cq * 4) * 8), (const char*)shk);
    gload16<16>(w1, (u32)((cb * 64 + cq * 4) * 8), (const char*)shk);
    {
      const u32 vo = (u32)((tb * 64 + w * 8 + rl) * (D_ * 2) + col);
      dma16p<false>((const char*)q, vo, L0 + QT + w * 1024);
      dma16p<false>((const char*)q + 32 * D_ * 2, vo, L0 + QT + (w + 4) * 1024);
    }
    ST(3);
    wait_vm2<2>(w0, w1);
    BARRIER();
    ST(4);
    u32x2 kr[7], vr[4];
#pragma unroll
    for (int i = 0; i < 7; ++i) kr[i] = *(const LAS u32x2*)(LP + (KT + (sl + 5 + i) * 128 + cq * 8));
#pragma unroll
    for (int i = 0; i < 4; ++i) vr[i] = *(const LAS u32x2*)(LP + (VT + (sl + i) * 128 + cq * 8));
    const u32 po = (u32)((r8 < 32 ? (b & 7) * 64 + r8 * 2 : 512 + (b & 7) * 8 + (r8 - 32) * 2) * 4096 + tid * 16);
    gload16<0>(pf0, po, (const char*)ssm);
    gload16<0>(pf1, po + 4096, (const char*)ssm);
    if (tb == 0 && sl == 0) { kr[0] = (u32x2){0u, 0u}; kr[1] = kr[0]; kr[2] = kr[0]; }
#pragma unroll
    for (int c = 0; c < 4; ++c) {
      const u32 wa = (c < 2) ? w0[2 * (c & 1)] : w1[2 * (c & 1)], wb = (c < 2) ? w0[2 * (c & 1) + 1] : w1[2 * (c & 1) + 1];
      const float f0 = bflo(wa), f1 = bfhi(wa), f2 = bflo(wb), f3 = bfhi(wb);
      float kk[7], kvv[4];
#pragma unroll
      for (int i = 0; i < 7; ++i) kk[i] = (c & 1) ? bfhi(kr[i][c >> 1]) : bflo(kr[i][c >> 1]);
#pragma unroll
      for (int i = 0; i < 4; ++i) {
        const float ks = kk[i + 3] * f0 + kk[i + 2] * f1 + kk[i + 1] * f2 + kk[i] * f3;
        const float vv = (c & 1) ? bfhi(vr[i][c >> 1]) : bflo(vr[i][c >> 1]);
        kvv[i] = ks * vv;
      }
      u32x2 o = {pack2(kvv[3], kvv[2]), pack2(kvv[1], kvv[0])};
      *(LAS u32x2*)(LP + (OT + (cq * 4 + c) * 128 + (((sl >> 3) ^ (cq & 7)) * 16) + ((sl & 4) ? 0 : 8))) = o;
    }
    wait_vm<2>();
    LDS_BARRIER();
#pragma unroll
    for (int i = 0; i < 2; ++i) {
      const int ch = w * 16 + i * 8 + (l >> 3);
      const u32x4 o = *(const LAS u32x4*)(LP + (OT + (w * 16 + i * 8) * 128 + l * 16));
      gstore16(ws + OFF_KVT, (u32)((cb * 64 + ch) * 4096 + (tb * 8 + ((l & 7) ^ ((ch >> 2) & 7))) * 16), o);
    }
#pragma unroll
    for (int i = 0; i < 2; ++i) {
      const int gl = i * 8 + (l >> 3), t2 = w * 16 + (l & 7) * 2;
      const u32x2 a = *(const LAS u32x2*)(LP + (QT + t2 * 128 + gl * 8)), c = *(const LAS u32x2*)(LP + (QT + (t2 + 1) * 128 + gl * 8));
      u32x4 o = {a[0], a[1], c[0], c[1]};
      gstore16(ws + OFF_XP, (u32)(((cb * 16 + gl) * S_ + tb * 64 + t2) * 8), o);
    }
    if (b < 41) {
      const u32 f = b < 9 ? 0u : 0xffffffffu;
      u32x4 z = {f, f, f, f};
      gstore16(ws + OFF_FLG, (u32)(b * 4096 + tid * 16), z);
    }
    ST(5);
    asm volatile("s_waitcnt vmcnt(0)" : "+v"(pf0), "+v"(pf1)::"memory");
  } else {
    if (w == 0) {
      unsigned long long c0;
      asm volatile("s_getpc_b64 s[40:41]\n\ts_add_u32 s40, s40, k1_conv@rel32@lo+4\n\ts_addc_u32 s41, s41, k1_conv@rel32@hi+12\n\ts_mov_b64 %0, s[40:41]" : "=s"(c0)::"s40", "s41", "scc");
      code_touch((const char*)c0, (u32)(l * 128), (u32)(uintptr_t)(ldsp)lds);
    }
    const int row = blockIdx.x * 32 + w * 8 + (l >> 3);
    const u16* p = q + (size_t)row * D_ + (l & 7) * 8;
    u32x4 x[9];
#pragma unroll
    for (int i = 0; i < 9; ++i) x[i] = *(const u32x4*)(p + i * 64);
    float sum = 0.f;
#pragma unroll
    for (int i = 0; i < 9; ++i)
#pragma unroll
      for (int j = 0; j < 4; ++j) sum += bflo(x[i][j]) + bfhi(x[i][j]);
    sum += __shfl_xor(sum, 1); sum += __shfl_xor(sum, 2); sum += __shfl_xor(sum, 4);
    const float mu = sum * (1.f / D_);
    float sq = 0.f;
#pragma unroll
    for (int i = 0; i < 9; ++i)
#pragma unroll
      for (int j = 0; j < 4; ++j) { const float a = bflo(x[i][j]) - mu, c = bfhi(x[i][j]) - mu; sq += a * a + c * c; }
    sq += __shfl_xor(sq, 1); sq += __shfl_xor(sq, 2); sq += __shfl_xor(sq, 4);
    if ((l & 7) == 0) { f32x2 o = {mu, rsqrtf(sq * (1.f / D_) + 1e-5f)}; ((f32x2*)(ws + OFF_ST1))[row] = o; }
  }
  ST(1); ST_DRAIN(); ST(2);
  ST_FLUSH((u32*)(ws + (202u << 20)), (blockIdx.x >= 64 ? blockIdx.x - 64 : blockIdx.x + 288) * 4 + w);
}

constexpr int K1_UPLANE = 2304, K1_UCH = 4608, K1_TOFF = 4 * K1_UCH, K1_TCOPY = 4160, K1_TCH = 4 * K1_TCOPY;
constexpr int K1_YOFF = K1_TOFF + 4 * K1_TCH, K1_YBUF = 8192;
constexpr int K1_LDS = K1_YOFF + 8 * K1_YBUF;
constexpr int K1_GRID = 256;

#define HSTORE gstore16
extern "C" __global__ __launch_bounds__(512) void k1_conv(const u16* __restrict__ ssm, const u16* __restrict__ ssmD, const u16* __restrict__ wg,
                                                          const u16* __restrict__ wu, char* __restrict__ ws) {
  extern __shared__ __attribute__((aligned(128))) char lds[];
  ST_DECL; ST(0);
  asm volatile("" ::"s"(ssm), "s"(ssmD), "s"(wg), "s"(wu), "s"(ws));
  const int tid = threadIdx.x, l = tid & 63;
  const int w = __builtin_amdgcn_readfirstlane(tid >> 6);
  const int b = blockIdx.x;
  const ldsp LP = (ldsp)lds;
  const u32 L0 = (u32)(uintptr_t)LP;
  if (b >= 144) {
    if (w == 7) {
      unsigned long long c0; u32 cl;
      asm volatile("s_getpc_b64 s[40:41]\n\ts_add_u32 s40, s40, ffn_fused@rel32@lo+4\n\ts_addc_u32 s41, s41, ffn_fused@rel32@hi+12\n\ts_mov_b64 %0, s[40:41]\n\ts_mov_b32 %1, .Lffn_code_end-ffn_fused"
                   : "=s"(c0), "=s"(cl)::"s40", "s41", "scc");
#pragma unroll
      for (int i = 0; i < 5; ++i) code_touch((const char*)c0, min((u32)(l * 128 + i * 8192), cl - 4u), L0);
    }
    for (int i = 0; i < SIDE_DELAY; ++i) __builtin_amdgcn_s_sleep(8);
    const int j = ((b - 144) >> 3) + 14 * w;
    if (j < 72 - K0_CS / 4) colsum_label(wg, wu, (float*)(ws + OFF_CS), b, j, l);
  } else {
    const u16* __restrict__ kvt = (const u16*)(ws + OFF_KVT);
    const u16* __restrict__ xp = (const u16*)(ws + OFF_XP);
    const f32x2* __restrict__ st1 = (const f32x2*)(ws + OFF_ST1);
    const int g = (b >> 3) < 16 ? (b & 7) * 16 + (b >> 3) : 128 + (b & 7) * 2 + ((b >> 3) - 16), ch = w & 3, hf_ = w >> 2;
    const int gch = g * 4 + ch;
    {
      const char* src = (const char*)(kvt + (size_t)gch * S_);
      const u32 vo = (2 * l + hf_) * 16;
      const u32 dst = L0 + ch * K1_UCH + hf_ * K1_UPLANE + 256;
      dma16(src, vo, dst);
      dma16(src + 2048, vo, dst + 1024);
    }
    const u16* krow = ssm + (size_t)gch * S_;
    u32x4 wv[2]; u32x2 wn[2];
  #pragma unroll
    for (int it = 0; it < 2; ++it) {
      const int idx = hf_ * 128 + it * 64 + l;
      wv[it] = *(const u32x4*)(krow + (idx > 0 ? idx - 1 : 0) * 8);
      wn[it] = *(const u32x2*)(krow + idx * 8);
    }
    const u32x4 wl = *(const u32x4*)(krow + opq(255 * 8));
    const u32 dv1 = ssmD[opq(gch)];
    SCHED0();
    u32x2 xr[4]; f32x2 st[4];
  #pragma unroll
    for (int i = 0; i < 4; ++i) { const int s = (tid >> 4) * 32 + (tid & 15) + (i & 1) * 16 + (i >> 1) * 1024; xr[i] = *(const u32x2*)(xp + ((size_t)g * S_ + s) * 4); st[i] = st1[s]; }
    SCHED0();
    const u32x4 z4 = {0u, 0u, 0u, 0u};
    if (l < 16) *(LAS u32x4*)(LP + (ch * K1_UCH + hf_ * K1_UPLANE + l * 16)) = z4;
    const ldsp tb = LP + (K1_TOFF + ch * K1_TCH);
    auto put = [&](int pos  , u32x4 a, u32x2 n) {
      const u32 o0 = __builtin_amdgcn_alignbit(a[1], a[0], 16), o1 = __builtin_amdgcn_alignbit(a[2], a[1], 16), o2 = __builtin_amdgcn_alignbit(a[3], a[2], 16),
                o3 = __builtin_amdgcn_alignbit(n[0], a[3], 16), o4 = __builtin_amdgcn_alignbit(n[1], n[0], 16);
      u32x4 c1 = {o0, o1, o2, o3}, c2 = {a[1], a[2], a[3], n[0]}, c3 = {o1, o2, o3, o4};
      *(LAS u32x4*)(tb + pos * 16) = a;
      *(LAS u32x4*)(tb + (K1_TCOPY + pos * 16)) = c1;
      *(LAS u32x4*)(tb + (2 * K1_TCOPY + pos * 16)) = c2;
      *(LAS u32x4*)(tb + (3 * K1_TCOPY + pos * 16)) = c3;
    };
    if (hf_ == 0 && l == 0) wv[0] = z4;
  #pragma unroll
    for (int it = 0; it < 2; ++it) put(hf_ * 128 + it * 64 + l + 1, wv[it], wn[it]);
    if (hf_ == 1 && l < 2) { u32x2 z2 = {0u, 0u}; put(l ? 257 : 0, l ? wl : z4, z2); }
    ST(1);
    wait_vm<8>();
    LDS_BARRIER();
    ST(2);
    const int a_ = l & 15, q_ = l >> 4;
    const int cpy = (1 + a_) & 3;
    const ldsp taddr = LP + (K1_TOFF + ch * K1_TCH + cpy * K1_TCOPY + 2 * (1 + a_ + 8 * q_ - cpy) + hf_ * 256);
    const ldsp uaddr = LP + (ch * K1_UCH + ((q_ + 1) & 1) * K1_UPLANE + (16 + a_ - (q_ >> 1) - 8 * hf_ - 6) * 16);
    f32x4 acc[8];
    u32x4 dg;
    {
      const int j = 15 - a_ - 8 * q_;
      const u32 dval = hf_ ? 0u : dv1;
  #pragma unroll
      for (int e = 0; e < 4; ++e) dg[e] = (j == 2 * e) ? dval : ((j == 2 * e + 1) ? (dval << 16) : 0u);
    }
    u32x4 U[2][8], T[2][8];
    auto ldU = [&](int e, int j) { return *(const LAS u32x4*)(uaddr + ((3 - e) * 32 + 256 * j)); };
    auto ldT = [&](int e, int m) {
      const u32x2 lo = *(volatile const LAS u32x2*)(taddr + (64 * e + 512 * m)), hi = *(volatile const LAS u32x2*)(taddr + (8 + 64 * e + 512 * m));
      u32x4 t = {lo[0], lo[1], hi[0], hi[1]};
      return t;
    };
#pragma unroll
    for (int j = 0; j < 8; ++j) U[0][j] = ldU(0, j);
#pragma unroll
    for (int j = 0; j < 8; ++j) T[0][j] = ldT(0, j);
    asm volatile("s_nop 1" : "+v"(dg));
    SCHED0();
#pragma unroll
    for (int n = 0; n < 8; ++n) mma<true>(acc[n], dg, U[0][n]);
    SCHED0();
    float xs[16], xn[16];
    static_for<0, 144>([&](auto ic) {
      constexpr int e = ic.value / 36, i = ic.value % 36;
      constexpr auto mn = [] { int k = 0; for (int m = 0; m < 8; ++m) for (int n = m; n < 8; ++n) { if (k == i) return m * 8 + n; ++k; } return 0; }();
      constexpr int m = mn >> 3, n = mn & 7;
      if constexpr (e < 3 && i < 32 && (i & 1) == 0) {
        if constexpr ((i & 2) == 0) U[(e + 1) & 1][i >> 2] = ldU(e + 1, i >> 2); else T[(e + 1) & 1][i >> 2] = ldT(e + 1, i >> 2);
      }
      mma<false>(acc[n], T[e & 1][m], U[e & 1][n - m]);
      if constexpr (e == 3 && i < 16) {
        constexpr int ti = i >> 2, c = i & 3;
        xs[i] = (c & 1) ? bfhi(xr[ti][c >> 1]) : bflo(xr[ti][c >> 1]);
        xn[i] = hide(xs[i] - st[ti][0]) * st[ti][1];
        pinf(xs[i]); pinf(xn[i]);
      }
      SCHED0();
    });
    MFMA_DRAIN(); SCHED0();
    ST(3);
    {
      const ldsp yb = LP + (K1_YOFF + (hf_ * 4 + ch) * K1_YBUF + ((4 * a_ + q_) ^ ((a_ >> 1) & 3) ^ ((a_ & 2) << 1)) * 16);
  #pragma unroll
      for (int n = 0; n < 8; ++n) *(LAS f32x4*)(yb + n * 1024) = acc[n];
    }
    LDS_BARRIER();
    ST(4);
    {
      float y[4][8];
#pragma unroll
      for (int i = 0; i < 4; ++i) {
        const int s = (tid >> 4) * 32 + (tid & 15) + (i & 1) * 16, sl = s >> 2;
        const ldsp yb = LP + (K1_YOFF + (sl ^ ((sl >> 3) & 3) ^ ((sl >> 1) & 4)) * 16 + (s & 3) * 4 + (i >> 1) * 4096);
#pragma unroll
        for (int c = 0; c < 8; ++c) y[i][c] = *(const LAS float*)(yb + c * K1_YBUF);
      }
#pragma unroll
      for (int j = 0; j < 2; ++j) {
        u32x4 o;
#pragma unroll
        for (int e = 0; e < 2; ++e) {
          const int i = 2 * j + e;
          float hv[4];
#pragma unroll
          for (int c = 0; c < 4; ++c) hv[c] = fmaf(xn[4 * i + c], hide(y[i][c] + y[i][4 + c]), xs[4 * i + c]);
          o[2 * e] = pack2(hv[0], hv[1]); o[2 * e + 1] = pack2(hv[2], hv[3]);
        }
        HSTORE(ws + OFF_HF + (size_t)j * 32 * 36864 + (g >> 3) * 2048 + (g & 1) * 1024 + ((g >> 1) & 3) * 256, (u32)((tid >> 4) * 36864 + (tid & 15) * 16), o);
      }
    }
    ST(5);
  }
  ST_DRAIN(); ST(6);
  ST_FLUSH((u32*)(ws + (203u << 20)), b * 8 + w);
}

constexpr int G1_QE = 147456;
constexpr int G1_QL = 14 * 8192;
constexpr int G1_CS = G1_QE + 8192;
constexpr int G1_CP = G1_CS + 2048;
constexpr int FF_LDS = G1_CP + 5120 + 256;
constexpr int PF_ISS0 = 86;
constexpr int STG = 18432, NST = 8;
constexpr int G1_D = 4, G1_DW = 6, HD = 5, HB = HD + 1;
constexpr int p1_ops(int x, bool even_only) { return (x + G1_DW < 18 ? 1 : 0) + (x + G1_D < 18 ? (even_only ? 1 : 2) : 0) + (x == 18 - G1_D ? 3 : 0); }
constexpr int p1_wait(int g) {
  int n = 0;
  if (g + 1 < G1_D) n += 3 * (G1_D - (g + 2)) + (G1_DW - G1_D);
  for (int x = (g + 2 - G1_D > 0 ? g + 2 - G1_D : 0); x < g; ++x) n += p1_ops(x, false);
  return n + p1_ops(g, true);
}
constexpr bool p1_bar(int g) { return g < G1_D - 1 || (g - (G1_D - 1)) % (G1_DW - G1_D + 1) == 0; }
constexpr float GC = -2.3022081983f;
constexpr int U2 = 216;
constexpr int g1_q_issue(int half, int r) { return 8 + 6 * r; }
constexpr int g1_b_issue(int half, int j) { return (j < 14 ? 29 + 3 * j : 73 + 3 * (j - 14)); }
constexpr int G1_E0 = 9;
constexpr int epi_start(int i) { return i < 8 ? G1_E0 + 26 * i : U2 + 2; }
static_assert(G1_E0 + 26 * 7 + 24 < 216, "epilogue stream");
constexpr bool epi_stores(int i) { return i == 1 || i == 3 || i == 4 || i == 6 || i == 8; }
constexpr int w_issue(int n, int k) {
  return n < 4 ? 191 + 4 * n + 2 * k : n < 6 ? 209 + 4 * (n - 4) + 2 * k : ((n & 1) || (G2_BAR_ALL && n > 6)) ? U2 + 9 * (n - 6) + 4 * k : U2 + 9 * (n - 6) + WE0 + WE1 * k;
}
constexpr int h_issue(int n, int k) { return n < 5 ? 192 + 4 * n + 2 * k : U2 + 9 * (n - (n < 10 ? HD : 4)) + 2 + 3 * k; }
constexpr int dq_issue(int r) { return r == 0 ? 183 : r < 4 ? U2 + 9 * (4 * r - 3) + 1 : U2 + 9 * 11 + 1; }
constexpr int poll_iss(int p) { return p == 0 ? 180 : p == 1 ? U2 + 9 * 2 + 3 : U2 + 9 * 7 + 3; }
constexpr int poll_chk(int p) { return p == 0 ? 191 : p == 1 ? U2 + 9 * 6 + 1 : U2 + 9 * 10 + 1; }
constexpr int RES_ISS = U2 + 9 * 16 + 7;
template <class F> constexpr int find_at(int n, int u, F f) { for (int i = 0; i < n; ++i) if (f(i) == u) return i; return -1; }
constexpr int cnt_ops(int half, int k0, int k1) {
  int n = 0;
  auto in = [&](int key) { return key > k0 && key < k1 ? 1 : 0; };
  for (int r = 0; r < 4; ++r) n += in(4 * g1_q_issue(half, r) + 2);
  for (int j = 0; j < 18; ++j) n += in(4 * g1_b_issue(half, j) + 2);
  for (int i = 0; i < 8; ++i) if (epi_stores(i)) n += in(4 * (epi_start(i) + 18)) + in(4 * (epi_start(i) + 24));
  n += in(4 * (U2 + 11)) + in(4 * (U2 + 14));
  for (int s = 0; s < 18; ++s) for (int k = 0; k < 2; ++k) n += in(4 * w_issue(s, k) + 2) + in(4 * h_issue(s, k) + 2);
  for (int p = 0; p < 3; ++p) n += in(4 * poll_iss(p) + 2);
  n += 3 * in(4 * RES_ISS + 2);
  for (int r = 0; r < 5; ++r) n += in(4 * (PF_ISS0 + 4 * r) + 2);
  for (int r = 0; r < 5; ++r) n += in(4 * dq_issue(r) + 2);
  return n;
}
constexpr int wait_n(int k0a, int k0b, int k1) { return cmin(cnt_ops(0, k0a, k1), cnt_ops(1, k0b, k1)); }
constexpr int wait_q(int r, int tw) { return wait_n(4 * g1_q_issue(0, r) + 2, 4 * g1_q_issue(1, r) + 2, 4 * tw + 1); }
constexpr int wait_b(int j, int tw) { return wait_n(4 * g1_b_issue(0, j) + 2, 4 * g1_b_issue(1, j) + 2, 4 * tw + 1); }
constexpr int wait_k(int k0, int k1) { return wait_n(k0, k0, k1); }
constexpr int cmax(int a, int b) { return a > b ? a : b; }
static_assert(p1_bar(6) && p1_bar(9), "column-sum hand-over uses these barriers");
static_assert(w_issue(1, 1) > epi_start(6) + 24 && h_issue(0, 0) > poll_chk(0) && h_issue(10, 0) > poll_chk(1) && h_issue(14, 0) > poll_chk(2), "schedule");
constexpr int kk_of(int s) { return s < 8 ? 9 * ((s >> 1) & 1) + 4 * (s & 1) + (s >> 2) : s < 10 ? 9 * (s - 8) + 8 : 9 * (((s - 10) >> 1) & 1) + 4 * (s & 1) + 2 + ((s - 10) >> 2); }
constexpr u32 rowc(int kk) { return (kk % 9 < 8 ? (kk / 9) * 256 + (kk % 9) * 32 : (kk / 9) * 32) * (D_ * 2); }
struct Acc { f32x4 a[2][2]; };

extern "C" __global__ __launch_bounds__(512, 2) void ffn_fused(const u16* __restrict__ wg, const u16* __restrict__ wu, const u16* __restrict__ wd,
                                                               char* __restrict__ ws, u16* __restrict__ out) {
  extern __shared__ __attribute__((aligned(128))) char lds[];
  ST_DECL; ST(0);
  asm volatile("" ::"s"(wg), "s"(wu), "s"(wd), "s"(ws), "s"(out));
  const int tid = threadIdx.x, l = tid & 63;
  const int w = __builtin_amdgcn_readfirstlane(tid >> 6);
  const int b = blockIdx.x;
  const int fg = (b & 3) * 8 + ((b >> 3) & 7), tg = ((b >> 2) & 1) * 4 + (b >> 6);
  const int tt = tg * 8 + w;
  const ldsp LP = (ldsp)lds;
  const u32 L0 = (u32)(uintptr_t)LP;
  constexpr u32 RS = DF_ * 2, GS = 32 * RS;
  const int wblk = w & 1, whr = (w >> 1) & 1, whalf = w >> 2;
  const rsrc_t rW = mk_rsrc((w & 1) ? wu : wg, D_ * DF_ * 2), rS = mk_rsrc(ws, (u32)WS_USED), rD = mk_rsrc(wd, D_ * DF_ * 2);
  const u32 ab_voff = (u32)((16 * whalf + 8 * whr + (l >> 3)) * RS + ((((l & 7) >> 1) ^ (((l >> 4) & 1) | (whr << 1))) * 32) + (l & 1) * 16 + fg * 256);
  const u32 ab_dst = L0 + whalf * 4096 + wblk * 2048 + whr * 1024;
  auto mk_qvoff = [&](u32 l2) { return (u32)(((l2 >> 1) ^ ((l2 >> 2) & 4)) * RS + (l2 & 1) * 16 + (w >> 1) * GS + 8192 + fg * 32); };
  u32x4 h[2][18];
  const u32 lo16 = l * 16;
  const u32 h_voff = lo16 + (u32)OFF_HF + tt * 36864;
  auto issue_dma = [&](auto ic) { constexpr int g = ic.value; bdma_i<g * 8192, g * GS>(rW, ab_voff, ab_dst); };
  auto issue_h = [&](auto ic, auto tc) { constexpr int g = ic.value, tl = tc.value; bload<(g & 1) * 2048 + tl * 1024>(h[tl][g], rS, h_voff, (g >> 1) * 4096); };
  static_for<0, G1_D>([&](auto ic) { issue_dma(ic); issue_h(ic, IC<0>{}); issue_h(ic, IC<1>{}); });
  static_for<G1_D, G1_DW>(issue_dma);
  const char* code0; u32 codelen;
  CODE_RANGE(code0, codelen, ".Lffn_code_end");
  u32x4 sx[6];
  stat_loads(fg, sx, rS, h_voff, fg * 6144, fg * 6144 + 4096);
  ST(1);
  const u32 lw = L0 + w * 1024;
  const int rq = l >> 4, rr = (l >> 2) & 3, rp = l & 3;
  const u32 rb0 = L0 + (rq >> 1) * 4096 + (rq & 1) * 1024 + rr * 128 + rp * 8 + (((rr >> 1) | ((rq & 1) << 1)) * 32);
#define abase(c, off) lds_base(rb0 , (c) * 32, (off))
  Acc acc[4];
  const char* stb = ws + OFF_ST2;
  u32x4 sg4; u32x2 sg2;
  {
    float s1[2] = {0.f, 0.f}, s2[2] = {0.f, 0.f};
    ldsp pb[3];
    Frag pf[2][3];
    auto fetch1 = [&](auto ic, int c, int hi) {
      constexpr int s = ic.value;
      if constexpr ((s & 15) == 0) { if (hi == 0) pb[c] = abase(c, s * 4096); }
      constexpr int o = ((s & 15) >> 1) * 8192 + (s & 1) * 2048;
      if (hi) pf[s & 1][c].hi = tr_half(pb[c] + (o + 512)); else pf[s & 1][c].lo = tr_half(pb[c] + o);
    };
    wait_vm2<3 * (G1_D - 1) + G1_DW - G1_D>(h[0][0], h[1][0]);
    BARRIER();
    swap_halves(h[0][0], h[1][0]);
#pragma unroll
    for (int c = 0; c < 3; ++c) { fetch1(IC<0>{}, c, 0); fetch1(IC<0>{}, c, 1); }
    static_for<0, 36>([&](auto ic) {
      constexpr int s = ic.value, g = s >> 1, mt = s & 1;
      constexpr int SQ = 2 * (18 - G1_D);
      auto mm = [&](int c) {
#pragma unroll
        for (int tl = 0; tl < 2; ++tl) {
          mma<g == 0>(acc[c].a[mt][tl], frag_of(pf[s & 1][c]), h[tl][g]);
          SCHED0();
          if constexpr (s < 35) fetch1(IC<s + 1>{}, c, tl);
          SCHED0();
        }
      };
      if constexpr (mt == 1 && s < 35) {
        wait_vm2<p1_wait(g)>(h[0][g + 1], h[1][g + 1]);
        if constexpr (p1_bar(g)) BARRIER();
      }
      SCHED0();
      touch(pf[s & 1][1]);
      mm(0);
      if constexpr (mt == 0 && g + G1_DW < 18) issue_dma(IC<g + G1_DW>{});
      if constexpr (mt == 1 && g + G1_D < 18) issue_h(IC<g + G1_D>{}, IC<1>{});
      if constexpr (s == 3) {
        const int ii = min(tid, 287), qd = ii / 72, r = ii - qd * 72;
        if (w < 5) dma16(ws + OFF_CS, (u32)(qd * 2304 + (r < 64 ? fg * 64 + r : 2048 + fg * 8 + (r - 64))) * 16u, L0 + G1_CP + w * 1024);
      }
      if constexpr (s == 16) {
        const u32 pi = min(opq(tid), 71u);
        const ldsp cp = LP + (G1_CP + pi * 16);
        const f32x4 r = (*(const LAS f32x4*)cp + *(const LAS f32x4*)(cp + 1152)) + (*(const LAS f32x4*)(cp + 2304) + *(const LAS f32x4*)(cp + 3456));
        *(LAS f32x4*)(LP + (G1_CS + pi * 16)) = r;
      }
      if constexpr (s == SQ) {
        bdma<G1_QE>(rW, mk_qvoff(l), 0, lw);
      }
      SCHED0();
      mm(1);
      if constexpr (g + G1_D < 18 && mt == 0) issue_h(IC<g + G1_D>{}, IC<0>{});
      if constexpr (s == SQ) { const u32 so = tt * 2048 + ((rq >> 1) * 16 + (l & 15)) * 64 + (rq & 1) * 24; ld_sc1_x4(sg4, so, stb); ld_sc1_x2<16>(sg2, so, stb); }
      if constexpr (mt == 1 && s < 35) swap_halves(h[0][g + 1], h[1][g + 1]);
      if constexpr (s >= 8 && s < 14) { if constexpr (s == 8) pin6(sx); stat_acc(fg, sx[s - 8], s1, s2); }
      SCHED0();
      mm(2);
      if constexpr (s == 14) {
        const float a0 = swap32_sum(swap16_sum(s1[0])), c0 = swap32_sum(swap16_sum(s2[0]));
        const float a1 = swap32_sum(swap16_sum(s1[1])), c1 = swap32_sum(swap16_sum(s2[1]));
        u32x2 gr = {__float_as_uint((l & 16) ? a1 : a0), __float_as_uint((l & 16) ? c1 : c0)};
        st_sc1_x2_if(fg, stb, (u32)(tt * 2048 + fg * 8 + (l & 31) * 64), gr);
      }
      SCHED0();
      if constexpr (s % 12 == 11) { ST(2 + s / 12); }
    });
  }
  STR(28);
  Frag wf[4];
  ldsp fb = nullptr, fb2 = nullptr;
  auto fetch = [&](auto ic, int hi) {
    constexpr int i = ic.value, p = i / 36, s = i % 36;
    if constexpr (p != 1) {
      constexpr int c = (p == 0) ? 3 : p - 2;
      constexpr int pos = (p == 0) ? (s + 28) % 36 : s;
      if constexpr (s == 0 || (pos & 15) == 0) { if (!hi) fb = abase(c, (pos & ~15) * 4096); }
      constexpr int o = ((pos & 15) >> 1) * 8192 + (pos & 1) * 2048;
      if (hi) wf[i & 3].hi = tr_half(fb + (o + 512)); else wf[i & 3].lo = tr_half(fb + o);
    } else {
      if constexpr (s == 0 || s == 8) {
        if (!hi) {
          const u32 l2 = opq(tid);
          const int qo = (l2 >> 4) & 1;
          const int qb = (8 * ((l2 >> 4) & 3) + ((l2 >> 2) & 3)) * 32 + (l2 & 3) * 8 + (s == 0 ? G1_QE : G1_QL);
          fb = opaque(LP + (qb + qo * 128)); fb2 = opaque(LP + (qb + (1 - qo) * 128));
        }
      }
      constexpr int o = (s < 8 ? s : s - 8) * 1024;
      if (hi) wf[i & 3].hi = tr_half(fb2 + o); else wf[i & 3].lo = tr_half(fb + o);
    }
  };
  static_for<0, 3>([&](auto ic) { fetch(ic, 0); fetch(ic, 1); });
  SCHED0();
  float nmu[2], k1[2], k2[2], kd[2];
  auto ln2_stats = [&]() {
    wait0_x6(sg4, sg2);
#pragma unroll 1
    for (int it = 0; it < (1 << 16); ++it) {
      const bool bad = (sg4[0] & sg4[1]) == 0xffffffffu || (sg4[2] & sg4[3]) == 0xffffffffu || (sg2[0] & sg2[1]) == 0xffffffffu;
      if (!__any(bad)) break;
      __builtin_amdgcn_s_sleep(2);
      const u32 so = tt * 2048 + ((rq >> 1) * 16 + (l & 15)) * 64 + (rq & 1) * 24;
      ld_sc1_x4(sg4, so, stb); ld_sc1_x2<16>(sg2, so, stb);
      wait0_x6(sg4, sg2);
    }
    {
      const float t1 = swap16_sum(__uint_as_float(sg4[0]) + __uint_as_float(sg4[2]) + __uint_as_float(sg2[0]));
      const float t2 = swap16_sum(__uint_as_float(sg4[1]) + __uint_as_float(sg4[3]) + __uint_as_float(sg2[1]));
      const float mu = hide(t1 * (1.f / D_));
      const float var = fmaxf(t2 * (1.f / D_) - mu * mu, 0.f) + 1e-5f;
      const auto m2 = __builtin_amdgcn_permlane32_swap(__float_as_uint(-mu), __float_as_uint(-mu), false, false);
      const auto v2 = __builtin_amdgcn_permlane32_swap(__float_as_uint(var), __float_as_uint(var), false, false);
#pragma unroll
      for (int tl = 0; tl < 2; ++tl) {
        kd[tl] = __uint_as_float(v2[tl]);
        const float r = __builtin_amdgcn_rsqf(kd[tl]);
        nmu[tl] = __uint_as_float(m2[tl]);
        k1[tl] = hide(GC * r);
        k2[tl] = hide(k1[tl] * 0.044715f * r * r);
      }
    }
  };
  char* hdst = ws + OFF_HH + ((size_t)tt * 288 + (fg >> 1) * 9 + (fg & 1) * 4) * 1024;
  const ldsp csb = opaque(LP + (G1_CS + rq * 32));
  f32x4 cc;
  auto ldcs = [&](int lc, int hfx) { cc = *(const LAS f32x4*)(csb + (lc * 128 + hfx * 16)); };
  u32x4 eo[2];
  u32x2 eq[2];
  float m0, m1, tu0, tu1, y0, y1;
  auto emit = [&](int lc, int i, u32 pk) {
    const int tl = i & 1, hx = i >> 1;
    if (lc == 8) {
      eq[tl][hx] = pk;
      if (hx) st_sc1_x2(hdst + (size_t)tl * 147456 + (8 - (fg & 1) * 4) * 1024 + (fg & 1) * 8, lo16, eq[tl]);
    } else {
      eo[tl][(lc & 1) * 2 + hx] = pk;
      if ((lc & 1) && hx) st_sc1_x4(hdst + (size_t)tl * 147456 + (lc >> 1) * 1024, lo16, eo[tl]);
    }
  };
  auto epi = [&](const Acc& a, int lc, int st, int nlc, int part) {
    const int i = st / 6, sg = st % 6, tl = i & 1, e = (i >> 1) * 2;
    if (sg == 0) {
      if (part == 0) { if (i > 0) { u32 pk = pack2(tu0, tu1 * y1); pin(pk); emit(lc, i - 1, pk); } }
      else if (i < 4) { m0 = fmaf(nmu[tl], cc[0], a.a[0][tl][e]); m1 = fmaf(nmu[tl], cc[1], a.a[0][tl][e + 1]); pinf(m0); pinf(m1); }
    } else if (sg == 1) {
      if (part == 0) { tu0 = fmaf(nmu[tl], cc[2], a.a[1][tl][e]); tu1 = fmaf(nmu[tl], cc[3], a.a[1][tl][e + 1]); pinf(tu0); pinf(tu1); }
      else {
        y0 = hide(m0 * m0); y1 = m1 * m1; pinf(y0); pinf(y1);
        if (i == 1) ldcs(lc, 1);
        if (i == 3 && nlc >= 0) ldcs(nlc, 0);
      }
    } else if (sg == 2) {
      if (part == 0) { tu0 = hide(tu0 * m0); tu1 = tu1 * m1; pinf(tu0); pinf(tu1); }
      else { y0 = hide(fmaf(y0, k2[tl], k1[tl])); y1 = fmaf(y1, k2[tl], k1[tl]); pinf(y0); pinf(y1); }
    } else if (sg == 3) {
      if (part == 0) { y0 = hide(y0 * m0); y1 = y1 * m1; pinf(y0); pinf(y1); }
      else { y0 = __builtin_amdgcn_exp2f(y0); pinf(y0); }
    } else if (sg == 4) {
      if (part == 0) { y1 = __builtin_amdgcn_exp2f(y1); pinf(y1); }
      else { y0 = fmaf(y0, kd[tl], kd[tl]); pinf(y0); }
    } else {
      if (part == 0) { y0 = __builtin_amdgcn_rcpf(y0); y1 = fmaf(y1, kd[tl], kd[tl]); pinf(y0); pinf(y1); }
      else { y1 = __builtin_amdgcn_rcpf(y1); tu0 = tu0 * y0; pinf(y1); pinf(tu0); }
    }
  };
  const int r5 = b >> 3;
  const int ks = b & 3, tg2 = ((b >> 2) & 1) * 8 + (r5 & 7), dt = r5 >> 3;
  const int tq = w & 3, kh = w >> 2;
  constexpr u32 RD = D_ * 2;
  const u32 d_dst = L0 + kh * 9216 + tq * 2048;
  const int mb = ks * 4 + kh * 2;
  const u32 rowA = mb * 256 * RD, rowQ = (4096 + mb * 32) * RD;
  auto row0 = [&](int kk) { const int q = kk / 9, i = kk - 9 * q; return i < 8 ? rowA + (q * 256 + i * 32) * RD : rowQ + q * 32 * RD; };
  u32x4 hb[HB][2];
  u32 hv0 = 0, hv1 = 0;
  const char* myflag = ws + OFF_FLG + 16384 + b * 64;
  const char* pflags = ws + OFF_FLG + 16384 + ((b & 7) + kh * 32 + ((r5 & 7) >> 1) * 64) * 64;
  u32 d_voff = 0, dq_voff = 0, p_voff = 0, pv = 0, q_voff = 0;
  f32x4 ac2[9][2];
  ldsp pb[5];
  u32 rb2 = 0, rbq = 0;
  auto fetch2 = [&](auto ic, int hi) {
    constexpr int i = ic.value, s = i / 9, c = i % 9, stg = s % NST;
    if constexpr (stg % 3 == 0 && c < 4) { if (!hi) pb[c] = lds_base(rb2, c * 32, stg * STG); }
    if constexpr (c == 8 && (stg % 3 == 0 || s == 4)) { if (!hi) pb[4] = opaque((ldsp)(uintptr_t)(opq(rbq) + (s < 4 ? (u32)(G1_QE - 8192) - kh * (9216 - 1024) : (u32)((stg - stg % 3) * STG)))); }
    constexpr int o = c < 8 ? (stg % 3) * STG + (c >> 2) * 1024 : (s < 4 ? s * 2048 : (stg % 3) * STG);
    if (hi) wf[i & 3].hi = tr_half(pb[c < 8 ? (c & 3) : 4] + (o + (c < 8 ? 4096 : 512))); else wf[i & 3].lo = tr_half(pb[c < 8 ? (c & 3) : 4] + o);
  };
  auto issue2 = [&](auto uc) {
    constexpr int u = uc.value;
    constexpr int nw0 = find_at(18, u, [](int n) { return w_issue(n, 0); }), nw1 = find_at(18, u, [](int n) { return w_issue(n, 1); });
    constexpr int nh0 = find_at(18, u, [](int n) { return h_issue(n, 0); }), nh1 = find_at(18, u, [](int n) { return h_issue(n, 1); });
    constexpr int r = find_at(5, u, [](int n) { return dq_issue(n); });
    constexpr int pi = find_at(3, u, [](int n) { return poll_iss(n); }), pc = find_at(3, u, [](int n) { return poll_chk(n); });
    if constexpr (nw0 >= 0) bdma_a<(nw0 % NST) * STG, rowc(kk_of(nw0))>(rD, d_voff, kk_of(nw0) % 9 < 8 ? rowA : rowQ, d_dst);
    if constexpr (nw1 >= 0) bdma_a<(nw1 % NST) * STG + 1024, rowc(kk_of(nw1)) + 128>(rD, d_voff, kk_of(nw1) % 9 < 8 ? rowA : rowQ, d_dst);
    if constexpr (nh0 >= 0) HLOAD<(kk_of(nh0) & 3) * 1024>(hb[nh0 % HB][0], rS, hv0, (kk_of(nh0) >> 2) * 4096);
    if constexpr (nh1 >= 0) HLOAD<(kk_of(nh1) & 3) * 1024>(hb[nh1 % HB][1], rS, hv1, (kk_of(nh1) >> 2) * 4096);
    if constexpr (r >= 0) {
      const int tsel = r == 4 ? (tq & 1) : tq;
      const u32 ra = row0(kk_of(4 * r)), rb = row0(kk_of(4 * r + 1)), rc_ = row0(kk_of(r == 4 ? 16 : 4 * r + 2)), rd = row0(kk_of(r == 4 ? 17 : 4 * r + 3));
      const u32 rsel = (tsel & 2) ? ((tsel & 1) ? rd : rc_) : ((tsel & 1) ? rb : ra);
      bdma<0>(rD, dq_voff, rsel, r == 0 ? L0 + G1_QE + (tq * 2 + kh) * 1024 : L0 + (((4 * r) % NST) + tsel) * STG + kh * 9216 + 8192);
    }
    if constexpr (pi >= 0) poll_issue(pv, p_voff, pflags);
    if constexpr (pc >= 0) poll_check<wait_k(4 * poll_iss(pc) + 2, 4 * u + 2), pc + 1>(pv, p_voff, pflags);
  };
  static_for<0, 216>([&](auto ic) {
    constexpr int t = ic.value, p = t / 36, s = t % 36;
    constexpr int slot = (p + 3) & 3;
    constexpr int hg = (p == 0) ? ((s >> 1) + 14) % 18 : (s >> 1);
    constexpr int ci = (t - G1_E0) / 26, st = (t - G1_E0) % 26;
    constexpr int lc = ci < 4 ? ci : (ci == 4 ? 8 : ci - 1), nlc = ci < 3 ? ci + 1 : (ci == 3 ? 8 : ci);
#pragma unroll
    for (int tl = 0; tl < 2; ++tl) {
      if constexpr ((t & 1) == 0) { if (tl == 0) touch(wf[(t + 1) & 3]); }
      mma<(s < 2)>(acc[slot].a[s & 1][tl], frag_of(wf[t & 3]), h[tl][hg]);
      SCHED0();
      if constexpr (t + 3 < 216) fetch(IC<t + 3>{}, tl);
      SCHED0();
      if constexpr (t >= G1_E0 && ci < 8 && st <= 24) epi(acc[ci & 3], lc, st, nlc, tl);
      SCHED0();
    }
    if constexpr (t == 2) ln2_stats();
    if constexpr (t == G1_E0 - 1) ldcs(0, 0);
    if constexpr (t == 7 || t == 28 || t == 72 || t == 190) { SCHED0(); BARRIER(); }
    if constexpr (t == 40) { wait_vm<wait_q(1, 40)>(); BARRIER(); }
    if constexpr (t == 56) { wait_vm<wait_q(3, 56)>(); BARRIER(); }
    if constexpr (t == 68) { wait_vm<wait_b(9, 68)>(); BARRIER(); }
    if constexpr (t == 88) { wait_vm<wait_b(14, 88)>(); BARRIER(); }
    if constexpr (t == 98) { wait_vm<wait_b(17, 98)>(); BARRIER(); }
    if constexpr (t == 160) { wait_vm<wait_k(4 * (epi_start(4) + 24), 4 * t + 1)>(); BARRIER(); flag_store(w, opq(0u), opq(1u), myflag); }
    if constexpr (t == 198) { SCHED0(); BARRIER(); }
    if constexpr (t == 208) { SCHED0(); wait_vm<wait_k(4 * w_issue(1, 1) + 2, 4 * t + 1)>(); BARRIER(); flag_store(w, opq(0u), opq(2u), myflag); }
    if constexpr (t == 7) q_voff = mk_qvoff(opq(tid) & 63);
    if constexpr (t == 179) {
      const u32 l2 = opq(tid) & 63;
      d_voff = (u32)((8 * tq + (l2 >> 3)) * RD + ((((l2 & 7) >> 1) ^ ((l2 >> 4) & 3)) * 32) + (l2 & 1) * 16 + dt * 256);
      dq_voff = (u32)((l2 >> 1) * RD + (l2 & 1) * 16 + 1024 + dt * 32);
      p_voff = (l2 & 3) * 512;
      hv0 = l2 * 16 + (u32)OFF_HH + ((tg2 * 8 + tq * 2 + kh) * 144 + mb * 9) * 1024;
      hv1 = l2 * 16 + (u32)OFF_HH + ((tg2 * 8 + tq * 2 + (kh ^ 1)) * 144 + mb * 9) * 1024;
      pin(d_voff); pin(dq_voff); pin(p_voff); pin(hv0); pin(hv1);
    }
    {
      constexpr int r = find_at(4, t, [](int n) { return g1_q_issue(0, n); }), j = find_at(18, t, [](int n) { return g1_b_issue(0, n); });
      if constexpr (r >= 0) bdma_i<G1_QL + r * 8192, (4 + 4 * r) * GS>(rW, q_voff, lw);
      if constexpr (j >= 0) bdma_i<j * 8192, j * GS + 128>(rW, ab_voff, ab_dst);
    }
    if constexpr (t >= PF_ISS0 && t < PF_ISS0 + 20 && (t - PF_ISS0) % 4 == 0) {
      const int piece = ((b >> 3) * 5 + (t - PF_ISS0) / 4) * 8 + w;
      const int byte = piece * 1024;
      bdma<G1_QE>(rD, lo16, (u32)(byte < 1024 * 1152 ? (b & 3) * 1024 * 1152 + byte : 4096 * 1152 + (b & 3) * 128 * 1152 + (byte - 1024 * 1152)), lw);
    }
    if constexpr (t == 3 || t == 102) code_touch(code0, min(opq((u32)tid) * 64u + (t == 3 ? 8192u : 18432u), codelen - 4u), L0 + FF_LDS - 256);
    if constexpr (t >= 180) issue2(ic);
    SCHED0();
    if constexpr (t % 24 == 23) { ST(5 + t / 24); }
  });
  STR(29);
  {
    const u32 l2 = opq(tid) & 63;
    const u32 q2 = l2 >> 4, r2_ = (l2 >> 2) & 3, p2 = l2 & 3;
    rb2 = L0 + kh * 9216 + (q2 >> 1) * 2048 + (4 * (q2 & 1) + r2_) * 128 + ((2 * (q2 & 1) + (r2_ >> 1)) * 32) + p2 * 8;
    rbq = L0 + kh * 9216 + 8192 + (4 * q2 + r2_) * 32 + p2 * 8;
  }
  const int ft = tg2 * 8 + ks * 2 + kh;
  const int dcol = dt * 128 + tq * 32 + 4 * rq;
  const int qcol = 512 + dt * 16 + 4 * rq;
  u32x2 r0, r1, r2;
  static_for<0, 3>([&](auto ic) { fetch2(ic, 0); fetch2(ic, 1); });
  wait_vm2<wait_k(4 * h_issue(0, 1) + 2, 4 * U2 - 1)>(hb[0][0], hb[0][1]);
  static_for<0, 162>([&](auto ic) {
    constexpr int i = ic.value, s = i / 9, c = i % 9, u = U2 + i;
    if constexpr (c == ((s & 1) ? 6 : 4) && s < 17) {
      constexpr int s2 = cmin(s + 2, 17);
      constexpr int tgt = (s & 1) ? 4 * h_issue(s + 1, 1) + 2
                                  : cmax(cmax(cmax(4 * h_issue(s + 1, 1) + 2, 4 * w_issue(s2, 1) + 2), 4 * dq_issue((s + 1) >> 2) + 2), s == 4 ? 4 * (U2 + 14) : 0);
      wait_vm2<wait_k(tgt, 4 * u - 1)>(hb[(s + 1) % HB][0], hb[(s + 1) % HB][1]);
      if constexpr ((s & 1) == 0 || G2_BAR_ALL) BARRIER();
      if constexpr (s == 4) flag_store(w, opq(0u), opq(3u), myflag);
    }
    SCHED0();
#pragma unroll
    for (int tl = 0; tl < 2; ++tl) {
      if constexpr ((i & 1) == 0 && i + 1 < 162) { if (tl == 0) touch(wf[(i + 1) & 3]); }
      mma<s == 0>(ac2[c][tl], frag_of(wf[i & 3]), hb[s % HB][tl]);
      SCHED0();
      if constexpr (i + 3 < 162) fetch2(IC<i + 3>{}, tl);
      SCHED0();
      if constexpr (i >= 2 && i <= 14) { if (i < 14 || tl == 0) { epi(acc[0], 7, 2 * (i - 2) + tl, -1, 0); epi(acc[0], 7, 2 * (i - 2) + tl, -1, 1); } }
      SCHED0();
    }
    issue2(IC<u>{});
    if constexpr (u == RES_ISS) {
      const char* hb_ = ws + OFF_HF + (size_t)(ft >> 1) * 36864 + (ft & 1) * 8;
      auto po = [&](int d) { return (u32)((d >> 5) * 2048 + ((d >> 2) & 1) * 1024 + ((d >> 3) & 3) * 256 + (l & 15) * 16); };
      gload8(r0, po(dcol), hb_); gload8(r1, po(dcol + 16), hb_); gload8(r2, po(qcol), hb_);
    }
    SCHED0();
    if constexpr (c == 8 && s % 3 == 2) { ST(14 + s / 3); }
  });
  MFMA_DRAIN(); SCHED0();
  {
    const ldsp xw = LP + (2 * STG + w * 9216 + l * 16);
#pragma unroll
    for (int c = 0; c < 9; ++c) *(LAS f32x4*)(xw + c * 1024) = ac2[c][1];
  }
  LDS_BARRIER();
  f32x4 sm[9];
  {
    const ldsp xr = LP + (2 * STG + (w ^ 4) * 9216 + l * 16);
#pragma unroll
    for (int c = 0; c < 9; ++c) sm[c] = ac2[c][0] + *(const LAS f32x4*)(xr + c * 1024);
  }
  ST(20);
  char* part = ws + OFF_PART;
  if (tq != ks) {
    char* pdst = part + ((((size_t)(tg2 * 4 + dt) * 4 + tq) * 4 + ks) * 2 + kh) * 4608;
#pragma unroll
    for (int pc = 0; pc < 4; ++pc) {
      u32x4 v = {pack2(sm[2 * pc][0], sm[2 * pc][1]), pack2(sm[2 * pc][2], sm[2 * pc][3]), pack2(sm[2 * pc + 1][0], sm[2 * pc + 1][1]), pack2(sm[2 * pc + 1][2], sm[2 * pc + 1][3])};
      st_sc1_x4(pdst + pc * 1024, lo16, v);
    }
    u32x2 v2 = {pack2(sm[8][0], sm[8][1]), pack2(sm[8][2], sm[8][3])};
    st_sc1_x2(pdst + 4096, l * 8, v2);
  } else {
    const ldsp ow = LP + (kh * 9216 + l * 16);
#pragma unroll
    for (int c = 0; c < 9; ++c) *(LAS f32x4*)(ow + c * 1024) = sm[c];
  }
  asm volatile("s_waitcnt vmcnt(0)" : "+v"(r0), "+v"(r1), "+v"(r2)::"memory");
  LDS_BARRIER();
  ST(21);
  const char* fl = ws + OFF_FLG + (tg2 * 4 + dt) * 256;
  flag_store(w, opq(0u), opq(1u), fl + ks * 64);
  const ldsp orp = LP + (kh * 9216 + l * 16);
  f32x4 o0 = *(const LAS f32x4*)(orp + tq * 2048), o1 = *(const LAS f32x4*)(orp + (tq * 2048 + 1024)), o2 = *(const LAS f32x4*)(orp + 8192);
  o0[0] += bflo(r0[0]); o0[1] += bfhi(r0[0]); o0[2] += bflo(r0[1]); o0[3] += bfhi(r0[1]);
  o1[0] += bflo(r1[0]); o1[1] += bfhi(r1[0]); o1[2] += bflo(r1[1]); o1[3] += bfhi(r1[1]);
  o2[0] += bflo(r2[0]); o2[1] += bfhi(r2[0]); o2[2] += bflo(r2[1]); o2[3] += bfhi(r2[1]);
  {
    const u32 fo = (l & 3) * 64;
    poll_issue(pv, fo, fl);
    poll_check<0, 1>(pv, fo, fl);
  }
  ST(22);
  {
    const char* psrc = part + (((size_t)(tg2 * 4 + dt) * 4 + ks) * 4 * 2 + kh) * 4608;
    u32x4 v[3]; u32x2 vq[3];
#pragma unroll
    for (int j = 0; j < 3; ++j) {
      const int src = (ks + 1 + j) & 3;
      ld_sc1_x4(v[j], lo16 + tq * 1024, psrc + src * 9216);
      ld_sc1_x2<0>(vq[j], l * 8 + 4096, psrc + src * 9216);
    }
    asm volatile("s_waitcnt vmcnt(0)" : "+v"(v[0]), "+v"(v[1]), "+v"(v[2]), "+v"(vq[0]), "+v"(vq[1]), "+v"(vq[2])::"memory");
#pragma unroll
    for (int j = 0; j < 3; ++j) {
      o0[0] += bflo(v[j][0]); o0[1] += bfhi(v[j][0]); o0[2] += bflo(v[j][1]); o0[3] += bfhi(v[j][1]);
      o1[0] += bflo(v[j][2]); o1[1] += bfhi(v[j][2]); o1[2] += bflo(v[j][3]); o1[3] += bfhi(v[j][3]);
      o2[0] += bflo(vq[j][0]); o2[1] += bfhi(vq[j][0]); o2[2] += bflo(vq[j][1]); o2[3] += bfhi(vq[j][1]);
    }
    u16* op = out + (size_t)(ft * 16 + (l & 15)) * D_;
    u32x2 oa = {pack2(o0[0], o0[1]), pack2(o0[2], o0[3])}, ob = {pack2(o1[0], o1[1]), pack2(o1[2], o1[3])}, oc = {pack2(o2[0], o2[1]), pack2(o2[2], o2[3])};
    *(u32x2*)(op + dcol) = oa; *(u32x2*)(op + dcol + 16) = ob;
    if (tq == 0) *(u32x2*)(op + qcol) = oc;
  }
  asm volatile(".Lffn_code_end:");
  ST(23); ST_DRAIN(); ST(24);
  ST_FLUSH((u32*)(ws + (200u << 20)), b * 8 + w);
}

extern "C" void kernel_launch(void* const* d_in, const int* in_sizes, int n_in, void* d_out, int out_size, void* d_ws, size_t ws_size,
                              hipStream_t stream) {
  const u16* q = (const u16*)d_in[0]; const u16* k = (const u16*)d_in[1]; const u16* v = (const u16*)d_in[2];
  const u16* shk = (const u16*)d_in[3]; const u16* ssm = (const u16*)d_in[4]; const u16* ssmD = (const u16*)d_in[5];
  const u16* wg = (const u16*)d_in[7]; const u16* wu = (const u16*)d_in[8]; const u16* wd = (const u16*)d_in[9];
  char* ws = (char*)d_ws;
  (void)hipFuncSetAttribute((const void*)k1_conv, hipFuncAttributeMaxDynamicSharedMemorySize, K1_LDS);
  (void)hipFuncSetAttribute((const void*)ffn_fused, hipFuncAttributeMaxDynamicSharedMemorySize, FF_LDS);
  k0_prep<<<K0_GRID, 256, 0, stream>>>(q, k, v, shk, ssm, wg, wu, ws);
  k1_conv<<<K1_GRID, 512, K1_LDS, stream>>>(ssm, ssmD, wg, wu, ws);
  ffn_fused<<<256, 512, FF_LDS, stream>>>(wg, wu, wd, ws, (u16*)d_out);
}
```

```cpp
#include <hip/hip_runtime.h>
#include <hip/hip_bf16.h>
#include <stdint.h>
#include <utility>
#define G2_BAR_ALL 0
#define WE0 7
#define WE1 1
#define HD2 4
#define LASTX 2
#define HH_PLAIN 0
#define HLOAD bload_sc1
#define NT0 3
#define LDK(p) ((NT0 & 1) ? __builtin_nontemporal_load(p) : *(p))
#define LDV(p) ((NT0 & 2) ? __builtin_nontemporal_load(p) : *(p))
#define LDQ(p) ((NT0 & 4) ? __builtin_nontemporal_load(p) : *(p))
#define TLBW 2
#define SIDE_DELAY 4

typedef unsigned short u16;
typedef unsigned int u32;
typedef __attribute__((ext_vector_type(2))) unsigned u32x2;
typedef __attribute__((ext_vector_type(4))) unsigned u32x4;
typedef __attribute__((ext_vector_type(2))) float f32x2;
typedef __attribute__((ext_vector_type(4))) float f32x4;
typedef __attribute__((ext_vector_type(8))) __bf16 bf16x8;
typedef __attribute__((ext_vector_type(2))) __bf16 bf16x2;
typedef __attribute__((ext_vector_type(4))) short s16x4;
#define GAS __attribute__((address_space(1)))
#define LAS __attribute__((address_space(3)))
#define DI __device__ __forceinline__
typedef LAS char* ldsp;

constexpr int S_ = 2048, D_ = 576, DF_ = 4608;
constexpr size_t OFF_KVT = 0;
constexpr size_t OFF_XP  = OFF_KVT + (size_t)D_ * S_ * 2;
constexpr size_t OFF_HF  = OFF_XP + (size_t)D_ * S_ * 2;
constexpr size_t OFF_ST1 = OFF_HF + (size_t)D_ * S_ * 2;
constexpr size_t OFF_CS  = OFF_ST1 + 2048 * 8;
constexpr size_t OFF_FLG = OFF_CS + 4 * 2 * 4608 * 4;
constexpr size_t OFF_ST2 = OFF_FLG + 36864;
constexpr size_t OFF_HH  = OFF_ST2 + 131072;
constexpr size_t OFF_PART = OFF_HH + (size_t)S_ * DF_ * 2;
constexpr size_t WS_USED = OFF_PART + 64 * 4 * 4 * 2 * 4608;
static_assert(WS_USED - OFF_HF <= 512 * 65536, "translation warm-up covers 32 MiB");

DI float bflo(u32 w) { return __uint_as_float(w << 16); }
DI float bfhi(u32 w) { return __uint_as_float(w & 0xffff0000u); }
DI u32 pack2(float a, float b) { bf16x2 p; p[0] = (__bf16)a; p[1] = (__bf16)b; return __builtin_bit_cast(u32, p); }

template <int I> struct IC { static constexpr int value = I; };
template <int... I> struct Seq {};
template <int B, class F, int... I> DI void static_for_impl(F&& f, std::integer_sequence<int, I...>) { (f(IC<B + I>{}), ...); }
template <int B, int E, class F> DI void static_for(F&& f) { static_for_impl<B>(f, std::make_integer_sequence<int, E - B>{}); }
template <int N> DI void wait_vm() { asm volatile("s_waitcnt vmcnt(%0)" ::"n"(N) : "memory"); }
#define BARRIER() do { asm volatile("" ::: "memory"); __builtin_amdgcn_s_barrier(); asm volatile("" ::: "memory"); } while (0)
#define LDS_BARRIER() do { asm volatile("s_waitcnt lgkmcnt(0)" ::: "memory"); __builtin_amdgcn_s_barrier(); asm volatile("" ::: "memory"); } while (0)
#define SCHED0() __builtin_amdgcn_sched_barrier(0)
constexpr int cmin(int a, int b) { return a < b ? a : b; }

DI u32x4 tr_frag(ldsp a0, ldsp a1) {
  const s16x4 lo = __builtin_amdgcn_ds_read_tr16_b64_v4i16((LAS s16x4*)a0);
  const s16x4 hi = __builtin_amdgcn_ds_read_tr16_b64_v4i16((LAS s16x4*)a1);
  return __builtin_bit_cast(u32x4, __builtin_shufflevector(lo, hi, 0, 1, 2, 3, 4, 5, 6, 7));
}
struct Frag { s16x4 lo, hi; };
DI s16x4 tr_half(ldsp a) { return __builtin_amdgcn_ds_read_tr16_b64_v4i16((LAS s16x4*)a); }
DI u32x4 frag_of(const Frag& f) { return __builtin_bit_cast(u32x4, __builtin_shufflevector(f.lo, f.hi, 0, 1, 2, 3, 4, 5, 6, 7)); }
DI f32x4 mfma16(const u32x4& a, const u32x4& b, const f32x4& c) {
  return __builtin_amdgcn_mfma_f32_16x16x32_bf16(__builtin_bit_cast(bf16x8, a), __builtin_bit_cast(bf16x8, b), c, 0, 0, 0);
}
template <bool FIRST> DI void mma(f32x4& c, const u32x4& a, const u32x4& b) {
  if constexpr (FIRST) asm volatile("v_mfma_f32_16x16x32_bf16 %0, %1, %2, 0" : "=&v"(c) : "v"(a), "v"(b));
  else asm volatile("v_mfma_f32_16x16x32_bf16 %0, %1, %2, %0" : "+v"(c) : "v"(a), "v"(b));
}
#define MFMA_DRAIN() asm volatile("s_nop 15" ::: "memory")
template <int OFF> DI void gload16_sc1(u32x4& dst, u32 voff, const char* sbase) {
  asm volatile("global_load_dwordx4 %0, %1, %2 offset:%3 sc1" : "=v"(dst) : "v"(voff), "s"(sbase), "n"(OFF) : "memory");
}
DI void poll_issue(u32& f, u32 voff, const char* sb) { asm volatile("global_load_dword %0, %1, %2 sc1" : "=v"(f) : "v"(voff), "s"(sb) : "memory"); }
template <int N, int PH> DI void poll_check(u32& f, u32 voff, const char* sb) {
  u32 cnt = 1u << 18;
  asm volatile("s_waitcnt vmcnt(%4)\n"
               "1:\n\tv_cmp_gt_u32_e32 vcc, %5, %0\n\ts_nop 4\n\ts_cbranch_vccz 2f\n\t"
               "s_sleep 1\n\tglobal_load_dword %0, %2, %3 sc1\n\ts_waitcnt vmcnt(0)\n\t"
               "s_sub_u32 %1, %1, 1\n\ts_cbranch_scc0 1b\n"
               "2:" : "+v"(f), "+s"(cnt) : "v"(voff), "s"(sb), "n"(N), "n"(PH) : "memory", "vcc", "scc");
}
DI void poll4(u32 voff, const char* sb) {
  u32 cnt = 1u << 18;
  u32x4 f; u32 m;
  asm volatile("1:\n\tglobal_load_dwordx4 %0, %3, %4 sc1\n\ts_waitcnt vmcnt(0)\n\t"
               "v_min3_u32 %1, v100, v101, v102\n\tv_min_u32_e32 %1, %1, v103\n\tv_cmp_eq_u32_e32 vcc, 0, %1\n\ts_nop 4\n\ts_cbranch_vccz 2f\n\t"
               "s_sleep 4\n\ts_sub_u32 %2, %2, 1\n\ts_cbranch_scc0 1b\n"
               "2:" : "=&{v[100:103]}"(f), "=&v"(m), "+s"(cnt) : "v"(voff), "s"(sb) : "memory", "vcc", "scc");
}
DI void swap_halves(u32x4& x, u32x4& y) {
  u32 a = x[2], b = x[3], c = y[0], d = y[1];
  asm volatile("v_swap_b32 %0, %2\n\tv_swap_b32 %1, %3" : "+v"(a), "+v"(b), "+v"(c), "+v"(d));
  x[2] = a; x[3] = b; y[0] = c; y[1] = d;
}
DI void flag_store(int w, u32 zero_off, u32 val, const char* sb) {
  asm volatile("s_cmp_lg_u32 %3, 0\n\ts_cbranch_scc1 1f\n\tglobal_store_dword %0, %1, %2 sc1\n1:" ::"v"(zero_off), "v"(val), "s"(sb), "s"(w) : "memory", "scc");
}
template <int OFF> DI void gload16(u32x4& dst, u32 voff, const char* sbase) {
  asm volatile("global_load_dwordx4 %0, %1, %2 offset:%3" : "=v"(dst) : "v"(voff), "s"(sbase), "n"(OFF) : "memory");
}
DI void gload8(u32x2& dst, u32 voff, const char* sbase) { asm volatile("global_load_dwordx2 %0, %1, %2" : "=v"(dst) : "v"(voff), "s"(sbase) : "memory"); }
template <int N, int VAL> DI void waitbar_if(int sel, u32x4& a, u32x4& b) {
  asm volatile("s_cmp_lg_u32 %2, %4\n\ts_cbranch_scc1 1f\n\ts_waitcnt vmcnt(%3)\n\ts_barrier\n1:" : "+v"(a), "+v"(b) : "s"(sel), "n"(N), "n"(VAL) : "memory", "scc");
}
template <int N> DI void wait_vm2(u32x4& a, u32x4& b) { asm volatile("s_waitcnt vmcnt(%2)" : "+v"(a), "+v"(b) : "n"(N) : "memory"); }
DI void dma16(const char* sbase, u32 voff, u32 lds_dst) {
  asm volatile("s_mov_b32 m0, %2\n\ts_nop 0\n\tglobal_load_lds_dwordx4 %0, %1" ::"v"(voff), "s"(sbase), "s"(lds_dst) : "memory");
}
template <int VAL> DI void dma16_if(int sel, const char* sbase, u32 voff, u32 lds_dst) {
  asm volatile("s_cmp_eq_u32 %3, %4\n\ts_mov_b32 m0, %2\n\ts_cselect_b64 exec, -1, 0\n\tglobal_load_lds_dwordx4 %0, %1\n\ts_mov_b64 exec, -1" ::"v"(voff), "s"(sbase), "s"(lds_dst), "s"(sel), "n"(VAL) : "memory", "scc");
}
typedef __amdgpu_buffer_rsrc_t rsrc_t;
DI rsrc_t mk_rsrc(const void* p, u32 bytes) { return __builtin_amdgcn_make_buffer_rsrc((void*)p, (short)0, (int)bytes, 0x00020000); }
template <int LOFF> DI void bdma(rsrc_t r, u32 voff, u32 soff, u32 ldsb) {
  asm volatile("s_add_u32 m0, %3, %4\n\ts_nop 0\n\tbuffer_load_dwordx4 %0, %1, %2 offen lds" ::"v"(voff), "s"(r), "s"(soff), "s"(ldsb), "n"(LOFF) : "memory", "scc");
}
template <int LOFF, u32 SOFF> DI void bdma_i(rsrc_t r, u32 voff, u32 ldsb) {
  u32 so;
  asm volatile("s_add_u32 m0, %3, %4\n\ts_mov_b32 %0, %5\n\tbuffer_load_dwordx4 %1, %2, %0 offen lds" : "=&s"(so) : "v"(voff), "s"(r), "s"(ldsb), "n"(LOFF), "n"(SOFF) : "memory", "scc");
}
template <int LOFF, u32 SOFF> DI void bdma_a(rsrc_t r, u32 voff, u32 sbase, u32 ldsb) {
  u32 so;
  asm volatile("s_add_u32 m0, %3, %4\n\ts_add_u32 %0, %6, %5\n\tbuffer_load_dwordx4 %1, %2, %0 offen lds" : "=&s"(so) : "v"(voff), "s"(r), "s"(ldsb), "n"(LOFF), "n"(SOFF), "s"(sbase) : "memory", "scc");
}
template <int X> DI ldsp lds_base_t(u32 rb, u32 off) {
  u32 r;
  if constexpr (X == 96) { asm volatile("v_xor_b32_e32 %0, 0x60, %1\n\tv_add_u32_e32 %0, %2, %0" : "=&v"(r) : "v"(rb), "s"(off)); }
  else asm volatile("v_xad_u32 %0, %1, %2, %3" : "=v"(r) : "v"(rb), "n"(X), "s"(off));
  return (ldsp)(uintptr_t)r;
}
DI ldsp lds_base(u32 rb, int x, u32 off) { return x == 0 ? lds_base_t<0>(rb, off) : x == 32 ? lds_base_t<32>(rb, off) : x == 64 ? lds_base_t<64>(rb, off) : lds_base_t<96>(rb, off); }
DI void touch(const Frag& f) { asm volatile("" ::"v"(f.lo), "v"(f.hi)); }
template <int OFF> DI void bload(u32x4& d, rsrc_t r, u32 voff, u32 soff) {
  asm volatile("buffer_load_dwordx4 %0, %1, %2, %3 offen offset:%4" : "=v"(d) : "v"(voff), "s"(r), "s"(soff), "n"(OFF) : "memory");
}
template <int OFF> DI void bload_sc1(u32x4& d, rsrc_t r, u32 voff, u32 soff) {
  asm volatile("buffer_load_dwordx4 %0, %1, %2, %3 offen offset:%4 sc1" : "=v"(d) : "v"(voff), "s"(r), "s"(soff), "n"(OFF) : "memory");
}
DI void stat_loads(int fg, u32x4 (&x)[6], rsrc_t r, u32 voff, u32 soff, u32 soff2) {
  asm volatile("s_cmp_gt_u32 %10, 5\n\ts_cbranch_scc1 1f\n\t"
               "buffer_load_dwordx4 %0, %6, %7, %8 offen\n\tbuffer_load_dwordx4 %1, %6, %7, %8 offen offset:1024\n\t"
               "buffer_load_dwordx4 %2, %6, %7, %8 offen offset:2048\n\tbuffer_load_dwordx4 %3, %6, %7, %8 offen offset:3072\n\t"
               "buffer_load_dwordx4 %4, %6, %7, %9 offen\n\tbuffer_load_dwordx4 %5, %6, %7, %9 offen offset:1024\n1:"
               : "=&v"(x[0]), "=&v"(x[1]), "=&v"(x[2]), "=&v"(x[3]), "=&v"(x[4]), "=&v"(x[5])
               : "v"(voff), "s"(r), "s"(soff), "s"(soff2), "s"(fg) : "memory", "scc");
}
DI void code_touch(const char* base, u32 voff, u32 lds_dst) {
  asm volatile("s_mov_b32 m0, %2\n\ts_nop 0\n\tglobal_load_lds_dword %0, %1" ::"v"(voff), "s"(base), "s"(lds_dst) : "memory");
}
#define CODE_RANGE(base, len, endlabel) \
  asm volatile("s_getpc_b64 s[40:41]\n\ts_mov_b64 %0, s[40:41]\n\ts_mov_b32 %1, " endlabel "@rel32@lo-4" : "=s"(base), "=s"(len)::"s40", "s41")
DI ldsp opaque(ldsp p) { asm volatile("" : "+v"(p)); return p; }
DI u32 opq(u32 p) { asm volatile("" : "+v"(p)); return p; }
DI void pin(u32& p) { asm volatile("" : "+v"(p)); }
DI void pin6(u32x4 (&x)[6]) { asm volatile("" : "+v"(x[0]), "+v"(x[1]), "+v"(x[2]), "+v"(x[3]), "+v"(x[4]), "+v"(x[5])); }
DI void pinf(float& p) { asm volatile("" : "+v"(p)); }
DI float hide(float p) { asm("" : "+v"(p)); return p; }
DI float swap16_sum(float x) { const auto r = __builtin_amdgcn_permlane16_swap(__float_as_uint(x), __float_as_uint(x), false, false); return __uint_as_float(r[0]) + __uint_as_float(r[1]); }
DI float swap32_sum(float x) { const auto r = __builtin_amdgcn_permlane32_swap(__float_as_uint(x), __float_as_uint(x), false, false); return __uint_as_float(r[0]) + __uint_as_float(r[1]); }
#define STAT1(R, A, B) "v_lshlrev_b32_e32 %0, 16, " R "\n\tv_and_b32_e32 %1, %10, " R "\n\tv_add_f32_e32 " A ", " A ", %0\n\tv_fmac_f32_e32 " B ", %0, %0\n\tv_add_f32_e32 " A ", " A ", %1\n\tv_fmac_f32_e32 " B ", %1, %1\n\t"
DI void stat_acc(int fg, const u32x4& v, float (&s1)[2], float (&s2)[2]) {
  float x, y;
  asm volatile("s_cmp_gt_u32 %11, 5\n\ts_cbranch_scc1 1f\n\t" STAT1("%6", "%2", "%3") STAT1("%8", "%4", "%5") STAT1("%7", "%2", "%3") STAT1("%9", "%4", "%5") "\n1:"
               : "=&v"(x), "=&v"(y), "+v"(s1[0]), "+v"(s2[0]), "+v"(s1[1]), "+v"(s2[1]) : "v"(v[0]), "v"(v[1]), "v"(v[2]), "v"(v[3]), "s"(0xffff0000u), "s"(fg) : "scc");
}
DI void ld_sc1_x4(u32x4& d, u32 voff, const char* sb) { asm volatile("global_load_dwordx4 %0, %1, %2 sc1" : "=v"(d) : "v"(voff), "s"(sb) : "memory"); }
template <int OFF> DI void ld_sc1_x2(u32x2& d, u32 voff, const char* sb) { asm volatile("global_load_dwordx2 %0, %1, %2 offset:%3 sc1" : "=v"(d) : "v"(voff), "s"(sb), "n"(OFF) : "memory"); }
DI void wait0_x6(u32x4& a, u32x2& b) { asm volatile("s_waitcnt vmcnt(0)" : "+v"(a), "+v"(b)::"memory"); }
DI void st_sc1_x4(char* sb, u32 voff, const u32x4& v) { asm volatile("global_store_dwordx4 %0, %1, %2 sc1\n\ts_nop 1" ::"v"(voff), "v"(v), "s"(sb) : "memory"); }
DI void gstore16(char* sb, u32 voff, const u32x4& v) { asm volatile("global_store_dwordx4 %0, %1, %2\n\ts_nop 1" ::"v"(voff), "v"(v), "s"(sb) : "memory"); }
DI void gstore8(char* sb, u32 voff, const u32x2& v) { asm volatile("global_store_dwordx2 %0, %1, %2" ::"v"(voff), "v"(v), "s"(sb) : "memory"); }
DI void st_sc1_x2_if(int skip, const char* sb, u32 voff, const u32x2& v) {
  asm volatile("s_cmp_gt_u32 %3, 5\n\ts_cbranch_scc1 1f\n\tglobal_store_dwordx2 %0, %1, %2 sc1\n1:" ::"v"(voff), "v"(v), "s"(sb), "s"(skip) : "memory", "scc");
}
DI void st_sc1_x2(char* sb, u32 voff, const u32x2& v) { asm volatile("global_store_dwordx2 %0, %1, %2 sc1" ::"v"(voff), "v"(v), "s"(sb) : "memory"); }

#define STR(i)
#define ST(i)
#define ST_DECL
#define ST_FLUSH(dbg, wv)
#define ST_DRAIN()

DI void colsum_slice(const u16* __restrict__ wg, const u16* __restrict__ wu, float* __restrict__ cs, int sl, int l) {
  const int mat = sl & 1, qr = (sl >> 1) & 3, cb = sl >> 3;
  const u16* p = (mat ? wu : wg) + (size_t)(qr * 144 + (l >> 3)) * DF_ + cb * 64 + (l & 7) * 8;
  u32x4 x[18];
#pragma unroll
  for (int i = 0; i < 18; ++i) x[i] = *(const u32x4*)(p + (size_t)i * 8 * DF_);
  float a[8];
#pragma unroll
  for (int e = 0; e < 8; ++e) a[e] = 0.f;
#pragma unroll
  for (int i = 0; i < 18; ++i)
#pragma unroll
    for (int e = 0; e < 4; ++e) { a[2 * e] += bflo(x[i][e]); a[2 * e + 1] += bfhi(x[i][e]); }
#pragma unroll
  for (int e = 0; e < 8; ++e) { a[e] += __shfl_xor(a[e], 8); a[e] = swap32_sum(swap16_sum(a[e])); }
  if (l < 8) {
    float* d = cs + qr * 9216 + (cb * 32 + l * 4) * 4 + mat * 2;
#pragma unroll
    for (int e = 0; e < 4; ++e) { f32x2 o = {a[2 * e], a[2 * e + 1]}; *(f32x2*)(d + e * 4) = o; }
  }
}

constexpr int K0_GRID = 64 + 288;
template <bool NT> DI void dma16p(const char* sbase, u32 voff, u32 lds_dst) {
  if constexpr (NT) asm volatile("s_mov_b32 m0, %2\n\ts_nop 0\n\tglobal_load_lds_dwordx4 %0, %1 nt" ::"v"(voff), "s"(sbase), "s"(lds_dst) : "memory");
  else dma16(sbase, voff, lds_dst);
}
extern "C" __global__ __launch_bounds__(256) void k0_prep(const u16* __restrict__ q, const u16* __restrict__ k, const u16* __restrict__ v,
                                                          const u16* __restrict__ shk, const u16* __restrict__ ssm, char* __restrict__ ws,
                                                          const u16* __restrict__ wg, const u16* __restrict__ wu, const u16* __restrict__ ssmD) {
  __shared__ __attribute__((aligned(128))) char lds[(72 + 64 + 64 + 64) * 128 + 256];
  ST_DECL; ST(0);
  asm volatile("" ::"s"(q), "s"(k), "s"(v), "s"(shk), "s"(ssm), "s"(ws));
  const int tid = threadIdx.x, l = tid & 63;
  const int w = __builtin_amdgcn_readfirstlane(tid >> 6);
  if (blockIdx.x >= 64) {
    const int b = blockIdx.x - 64;
    const int r8 = b >> 3, tb = r8 < 32 ? r8 : (b & 7) * 4 + (r8 - 32), cb = r8 < 32 ? (b & 7) : 8;
    const ldsp LP = (ldsp)lds;
    const u32 L0 = (u32)(uintptr_t)LP;
    constexpr int KT = 0, VT = 72 * 128, QT = VT + 8192, OT = QT + 8192;
    const int col = cb * 128 + (l & 7) * 16, rl = l >> 3;
    {
      const int r0 = tb * 64 - 8 + w * 8 + rl;
      dma16p<true>((const char*)k, (u32)(max(r0, 0) * (D_ * 2) + col), L0 + KT + w * 1024);
      const u32 vo = (u32)((tb * 64 + w * 8 + rl) * (D_ * 2) + col);
      dma16p<true>((const char*)k + 24 * D_ * 2, vo, L0 + KT + (w + 4) * 1024);
      if (w == 0) dma16p<true>((const char*)k + 56 * D_ * 2, vo, L0 + KT + 8 * 1024);
      dma16p<true>((const char*)v, vo, L0 + VT + w * 1024);
      dma16p<true>((const char*)v + 32 * D_ * 2, vo, L0 + VT + (w + 4) * 1024);
    }
    const int sl = w * 16 + (l >> 4) * 4, cq = l & 15;
    u32x4 w0, w1, pf0, pf1;
    gload16<0>(w0, (u32)((cb * 64 + cq * 4) * 8), (const char*)shk);
    gload16<16>(w1, (u32)((cb * 64 + cq * 4) * 8), (const char*)shk);
    {
      const u32 vo = (u32)((tb * 64 + w * 8 + rl) * (D_ * 2) + col);
      dma16p<false>((const char*)q, vo, L0 + QT + w * 1024);
      dma16p<false>((const char*)q + 32 * D_ * 2, vo, L0 + QT + (w + 4) * 1024);
    }
    ST(3);
    wait_vm2<2>(w0, w1);
    BARRIER();
    ST(4);
    u32x2 kr[7], vr[4];
#pragma unroll
    for (int i = 0; i < 7; ++i) kr[i] = *(const LAS u32x2*)(LP + (KT + (sl + 5 + i) * 128 + cq * 8));
#pragma unroll
    for (int i = 0; i < 4; ++i) vr[i] = *(const LAS u32x2*)(LP + (VT + (sl + i) * 128 + cq * 8));
    const u32 po = (u32)((r8 < 32 ? (b & 7) * 64 + r8 * 2 : 512 + (b & 7) * 8 + (r8 - 32) * 2) * 4096 + tid * 16);
    gload16<0>(pf0, po, (const char*)ssm);
    gload16<0>(pf1, po + 4096, (const char*)ssm);
    if (tb == 0 && sl == 0) { kr[0] = (u32x2){0u, 0u}; kr[1] = kr[0]; kr[2] = kr[0]; }
#pragma unroll
    for (int c = 0; c < 4; ++c) {
      const u32 wa = (c < 2) ? w0[2 * (c & 1)] : w1[2 * (c & 1)], wb = (c < 2) ? w0[2 * (c & 1) + 1] : w1[2 * (c & 1) + 1];
      const float f0 = bflo(wa), f1 = bfhi(wa), f2 = bflo(wb), f3 = bfhi(wb);
      float kk[7], kvv[4];
#pragma unroll
      for (int i = 0; i < 7; ++i) kk[i] = (c & 1) ? bfhi(kr[i][c >> 1]) : bflo(kr[i][c >> 1]);
#pragma unroll
      for (int i = 0; i < 4; ++i) {
        const float ks = kk[i + 3] * f0 + kk[i + 2] * f1 + kk[i + 1] * f2 + kk[i] * f3;
        const float vv = (c & 1) ? bfhi(vr[i][c >> 1]) : bflo(vr[i][c >> 1]);
        kvv[i] = ks * vv;
      }
      u32x2 o = {pack2(kvv[3], kvv[2]), pack2(kvv[1], kvv[0])};
      *(LAS u32x2*)(LP + (OT + (cq * 4 + c) * 128 + (((sl >> 3) ^ (cq & 7)) * 16) + ((sl & 4) ? 0 : 8))) = o;
    }
    wait_vm<2>();
    LDS_BARRIER();
#pragma unroll
    for (int i = 0; i < 2; ++i) {
      const int ch = w * 16 + i * 8 + (l >> 3);
      const u32x4 o = *(const LAS u32x4*)(LP + (OT + (w * 16 + i * 8) * 128 + l * 16));
      gstore16(ws + OFF_KVT, (u32)((cb * 64 + ch) * 4096 + (tb * 8 + ((l & 7) ^ ((ch >> 2) & 7))) * 16), o);
    }
#pragma unroll
    for (int i = 0; i < 2; ++i) {
      const int gl = i * 8 + (l >> 3), t2 = w * 16 + (l & 7) * 2;
      const u32x2 a = *(const LAS u32x2*)(LP + (QT + t2 * 128 + gl * 8)), c = *(const LAS u32x2*)(LP + (QT + (t2 + 1) * 128 + gl * 8));
      u32x4 o = {a[0], a[1], c[0], c[1]};
      gstore16(ws + OFF_XP, (u32)(((cb * 16 + gl) * S_ + tb * 64 + t2) * 8), o);
    }
    if (b < 41) {
      const u32 f = b < 9 ? 0u : 0xffffffffu;
      u32x4 z = {f, f, f, f};
      gstore16(ws + OFF_FLG, (u32)(b * 4096 + tid * 16), z);
    }
    ST(5);
    asm volatile("s_waitcnt vmcnt(0)" : "+v"(pf0), "+v"(pf1)::"memory");
  } else {
    if (w == 1 && blockIdx.x < 8) {
      const u32 sc = (u32)(uintptr_t)(ldsp)lds + 256;
      code_touch((const char*)wg, min((u32)l << 20, (u32)(D_ * DF_ * 2 - 4)), sc);
      code_touch((const char*)wu, min((u32)l << 20, (u32)(D_ * DF_ * 2 - 4)), sc);
      code_touch((const char*)ssmD, opq(0u), sc);
      code_touch(ws, min((u32)l << 20, (u32)OFF_FLG), sc);
    }
    if (w == 0) {
      unsigned long long c0;
      asm volatile("s_getpc_b64 s[40:41]\n\ts_add_u32 s40, s40, k1_conv@rel32@lo+4\n\ts_addc_u32 s41, s41, k1_conv@rel32@hi+12\n\ts_mov_b64 %0, s[40:41]" : "=s"(c0)::"s40", "s41", "scc");
      code_touch((const char*)c0, (u32)(l * 128), (u32)(uintptr_t)(ldsp)lds);
    }
    const int row = blockIdx.x * 32 + w * 8 + (l >> 3);
    const u16* p = q + (size_t)row * D_ + (l & 7) * 8;
    u32x4 x[9];
#pragma unroll
    for (int i = 0; i < 9; ++i) x[i] = *(const u32x4*)(p + i * 64);
    float sum = 0.f;
#pragma unroll
    for (int i = 0; i < 9; ++i)
#pragma unroll
      for (int j = 0; j < 4; ++j) sum += bflo(x[i][j]) + bfhi(x[i][j]);
    sum += __shfl_xor(sum, 1); sum += __shfl_xor(sum, 2); sum += __shfl_xor(sum, 4);
    const float mu = sum * (1.f / D_);
    float sq = 0.f;
#pragma unroll
    for (int i = 0; i < 9; ++i)
#pragma unroll
      for (int j = 0; j < 4; ++j) { const float a = bflo(x[i][j]) - mu, c = bfhi(x[i][j]) - mu; sq += a * a + c * c; }
    sq += __shfl_xor(sq, 1); sq += __shfl_xor(sq, 2); sq += __shfl_xor(sq, 4);
    if ((l & 7) == 0) { f32x2 o = {mu, rsqrtf(sq * (1.f / D_) + 1e-5f)}; ((f32x2*)(ws + OFF_ST1))[row] = o; }
  }
  ST(1); ST_DRAIN(); ST(2);
  ST_FLUSH((u32*)(ws + (202u << 20)), (blockIdx.x >= 64 ? blockIdx.x - 64 : blockIdx.x + 288) * 4 + w);
}

constexpr int K1_UPLANE = 2304, K1_UCH = 4608, K1_TOFF = 4 * K1_UCH, K1_TCOPY = 4160, K1_TCH = 4 * K1_TCOPY;
constexpr int K1_YOFF = K1_TOFF + 4 * K1_TCH, K1_YBUF = 8192;
constexpr int K1_LDS = K1_YOFF + 8 * K1_YBUF;
constexpr int K1_GRID = 256;

#define HSTORE gstore16
extern "C" __global__ __launch_bounds__(512) void k1_conv(const u16* __restrict__ ssm, const u16* __restrict__ ssmD, const u16* __restrict__ wg,
                                                          const u16* __restrict__ wu, char* __restrict__ ws, const u16* __restrict__ wd, const u16* __restrict__ outp) {
  extern __shared__ __attribute__((aligned(128))) char lds[];
  ST_DECL; ST(0);
  asm volatile("" ::"s"(ssm), "s"(ssmD), "s"(wg), "s"(wu), "s"(ws), "s"(wd), "s"(outp));
  const int tid = threadIdx.x, l = tid & 63;
  const int w = __builtin_amdgcn_readfirstlane(tid >> 6);
  const int b = blockIdx.x;
  const ldsp LP = (ldsp)lds;
  const u32 L0 = (u32)(uintptr_t)LP;
  if (b >= 144) {
    if (w == 6 && b < 152) {
      code_touch(ws + OFF_HF, min((u32)l << 20, (u32)(WS_USED - OFF_HF - 4)), L0 + 256);
      code_touch((const char*)wd, min((u32)l << 20, (u32)(D_ * DF_ * 2 - 4)), L0 + 256);
      code_touch((const char*)outp, min((u32)l << 20, (u32)(S_ * D_ * 2 - 4)), L0 + 256);
    }
    if (w == 7) {
      unsigned long long c0; u32 cl;
      asm volatile("s_getpc_b64 s[40:41]\n\ts_add_u32 s40, s40, ffn_fused@rel32@lo+4\n\ts_addc_u32 s41, s41, ffn_fused@rel32@hi+12\n\ts_mov_b64 %0, s[40:41]\n\ts_mov_b32 %1, .Lffn_code_end-ffn_fused"
                   : "=s"(c0), "=s"(cl)::"s40", "s41", "scc");
#pragma unroll
      for (int i = 0; i < 5; ++i) code_touch((const char*)c0, min((u32)(l * 128 + i * 8192), cl - 4u), L0);
    }
    for (int i = 0; i < SIDE_DELAY; ++i) __builtin_amdgcn_s_sleep(8);
    const int j = ((b - 144) >> 3) + 14 * w, jj = j * 2 + ((b >> 2) & 1), cbl = jj >> 3;
    if (j < 72) colsum_slice(wg, wu, (float*)(ws + OFF_CS), (cbl < 16 ? (b & 3) * 16 + cbl : 64 + (b & 3) * 2 + (cbl - 16)) * 8 + (jj & 7), l);
  } else {
    const u16* __restrict__ kvt = (const u16*)(ws + OFF_KVT);
    const u16* __restrict__ xp = (const u16*)(ws + OFF_XP);
    const f32x2* __restrict__ st1 = (const f32x2*)(ws + OFF_ST1);
    const int g = (b >> 3) < 16 ? (b & 7) * 16 + (b >> 3) : 128 + (b & 7) * 2 + ((b >> 3) - 16), ch = w & 3, hf_ = w >> 2;
    const int gch = g * 4 + ch;
    {
      const char* src = (const char*)(kvt + (size_t)gch * S_);
      const u32 vo = (2 * l + hf_) * 16;
      const u32 dst = L0 + ch * K1_UCH + hf_ * K1_UPLANE + 256;
      dma16(src, vo, dst);
      dma16(src + 2048, vo, dst + 1024);
    }
    const u16* krow = ssm + (size_t)gch * S_;
    u32x4 wv[2]; u32x2 wn[2];
  #pragma unroll
    for (int it = 0; it < 2; ++it) {
      const int idx = hf_ * 128 + it * 64 + l;
      wv[it] = *(const u32x4*)(krow + (idx > 0 ? idx - 1 : 0) * 8);
      wn[it] = *(const u32x2*)(krow + idx * 8);
    }
    const u32x4 wl = *(const u32x4*)(krow + opq(255 * 8));
    const u32 dv1 = ssmD[opq(gch)];
    SCHED0();
    u32x2 xr[4]; f32x2 st[4];
  #pragma unroll
    for (int i = 0; i < 4; ++i) { const int s = (tid >> 4) * 32 + (tid & 15) + (i & 1) * 16 + (i >> 1) * 1024; xr[i] = *(const u32x2*)(xp + ((size_t)g * S_ + s) * 4); st[i] = st1[s]; }
    SCHED0();
    const u32x4 z4 = {0u, 0u, 0u, 0u};
    if (l < 16) *(LAS u32x4*)(LP + (ch * K1_UCH + hf_ * K1_UPLANE + l * 16)) = z4;
    const ldsp tb = LP + (K1_TOFF + ch * K1_TCH);
    auto put = [&](int pos  , u32x4 a, u32x2 n) {
      const u32 o0 = __builtin_amdgcn_alignbit(a[1], a[0], 16), o1 = __builtin_amdgcn_alignbit(a[2], a[1], 16), o2 = __builtin_amdgcn_alignbit(a[3], a[2], 16),
                o3 = __builtin_amdgcn_alignbit(n[0], a[3], 16), o4 = __builtin_amdgcn_alignbit(n[1], n[0], 16);
      u32x4 c1 = {o0, o1, o2, o3}, c2 = {a[1], a[2], a[3], n[0]}, c3 = {o1, o2, o3, o4};
      *(LAS u32x4*)(tb + pos * 16) = a;
      *(LAS u32x4*)(tb + (K1_TCOPY + pos * 16)) = c1;
      *(LAS u32x4*)(tb + (2 * K1_TCOPY + pos * 16)) = c2;
      *(LAS u32x4*)(tb + (3 * K1_TCOPY + pos * 16)) = c3;
    };
    if (hf_ == 0 && l == 0) wv[0] = z4;
  #pragma unroll
    for (int it = 0; it < 2; ++it) put(hf_ * 128 + it * 64 + l + 1, wv[it], wn[it]);
    if (hf_ == 1 && l < 2) { u32x2 z2 = {0u, 0u}; put(l ? 257 : 0, l ? wl : z4, z2); }
    ST(1);
    wait_vm<8>();
    LDS_BARRIER();
    ST(2);
    const int a_ = l & 15, q_ = l >> 4;
    const int cpy = (1 + a_) & 3;
    const ldsp taddr = LP + (K1_TOFF + ch * K1_TCH + cpy * K1_TCOPY + 2 * (1 + a_ + 8 * q_ - cpy) + hf_ * 256);
    const ldsp uaddr = LP + (ch * K1_UCH + ((q_ + 1) & 1) * K1_UPLANE + (16 + a_ - (q_ >> 1) - 8 * hf_ - 6) * 16);
    f32x4 acc[8];
    u32x4 dg;
    {
      const int j = 15 - a_ - 8 * q_;
      const u32 dval = hf_ ? 0u : dv1;
  #pragma unroll
      for (int e = 0; e < 4; ++e) dg[e] = (j == 2 * e) ? dval : ((j == 2 * e + 1) ? (dval << 16) : 0u);
    }
    u32x4 U[2][8], T[2][8];
    auto ldU = [&](int e, int j) { return *(const LAS u32x4*)(uaddr + ((3 - e) * 32 + 256 * j)); };
    auto ldT = [&](int e, int m) {
      const u32x2 lo = *(volatile const LAS u32x2*)(taddr + (64 * e + 512 * m)), hi = *(volatile const LAS u32x2*)(taddr + (8 + 64 * e + 512 * m));
      u32x4 t = {lo[0], lo[1], hi[0], hi[1]};
      return t;
    };
#pragma unroll
    for (int j = 0; j < 8; ++j) U[0][j] = ldU(0, j);
#pragma unroll
    for (int j = 0; j < 8; ++j) T[0][j] = ldT(0, j);
    asm volatile("s_nop 1" : "+v"(dg));
    SCHED0();
#pragma unroll
    for (int n = 0; n < 8; ++n) mma<true>(acc[n], dg, U[0][n]);
    SCHED0();
    float xs[16], xn[16];
    static_for<0, 144>([&](auto ic) {
      constexpr int e = ic.value / 36, i = ic.value % 36;
      constexpr auto mn = [] { int k = 0; for (int m = 0; m < 8; ++m) for (int n = m; n < 8; ++n) { if (k == i) return m * 8 + n; ++k; } return 0; }();
      constexpr int m = mn >> 3, n = mn & 7;
      if constexpr (e < 3 && i < 32 && (i & 1) == 0) {
        if constexpr ((i & 2) == 0) U[(e + 1) & 1][i >> 2] = ldU(e + 1, i >> 2); else T[(e + 1) & 1][i >> 2] = ldT(e + 1, i >> 2);
      }
      mma<false>(acc[n], T[e & 1][m], U[e & 1][n - m]);
      if constexpr (e == 3 && i < 16) {
        constexpr int ti = i >> 2, c = i & 3;
        xs[i] = (c & 1) ? bfhi(xr[ti][c >> 1]) : bflo(xr[ti][c >> 1]);
        xn[i] = hide(xs[i] - st[ti][0]) * st[ti][1];
        pinf(xs[i]); pinf(xn[i]);
      }
      SCHED0();
    });
    MFMA_DRAIN(); SCHED0();
    ST(3);
    {
      const ldsp yb = LP + (K1_YOFF + (hf_ * 4 + ch) * K1_YBUF + ((4 * a_ + q_) ^ ((a_ >> 1) & 3) ^ ((a_ & 2) << 1)) * 16);
  #pragma unroll
      for (int n = 0; n < 8; ++n) *(LAS f32x4*)(yb + n * 1024) = acc[n];
    }
    LDS_BARRIER();
    ST(4);
    {
      float y[4][8];
#pragma unroll
      for (int i = 0; i < 4; ++i) {
        const int s = (tid >> 4) * 32 + (tid & 15) + (i & 1) * 16, sl = s >> 2;
        const ldsp yb = LP + (K1_YOFF + (sl ^ ((sl >> 3) & 3) ^ ((sl >> 1) & 4)) * 16 + (s & 3) * 4 + (i >> 1) * 4096);
#pragma unroll
        for (int c = 0; c < 8; ++c) y[i][c] = *(const LAS float*)(yb + c * K1_YBUF);
      }
#pragma unroll
      for (int j = 0; j < 2; ++j) {
        u32x4 o;
#pragma unroll
        for (int e = 0; e < 2; ++e) {
          const int i = 2 * j + e;
          float hv[4];
#pragma unroll
          for (int c = 0; c < 4; ++c) hv[c] = fmaf(xn[4 * i + c], hide(y[i][c] + y[i][4 + c]), xs[4 * i + c]);
          o[2 * e] = pack2(hv[0], hv[1]); o[2 * e + 1] = pack2(hv[2], hv[3]);
        }
        HSTORE(ws + OFF_HF + (size_t)j * 32 * 36864 + (g >> 3) * 2048 + (g & 1) * 1024 + ((g >> 1) & 3) * 256, (u32)((tid >> 4) * 36864 + (tid & 15) * 16), o);
      }
    }
    ST(5);
  }
  ST_DRAIN(); ST(6);
  ST_FLUSH((u32*)(ws + (203u << 20)), b * 8 + w);
}

constexpr int G1_QE = 147456;
constexpr int G1_QL = 14 * 8192;
constexpr int G1_CS = G1_QE + 8192;
constexpr int G1_CP = G1_CS + 2048;
constexpr int FF_LDS = G1_CP + 5120 + 256;
constexpr int PF_ISS0 = 86;
constexpr int STG = 18432, NST = 8;
constexpr int G1_D = 4, G1_DW = 6, HD = 5, HB = HD + 1;
constexpr int p1_ops(int x, bool even_only) { return (x + G1_DW < 18 ? 1 : 0) + (x + G1_D < 18 ? (even_only ? 1 : 2) : 0) + (x == 18 - G1_D ? 3 : 0); }
constexpr int p1_wait(int g) {
  int n = 0;
  if (g + 1 < G1_D) n += 3 * (G1_D - (g + 2)) + (G1_DW - G1_D);
  for (int x = (g + 2 - G1_D > 0 ? g + 2 - G1_D : 0); x < g; ++x) n += p1_ops(x, false);
  return n + p1_ops(g, true);
}
constexpr bool p1_bar(int g) { return g < G1_D - 1 || (g - (G1_D - 1)) % (G1_DW - G1_D + 1) == 0; }
constexpr float GC = -2.3022081983f;
constexpr int U2 = 216;
constexpr int g1_q_issue(int half, int r) { return 8 + 6 * r; }
constexpr int g1_b_issue(int half, int j) { return (j < 14 ? 29 + 3 * j : 73 + 3 * (j - 14)); }
constexpr int G1_E0 = 9;
constexpr int epi_start(int i) { return i < 8 ? G1_E0 + 26 * i : U2 + 2; }
static_assert(G1_E0 + 26 * 7 + 24 < 216, "epilogue stream");
constexpr bool epi_stores(int i) { return i == 1 || i == 3 || i == 4 || i == 6 || i == 8; }
constexpr int w_issue(int n, int k) {
  return n < 4 ? 191 + 4 * n + 2 * k : n < 6 ? 209 + 4 * (n - 4) + 2 * k : ((n & 1) || (G2_BAR_ALL && n > 6)) ? U2 + 9 * (n - 6) + 4 * k : U2 + 9 * (n - 6) + WE0 + WE1 * k;
}
constexpr int h_issue(int n, int k) { return n < 5 ? 192 + 4 * n + 2 * k : U2 + 9 * (n - (n < 10 ? HD : 4)) + 2 + 3 * k; }
constexpr int dq_issue(int r) { return r == 0 ? 183 : r < 4 ? U2 + 9 * (4 * r - 3) + 1 : U2 + 9 * 11 + 1; }
constexpr int poll_iss(int p) { return p == 0 ? 180 : p == 1 ? U2 + 9 * 2 + 3 : U2 + 9 * 7 + 3; }
constexpr int poll_chk(int p) { return p == 0 ? 191 : p == 1 ? U2 + 9 * 6 + 1 : U2 + 9 * 10 + 1; }
constexpr int RES_ISS = U2 + 9 * 16 + 7;
template <class F> constexpr int find_at(int n, int u, F f) { for (int i = 0; i < n; ++i) if (f(i) == u) return i; return -1; }
constexpr int cnt_ops(int half, int k0, int k1) {
  int n = 0;
  auto in = [&](int key) { return key > k0 && key < k1 ? 1 : 0; };
  for (int r = 0; r < 4; ++r) n += in(4 * g1_q_issue(half, r) + 2);
  for (int j = 0; j < 18; ++j) n += in(4 * g1_b_issue(half, j) + 2);
  for (int i = 0; i < 8; ++i) if (epi_stores(i)) n += in(4 * (epi_start(i) + 18)) + in(4 * (epi_start(i) + 24));
  n += in(4 * (U2 + 11)) + in(4 * (U2 + 14));
  for (int s = 0; s < 18; ++s) for (int k = 0; k < 2; ++k) n += in(4 * w_issue(s, k) + 2) + in(4 * h_issue(s, k) + 2);
  for (int p = 0; p < 3; ++p) n += in(4 * poll_iss(p) + 2);
  n += 3 * in(4 * RES_ISS + 2);
  for (int r = 0; r < 5; ++r) n += in(4 * (PF_ISS0 + 4 * r) + 2);
  for (int r = 0; r < 5; ++r) n += in(4 * dq_issue(r) + 2);
  return n;
}
constexpr int wait_n(int k0a, int k0b, int k1) { return cmin(cnt_ops(0, k0a, k1), cnt_ops(1, k0b, k1)); }
constexpr int wait_q(int r, int tw) { return wait_n(4 * g1_q_issue(0, r) + 2, 4 * g1_q_issue(1, r) + 2, 4 * tw + 1); }
constexpr int wait_b(int j, int tw) { return wait_n(4 * g1_b_issue(0, j) + 2, 4 * g1_b_issue(1, j) + 2, 4 * tw + 1); }
constexpr int wait_k(int k0, int k1) { return wait_n(k0, k0, k1); }
constexpr int cmax(int a, int b) { return a > b ? a : b; }
static_assert(p1_bar(6) && p1_bar(9), "column-sum hand-over uses these barriers");
static_assert(w_issue(1, 1) > epi_start(6) + 24 && h_issue(0, 0) > poll_chk(0) && h_issue(10, 0) > poll_chk(1) && h_issue(14, 0) > poll_chk(2), "schedule");
constexpr int kk_of(int s) { return s < 8 ? 9 * ((s >> 1) & 1) + 4 * (s & 1) + (s >> 2) : s < 10 ? 9 * (s - 8) + 8 : 9 * (((s - 10) >> 1) & 1) + 4 * (s & 1) + 2 + ((s - 10) >> 2); }
constexpr u32 rowc(int kk) { return (kk % 9 < 8 ? (kk / 9) * 256 + (kk % 9) * 32 : (kk / 9) * 32) * (D_ * 2); }
struct Acc { f32x4 a[2][2]; };

extern "C" __global__ __launch_bounds__(512, 2) void ffn_fused(const u16* __restrict__ wg, const u16* __restrict__ wu, const u16* __restrict__ wd,
                                                               char* __restrict__ ws, u16* __restrict__ out) {
  extern __shared__ __attribute__((aligned(128))) char lds[];
  ST_DECL; ST(0);
  asm volatile("" ::"s"(wg), "s"(wu), "s"(wd), "s"(ws), "s"(out));
  const int tid = threadIdx.x, l = tid & 63;
  const int w = __builtin_amdgcn_readfirstlane(tid >> 6);
  const int b = blockIdx.x;
  const int fg = (b & 3) * 8 + ((b >> 3) & 7), tg = ((b >> 2) & 1) * 4 + (b >> 6);
  const int tt = tg * 8 + w;
  const ldsp LP = (ldsp)lds;
  const u32 L0 = (u32)(uintptr_t)LP;
  constexpr u32 RS = DF_ * 2, GS = 32 * RS;
  const int wblk = w & 1, whr = (w >> 1) & 1, whalf = w >> 2;
  const rsrc_t rW = mk_rsrc((w & 1) ? wu : wg, D_ * DF_ * 2), rS = mk_rsrc(ws, (u32)WS_USED), rD = mk_rsrc(wd, D_ * DF_ * 2);
  const u32 ab_voff = (u32)((16 * whalf + 8 * whr + (l >> 3)) * RS + ((((l & 7) >> 1) ^ (((l >> 4) & 1) | (whr << 1))) * 32) + (l & 1) * 16 + fg * 256);
  const u32 ab_dst = L0 + whalf * 4096 + wblk * 2048 + whr * 1024;
  auto mk_qvoff = [&](u32 l2) { return (u32)(((l2 >> 1) ^ ((l2 >> 2) & 4)) * RS + (l2 & 1) * 16 + (w >> 1) * GS + 8192 + fg * 32); };
  u32x4 h[2][18];
  const u32 lo16 = l * 16;
  const u32 h_voff = lo16 + (u32)OFF_HF + tt * 36864;
  auto issue_dma = [&](auto ic) { constexpr int g = ic.value; bdma_i<g * 8192, g * GS>(rW, ab_voff, ab_dst); };
  auto issue_h = [&](auto ic, auto tc) { constexpr int g = ic.value, tl = tc.value; bload<(g & 1) * 2048 + tl * 1024>(h[tl][g], rS, h_voff, (g >> 1) * 4096); };
  static_for<0, G1_D>([&](auto ic) { issue_dma(ic); issue_h(ic, IC<0>{}); issue_h(ic, IC<1>{}); });
  static_for<G1_D, G1_DW>(issue_dma);
  const char* code0; u32 codelen;
  CODE_RANGE(code0, codelen, ".Lffn_code_end");
  u32x4 sx[6];
  stat_loads(fg, sx, rS, h_voff, fg * 6144, fg * 6144 + 4096);
  ST(1);
  const u32 lw = L0 + w * 1024;
  const int rq = l >> 4, rr = (l >> 2) & 3, rp = l & 3;
  const u32 rb0 = L0 + (rq >> 1) * 4096 + (rq & 1) * 1024 + rr * 128 + rp * 8 + (((rr >> 1) | ((rq & 1) << 1)) * 32);
#define abase(c, off) lds_base(rb0 , (c) * 32, (off))
  Acc acc[4];
  const char* stb = ws + OFF_ST2;
  u32x4 sg4; u32x2 sg2;
  {
    float s1[2] = {0.f, 0.f}, s2[2] = {0.f, 0.f};
    ldsp pb[3];
    Frag pf[2][3];
    auto fetch1 = [&](auto ic, int c, int hi) {
      constexpr int s = ic.value;
      if constexpr ((s & 15) == 0) { if (hi == 0) pb[c] = abase(c, s * 4096); }
      constexpr int o = ((s & 15) >> 1) * 8192 + (s & 1) * 2048;
      if (hi) pf[s & 1][c].hi = tr_half(pb[c] + (o + 512)); else pf[s & 1][c].lo = tr_half(pb[c] + o);
    };
    wait_vm2<3 * (G1_D - 1) + G1_DW - G1_D>(h[0][0], h[1][0]);
    BARRIER();
    swap_halves(h[0][0], h[1][0]);
#pragma unroll
    for (int c = 0; c < 3; ++c) { fetch1(IC<0>{}, c, 0); fetch1(IC<0>{}, c, 1); }
    static_for<0, 36>([&](auto ic) {
      constexpr int s = ic.value, g = s >> 1, mt = s & 1;
      constexpr int SQ = 2 * (18 - G1_D);
      auto mm = [&](int c) {
#pragma unroll
        for (int tl = 0; tl < 2; ++tl) {
          mma<g == 0>(acc[c].a[mt][tl], frag_of(pf[s & 1][c]), h[tl][g]);
          SCHED0();
          if constexpr (s < 35) fetch1(IC<s + 1>{}, c, tl);
          SCHED0();
        }
      };
      if constexpr (mt == 1 && s < 35) {
        wait_vm2<p1_wait(g)>(h[0][g + 1], h[1][g + 1]);
        if constexpr (p1_bar(g)) BARRIER();
      }
      SCHED0();
      touch(pf[s & 1][1]);
      mm(0);
      if constexpr (mt == 0 && g + G1_DW < 18) issue_dma(IC<g + G1_DW>{});
      if constexpr (mt == 1 && g + G1_D < 18) issue_h(IC<g + G1_D>{}, IC<1>{});
      if constexpr (s == 3) {
        const int ii = min(tid, 287), qd = ii / 72, r = ii - qd * 72;
        if (w < 5) dma16(ws + OFF_CS, (u32)(qd * 2304 + (r < 64 ? fg * 64 + r : 2048 + fg * 8 + (r - 64))) * 16u, L0 + G1_CP + w * 1024);
      }
      if constexpr (s == 16) {
        const u32 pi = min(opq(tid), 71u);
        const ldsp cp = LP + (G1_CP + pi * 16);
        const f32x4 r = (*(const LAS f32x4*)cp + *(const LAS f32x4*)(cp + 1152)) + (*(const LAS f32x4*)(cp + 2304) + *(const LAS f32x4*)(cp + 3456));
        *(LAS f32x4*)(LP + (G1_CS + pi * 16)) = r;
      }
      if constexpr (s == SQ) {
        bdma<G1_QE>(rW, mk_qvoff(l), 0, lw);
      }
      SCHED0();
      mm(1);
      if constexpr (g + G1_D < 18 && mt == 0) issue_h(IC<g + G1_D>{}, IC<0>{});
      if constexpr (s == SQ) { const u32 so = tt * 2048 + ((rq >> 1) * 16 + (l & 15)) * 64 + (rq & 1) * 24; ld_sc1_x4(sg4, so, stb); ld_sc1_x2<16>(sg2, so, stb); }
      if constexpr (mt == 1 && s < 35) swap_halves(h[0][g + 1], h[1][g + 1]);
      if constexpr (s >= 8 && s < 14) { if constexpr (s == 8) pin6(sx); stat_acc(fg, sx[s - 8], s1, s2); }
      SCHED0();
      mm(2);
      if constexpr (s == 14) {
        const float a0 = swap32_sum(swap16_sum(s1[0])), c0 = swap32_sum(swap16_sum(s2[0]));
        const float a1 = swap32_sum(swap16_sum(s1[1])), c1 = swap32_sum(swap16_sum(s2[1]));
        u32x2 gr = {__float_as_uint((l & 16) ? a1 : a0), __float_as_uint((l & 16) ? c1 : c0)};
        st_sc1_x2_if(fg, stb, (u32)(tt * 2048 + fg * 8 + (l & 31) * 64), gr);
      }
      SCHED0();
      if constexpr (s % 12 == 11) { ST(2 + s / 12); }
    });
  }
  STR(28);
  Frag wf[4];
  ldsp fb = nullptr, fb2 = nullptr;
  auto fetch = [&](auto ic, int hi) {
    constexpr int i = ic.value, p = i / 36, s = i % 36;
    if constexpr (p != 1) {
      constexpr int c = (p == 0) ? 3 : p - 2;
      constexpr int pos = (p == 0) ? (s + 28) % 36 : s;
      if constexpr (s == 0 || (pos & 15) == 0) { if (!hi) fb = abase(c, (pos & ~15) * 4096); }
      constexpr int o = ((pos & 15) >> 1) * 8192 + (pos & 1) * 2048;
      if (hi) wf[i & 3].hi = tr_half(fb + (o + 512)); else wf[i & 3].lo = tr_half(fb + o);
    } else {
      if constexpr (s == 0 || s == 8) {
        if (!hi) {
          const u32 l2 = opq(tid);
          const int qo = (l2 >> 4) & 1;
          const int qb = (8 * ((l2 >> 4) & 3) + ((l2 >> 2) & 3)) * 32 + (l2 & 3) * 8 + (s == 0 ? G1_QE : G1_QL);
          fb = opaque(LP + (qb + qo * 128)); fb2 = opaque(LP + (qb + (1 - qo) * 128));
        }
      }
      constexpr int o = (s < 8 ? s : s - 8) * 1024;
      if (hi) wf[i & 3].hi = tr_half(fb2 + o); else wf[i & 3].lo = tr_half(fb + o);
    }
  };
  static_for<0, 3>([&](auto ic) { fetch(ic, 0); fetch(ic, 1); });
  SCHED0();
  float nmu[2], k1[2], k2[2], kd[2];
  auto ln2_stats = [&]() {
    wait0_x6(sg4, sg2);
#pragma unroll 1
    for (int it = 0; it < (1 << 16); ++it) {
      const bool bad = (sg4[0] & sg4[1]) == 0xffffffffu || (sg4[2] & sg4[3]) == 0xffffffffu || (sg2[0] & sg2[1]) == 0xffffffffu;
      if (!__any(bad)) break;
      __builtin_amdgcn_s_sleep(2);
      const u32 so = tt * 2048 + ((rq >> 1) * 16 + (l & 15)) * 64 + (rq & 1) * 24;
      ld_sc1_x4(sg4, so, stb); ld_sc1_x2<16>(sg2, so, stb);
      wait0_x6(sg4, sg2);
    }
    {
      const float t1 = swap16_sum(__uint_as_float(sg4[0]) + __uint_as_float(sg4[2]) + __uint_as_float(sg2[0]));
      const float t2 = swap16_sum(__uint_as_float(sg4[1]) + __uint_as_float(sg4[3]) + __uint_as_float(sg2[1]));
      const float mu = hide(t1 * (1.f / D_));
      const float var = fmaxf(t2 * (1.f / D_) - mu * mu, 0.f) + 1e-5f;
      const auto m2 = __builtin_amdgcn_permlane32_swap(__float_as_uint(-mu), __float_as_uint(-mu), false, false);
      const auto v2 = __builtin_amdgcn_permlane32_swap(__float_as_uint(var), __float_as_uint(var), false, false);
#pragma unroll
      for (int tl = 0; tl < 2; ++tl) {
        kd[tl] = __uint_as_float(v2[tl]);
        const float r = __builtin_amdgcn_rsqf(kd[tl]);
        nmu[tl] = __uint_as_float(m2[tl]);
        k1[tl] = hide(GC * r);
        k2[tl] = hide(k1[tl] * 0.044715f * r * r);
      }
    }
  };
  char* hdst = ws + OFF_HH + ((size_t)tt * 288 + (fg >> 1) * 9 + (fg & 1) * 4) * 1024;
  const ldsp csb = opaque(LP + (G1_CS + rq * 32));
  f32x4 cc;
  auto ldcs = [&](int lc, int hfx) { cc = *(const LAS f32x4*)(csb + (lc * 128 + hfx * 16)); };
  u32x4 eo[2];
  u32x2 eq[2];
  float m0, m1, tu0, tu1, y0, y1;
  auto emit = [&](int lc, int i, u32 pk) {
    const int tl = i & 1, hx = i >> 1;
    if (lc == 8) {
      eq[tl][hx] = pk;
      if (hx) st_sc1_x2(hdst + (size_t)tl * 147456 + (8 - (fg & 1) * 4) * 1024 + (fg & 1) * 8, lo16, eq[tl]);
    } else {
      eo[tl][(lc & 1) * 2 + hx] = pk;
      if ((lc & 1) && hx) st_sc1_x4(hdst + (size_t)tl * 147456 + (lc >> 1) * 1024, lo16, eo[tl]);
    }
  };
  auto epi = [&](const Acc& a, int lc, int st, int nlc, int part) {
    const int i = st / 6, sg = st % 6, tl = i & 1, e = (i >> 1) * 2;
    if (sg == 0) {
      if (part == 0) { if (i > 0) { u32 pk = pack2(tu0, tu1 * y1); pin(pk); emit(lc, i - 1, pk); } }
      else if (i < 4) { m0 = fmaf(nmu[tl], cc[0], a.a[0][tl][e]); m1 = fmaf(nmu[tl], cc[1], a.a[0][tl][e + 1]); pinf(m0); pinf(m1); }
    } else if (sg == 1) {
      if (part == 0) { tu0 = fmaf(nmu[tl], cc[2], a.a[1][tl][e]); tu1 = fmaf(nmu[tl], cc[3], a.a[1][tl][e + 1]); pinf(tu0); pinf(tu1); }
      else {
        y0 = hide(m0 * m0); y1 = m1 * m1; pinf(y0); pinf(y1);
        if (i == 1) ldcs(lc, 1);
        if (i == 3 && nlc >= 0) ldcs(nlc, 0);
      }
    } else if (sg == 2) {
      if (part == 0) { tu0 = hide(tu0 * m0); tu1 = tu1 * m1; pinf(tu0); pinf(tu1); }
      else { y0 = hide(fmaf(y0, k2[tl], k1[tl])); y1 = fmaf(y1, k2[tl], k1[tl]); pinf(y0); pinf(y1); }
    } else if (sg == 3) {
      if (part == 0) { y0 = hide(y0 * m0); y1 = y1 * m1; pinf(y0); pinf(y1); }
      else { y0 = __builtin_amdgcn_exp2f(y0); pinf(y0); }
    } else if (sg == 4) {
      if (part == 0) { y1 = __builtin_amdgcn_exp2f(y1); pinf(y1); }
      else { y0 = fmaf(y0, kd[tl], kd[tl]); pinf(y0); }
    } else {
      if (part == 0) { y0 = __builtin_amdgcn_rcpf(y0); y1 = fmaf(y1, kd[tl], kd[tl]); pinf(y0); pinf(y1); }
      else { y1 = __builtin_amdgcn_rcpf(y1); tu0 = tu0 * y0; pinf(y1); pinf(tu0); }
    }
  };
  const int r5 = b >> 3;
  const int ks = b & 3, tg2 = ((b >> 2) & 1) * 8 + (r5 & 7), dt = r5 >> 3;
  const int tq = w & 3, kh = w >> 2;
  constexpr u32 RD = D_ * 2;
  const u32 d_dst = L0 + kh * 9216 + tq * 2048;
  const int mb = ks * 4 + kh * 2;
  const u32 rowA = mb * 256 * RD, rowQ = (4096 + mb * 32) * RD;
  auto row0 = [&](int kk) { const int q = kk / 9, i = kk - 9 * q; return i < 8 ? rowA + (q * 256 + i * 32) * RD : rowQ + q * 32 * RD; };
  u32x4 hb[HB][2];
  u32 hv0 = 0, hv1 = 0;
  const char* myflag = ws + OFF_FLG + 16384 + b * 64;
  const char* pflags = ws + OFF_FLG + 16384 + ((b & 7) + kh * 32 + ((r5 & 7) >> 1) * 64) * 64;
  u32 d_voff = 0, dq_voff = 0, p_voff = 0, pv = 0, q_voff = 0;
  f32x4 ac2[9][2];
  ldsp pb[5];
  u32 rb2 = 0, rbq = 0;
  auto fetch2 = [&](auto ic, int hi) {
    constexpr int i = ic.value, s = i / 9, c = i % 9, stg = s % NST;
    if constexpr (stg % 3 == 0 && c < 4) { if (!hi) pb[c] = lds_base(rb2, c * 32, stg * STG); }
    if constexpr (c == 8 && (stg % 3 == 0 || s == 4)) { if (!hi) pb[4] = opaque((ldsp)(uintptr_t)(opq(rbq) + (s < 4 ? (u32)(G1_QE - 8192) - kh * (9216 - 1024) : (u32)((stg - stg % 3) * STG)))); }
    constexpr int o = c < 8 ? (stg % 3) * STG + (c >> 2) * 1024 : (s < 4 ? s * 2048 : (stg % 3) * STG);
    if (hi) wf[i & 3].hi = tr_half(pb[c < 8 ? (c & 3) : 4] + (o + (c < 8 ? 4096 : 512))); else wf[i & 3].lo = tr_half(pb[c < 8 ? (c & 3) : 4] + o);
  };
  auto issue2 = [&](auto uc) {
    constexpr int u = uc.value;
    constexpr int nw0 = find_at(18, u, [](int n) { return w_issue(n, 0); }), nw1 = find_at(18, u, [](int n) { return w_issue(n, 1); });
    constexpr int nh0 = find_at(18, u, [](int n) { return h_issue(n, 0); }), nh1 = find_at(18, u, [](int n) { return h_issue(n, 1); });
    constexpr int r = find_at(5, u, [](int n) { return dq_issue(n); });
    constexpr int pi = find_at(3, u, [](int n) { return poll_iss(n); }), pc = find_at(3, u, [](int n) { return poll_chk(n); });
    if constexpr (nw0 >= 0) bdma_a<(nw0 % NST) * STG, rowc(kk_of(nw0))>(rD, d_voff, kk_of(nw0) % 9 < 8 ? rowA : rowQ, d_dst);
    if constexpr (nw1 >= 0) bdma_a<(nw1 % NST) * STG + 1024, rowc(kk_of(nw1)) + 128>(rD, d_voff, kk_of(nw1) % 9 < 8 ? rowA : rowQ, d_dst);
    if constexpr (nh0 >= 0) HLOAD<(kk_of(nh0) & 3) * 1024>(hb[nh0 % HB][0], rS, hv0, (kk_of(nh0) >> 2) * 4096);
    if constexpr (nh1 >= 0) HLOAD<(kk_of(nh1) & 3) * 1024>(hb[nh1 % HB][1], rS, hv1, (kk_of(nh1) >> 2) * 4096);
    if constexpr (r >= 0) {
      const int tsel = r == 4 ? (tq & 1) : tq;
      const u32 ra = row0(kk_of(4 * r)), rb = row0(kk_of(4 * r + 1)), rc_ = row0(kk_of(r == 4 ? 16 : 4 * r + 2)), rd = row0(kk_of(r == 4 ? 17 : 4 * r + 3));
      const u32 rsel = (tsel & 2) ? ((tsel & 1) ? rd : rc_) : ((tsel & 1) ? rb : ra);
      bdma<0>(rD, dq_voff, rsel, r == 0 ? L0 + G1_QE + (tq * 2 + kh) * 1024 : L0 + (((4 * r) % NST) + tsel) * STG + kh * 9216 + 8192);
    }
    if constexpr (pi >= 0) poll_issue(pv, p_voff, pflags);
    if constexpr (pc >= 0) poll_check<wait_k(4 * poll_iss(pc) + 2, 4 * u + 2), pc + 1>(pv, p_voff, pflags);
  };
  static_for<0, 216>([&](auto ic) {
    constexpr int t = ic.value, p = t / 36, s = t % 36;
    constexpr int slot = (p + 3) & 3;
    constexpr int hg = (p == 0) ? ((s >> 1) + 14) % 18 : (s >> 1);
    constexpr int ci = (t - G1_E0) / 26, st = (t - G1_E0) % 26;
    constexpr int lc = ci < 4 ? ci : (ci == 4 ? 8 : ci - 1), nlc = ci < 3 ? ci + 1 : (ci == 3 ? 8 : ci);
#pragma unroll
    for (int tl = 0; tl < 2; ++tl) {
      if constexpr ((t & 1) == 0) { if (tl == 0) touch(wf[(t + 1) & 3]); }
      mma<(s < 2)>(acc[slot].a[s & 1][tl], frag_of(wf[t & 3]), h[tl][hg]);
      SCHED0();
      if constexpr (t + 3 < 216) fetch(IC<t + 3>{}, tl);
      SCHED0();
      if constexpr (t >= G1_E0 && ci < 8 && st <= 24) epi(acc[ci & 3], lc, st, nlc, tl);
      SCHED0();
    }
    if constexpr (t == 2) ln2_stats();
    if constexpr (t == G1_E0 - 1) ldcs(0, 0);
    if constexpr (t == 7 || t == 28 || t == 72 || t == 190) { SCHED0(); BARRIER(); }
    if constexpr (t == 40) { wait_vm<wait_q(1, 40)>(); BARRIER(); }
    if constexpr (t == 56) { wait_vm<wait_q(3, 56)>(); BARRIER(); }
    if constexpr (t == 68) { wait_vm<wait_b(9, 68)>(); BARRIER(); }
    if constexpr (t == 88) { wait_vm<wait_b(14, 88)>(); BARRIER(); }
    if constexpr (t == 98) { wait_vm<wait_b(17, 98)>(); BARRIER(); }
    if constexpr (t == 160) { wait_vm<wait_k(4 * (epi_start(4) + 24), 4 * t + 1)>(); BARRIER(); flag_store(w, opq(0u), opq(1u), myflag); }
    if constexpr (t == 198) { SCHED0(); BARRIER(); }
    if constexpr (t == 208) { SCHED0(); wait_vm<wait_k(4 * w_issue(1, 1) + 2, 4 * t + 1)>(); BARRIER(); flag_store(w, opq(0u), opq(2u), myflag); }
    if constexpr (t == 7) q_voff = mk_qvoff(opq(tid) & 63);
    if constexpr (t == 179) {
      const u32 l2 = opq(tid) & 63;
      d_voff = (u32)((8 * tq + (l2 >> 3)) * RD + ((((l2 & 7) >> 1) ^ ((l2 >> 4) & 3)) * 32) + (l2 & 1) * 16 + dt * 256);
      dq_voff = (u32)((l2 >> 1) * RD + (l2 & 1) * 16 + 1024 + dt * 32);
      p_voff = (l2 & 3) * 512;
      hv0 = l2 * 16 + (u32)OFF_HH + ((tg2 * 8 + tq * 2 + kh) * 144 + mb * 9) * 1024;
      hv1 = l2 * 16 + (u32)OFF_HH + ((tg2 * 8 + tq * 2 + (kh ^ 1)) * 144 + mb * 9) * 1024;
      pin(d_voff); pin(dq_voff); pin(p_voff); pin(hv0); pin(hv1);
    }
    {
      constexpr int r = find_at(4, t, [](int n) { return g1_q_issue(0, n); }), j = find_at(18, t, [](int n) { return g1_b_issue(0, n); });
      if constexpr (r >= 0) bdma_i<G1_QL + r * 8192, (4 + 4 * r) * GS>(rW, q_voff, lw);
      if constexpr (j >= 0) bdma_i<j * 8192, j * GS + 128>(rW, ab_voff, ab_dst);
    }
    if constexpr (t >= PF_ISS0 && t < PF_ISS0 + 20 && (t - PF_ISS0) % 4 == 0) {
      const int piece = ((b >> 3) * 5 + (t - PF_ISS0) / 4) * 8 + w;
      const int byte = piece * 1024;
      bdma<G1_QE>(rD, lo16, (u32)(byte < 1024 * 1152 ? (b & 3) * 1024 * 1152 + byte : 4096 * 1152 + (b & 3) * 128 * 1152 + (byte - 1024 * 1152)), lw);
    }
    if constexpr (t == 3 || t == 102) code_touch(code0, min(opq((u32)tid) * 64u + (t == 3 ? 8192u : 18432u), codelen - 4u), L0 + FF_LDS - 256);
    if constexpr (t >= 180) issue2(ic);
    SCHED0();
    if constexpr (t % 24 == 23) { ST(5 + t / 24); }
  });
  STR(29);
  {
    const u32 l2 = opq(tid) & 63;
    const u32 q2 = l2 >> 4, r2_ = (l2 >> 2) & 3, p2 = l2 & 3;
    rb2 = L0 + kh * 9216 + (q2 >> 1) * 2048 + (4 * (q2 & 1) + r2_) * 128 + ((2 * (q2 & 1) + (r2_ >> 1)) * 32) + p2 * 8;
    rbq = L0 + kh * 9216 + 8192 + (4 * q2 + r2_) * 32 + p2 * 8;
  }
  const int ft = tg2 * 8 + ks * 2 + kh;
  const int dcol = dt * 128 + tq * 32 + 4 * rq;
  const int qcol = 512 + dt * 16 + 4 * rq;
  u32x2 r0, r1, r2;
  static_for<0, 3>([&](auto ic) { fetch2(ic, 0); fetch2(ic, 1); });
  wait_vm2<wait_k(4 * h_issue(0, 1) + 2, 4 * U2 - 1)>(hb[0][0], hb[0][1]);
  static_for<0, 162>([&](auto ic) {
    constexpr int i = ic.value, s = i / 9, c = i % 9, u = U2 + i;
    if constexpr (c == ((s & 1) ? 6 : 4) && s < 17) {
      constexpr int s2 = cmin(s + 2, 17);
      constexpr int tgt = (s & 1) ? 4 * h_issue(s + 1, 1) + 2
                                  : cmax(cmax(cmax(4 * h_issue(s + 1, 1) + 2, 4 * w_issue(s2, 1) + 2), 4 * dq_issue((s + 1) >> 2) + 2), s == 4 ? 4 * (U2 + 14) : 0);
      wait_vm2<wait_k(tgt, 4 * u - 1)>(hb[(s + 1) % HB][0], hb[(s + 1) % HB][1]);
      if constexpr ((s & 1) == 0 || G2_BAR_ALL) BARRIER();
      if constexpr (s == 4) flag_store(w, opq(0u), opq(3u), myflag);
    }
    SCHED0();
#pragma unroll
    for (int tl = 0; tl < 2; ++tl) {
      if constexpr ((i & 1) == 0 && i + 1 < 162) { if (tl == 0) touch(wf[(i + 1) & 3]); }
      mma<s == 0>(ac2[c][tl], frag_of(wf[i & 3]), hb[s % HB][tl]);
      SCHED0();
      if constexpr (i + 3 < 162) fetch2(IC<i + 3>{}, tl);
      SCHED0();
      if constexpr (i >= 2 && i <= 14) { if (i < 14 || tl == 0) { epi(acc[0], 7, 2 * (i - 2) + tl, -1, 0); epi(acc[0], 7, 2 * (i - 2) + tl, -1, 1); } }
      SCHED0();
    }
    issue2(IC<u>{});
    if constexpr (u == RES_ISS) {
      const char* hb_ = ws + OFF_HF + (size_t)(ft >> 1) * 36864 + (ft & 1) * 8;
      auto po = [&](int d) { return (u32)((d >> 5) * 2048 + ((d >> 2) & 1) * 1024 + ((d >> 3) & 3) * 256 + (l & 15) * 16); };
      gload8(r0, po(dcol), hb_); gload8(r1, po(dcol + 16), hb_); gload8(r2, po(qcol), hb_);
    }
    SCHED0();
    if constexpr (c == 8 && s % 3 == 2) { ST(14 + s / 3); }
  });
  MFMA_DRAIN(); SCHED0();
  {
    const ldsp xw = LP + (2 * STG + w * 9216 + l * 16);
#pragma unroll
    for (int c = 0; c < 9; ++c) *(LAS f32x4*)(xw + c * 1024) = ac2[c][1];
  }
  LDS_BARRIER();
  f32x4 sm[9];
  {
    const ldsp xr = LP + (2 * STG + (w ^ 4) * 9216 + l * 16);
#pragma unroll
    for (int c = 0; c < 9; ++c) sm[c] = ac2[c][0] + *(const LAS f32x4*)(xr + c * 1024);
  }
  ST(20);
  char* part = ws + OFF_PART;
  if (tq != ks) {
    char* pdst = part + ((((size_t)(tg2 * 4 + dt) * 4 + tq) * 4 + ks) * 2 + kh) * 4608;
#pragma unroll
    for (int pc = 0; pc < 4; ++pc) {
      u32x4 v = {pack2(sm[2 * pc][0], sm[2 * pc][1]), pack2(sm[2 * pc][2], sm[2 * pc][3]), pack2(sm[2 * pc + 1][0], sm[2 * pc + 1][1]), pack2(sm[2 * pc + 1][2], sm[2 * pc + 1][3])};
      st_sc1_x4(pdst + pc * 1024, lo16, v);
    }
    u32x2 v2 = {pack2(sm[8][0], sm[8][1]), pack2(sm[8][2], sm[8][3])};
    st_sc1_x2(pdst + 4096, l * 8, v2);
  } else {
    const ldsp ow = LP + (kh * 9216 + l * 16);
#pragma unroll
    for (int c = 0; c < 9; ++c) *(LAS f32x4*)(ow + c * 1024) = sm[c];
  }
  asm volatile("s_waitcnt vmcnt(0)" : "+v"(r0), "+v"(r1), "+v"(r2)::"memory");
  LDS_BARRIER();
  ST(21);
  const char* fl = ws + OFF_FLG + (tg2 * 4 + dt) * 256;
  flag_store(w, opq(0u), opq(1u), fl + ks * 64);
  const ldsp orp = LP + (kh * 9216 + l * 16);
  f32x4 o0 = *(const LAS f32x4*)(orp + tq * 2048), o1 = *(const LAS f32x4*)(orp + (tq * 2048 + 1024)), o2 = *(const LAS f32x4*)(orp + 8192);
  o0[0] += bflo(r0[0]); o0[1] += bfhi(r0[0]); o0[2] += bflo(r0[1]); o0[3] += bfhi(r0[1]);
  o1[0] += bflo(r1[0]); o1[1] += bfhi(r1[0]); o1[2] += bflo(r1[1]); o1[3] += bfhi(r1[1]);
  o2[0] += bflo(r2[0]); o2[1] += bfhi(r2[0]); o2[2] += bflo(r2[1]); o2[3] += bfhi(r2[1]);
  {
    const u32 fo = (l & 3) * 64;
    poll_issue(pv, fo, fl);
    poll_check<0, 1>(pv, fo, fl);
  }
  ST(22);
  {
    const char* psrc = part + (((size_t)(tg2 * 4 + dt) * 4 + ks) * 4 * 2 + kh) * 4608;
    u32x4 v[3]; u32x2 vq[3];
#pragma unroll
    for (int j = 0; j < 3; ++j) {
      const int src = (ks + 1 + j) & 3;
      ld_sc1_x4(v[j], lo16 + tq * 1024, psrc + src * 9216);
      ld_sc1_x2<0>(vq[j], l * 8 + 4096, psrc + src * 9216);
    }
    asm volatile("s_waitcnt vmcnt(0)" : "+v"(v[0]), "+v"(v[1]), "+v"(v[2]), "+v"(vq[0]), "+v"(vq[1]), "+v"(vq[2])::"memory");
#pragma unroll
    for (int j = 0; j < 3; ++j) {
      o0[0] += bflo(v[j][0]); o0[1] += bfhi(v[j][0]); o0[2] += bflo(v[j][1]); o0[3] += bfhi(v[j][1]);
      o1[0] += bflo(v[j][2]); o1[1] += bfhi(v[j][2]); o1[2] += bflo(v[j][3]); o1[3] += bfhi(v[j][3]);
      o2[0] += bflo(vq[j][0]); o2[1] += bfhi(vq[j][0]); o2[2] += bflo(vq[j][1]); o2[3] += bfhi(vq[j][1]);
    }
    u16* op = out + (size_t)(ft * 16 + (l & 15)) * D_;
    u32x2 oa = {pack2(o0[0], o0[1]), pack2(o0[2], o0[3])}, ob = {pack2(o1[0], o1[1]), pack2(o1[2], o1[3])}, oc = {pack2(o2[0], o2[1]), pack2(o2[2], o2[3])};
    *(u32x2*)(op + dcol) = oa; *(u32x2*)(op + dcol + 16) = ob;
    if (tq == 0) *(u32x2*)(op + qcol) = oc;
  }
  asm volatile(".Lffn_code_end:");
  ST(23); ST_DRAIN(); ST(24);
  ST_FLUSH((u32*)(ws + (200u << 20)), b * 8 + w);
}

extern "C" void kernel_launch(void* const* d_in, const int* in_sizes, int n_in, void* d_out, int out_size, void* d_ws, size_t ws_size,
                              hipStream_t stream) {
  const u16* q = (const u16*)d_in[0]; const u16* k = (const u16*)d_in[1]; const u16* v = (const u16*)d_in[2];
  const u16* shk = (const u16*)d_in[3]; const u16* ssm = (const u16*)d_in[4]; const u16* ssmD = (const u16*)d_in[5];
  const u16* wg = (const u16*)d_in[7]; const u16* wu = (const u16*)d_in[8]; const u16* wd = (const u16*)d_in[9];
  char* ws = (char*)d_ws;
  (void)hipFuncSetAttribute((const void*)k1_conv, hipFuncAttributeMaxDynamicSharedMemorySize, K1_LDS);
  (void)hipFuncSetAttribute((const void*)ffn_fused, hipFuncAttributeMaxDynamicSharedMemorySize, FF_LDS);
  k0_prep<<<K0_GRID, 256, 0, stream>>>(q, k, v, shk, ssm, ws, wg, wu, ssmD);
  k1_conv<<<K1_GRID, 512, K1_LDS, stream>>>(ssm, ssmD, wg, wu, ws, wd, (const u16*)d_out);
  ffn_fused<<<256, 512, FF_LDS, stream>>>(wg, wu, wd, ws, (u16*)d_out);
}
```

```cpp
#include <hip/hip_runtime.h>
#include <hip/hip_bf16.h>
#include <stdint.h>
#include <utility>

typedef unsigned short u16;
typedef unsigned int u32;
typedef __attribute__((ext_vector_type(2))) unsigned u32x2;
typedef __attribute__((ext_vector_type(4))) unsigned u32x4;
typedef __attribute__((ext_vector_type(2))) float f32x2;
typedef __attribute__((ext_vector_type(4))) float f32x4;
typedef __attribute__((ext_vector_type(2))) __bf16 bf16x2;
typedef __attribute__((ext_vector_type(4))) short s16x4;
#define LAS __attribute__((address_space(3)))
#define DI __device__ __forceinline__
typedef LAS char* ldsp;

constexpr int S_ = 2048, D_ = 576, DF_ = 4608;
constexpr size_t OFF_KVT = 0;
constexpr size_t OFF_XP  = OFF_KVT + (size_t)D_ * S_ * 2;
constexpr size_t OFF_HF  = OFF_XP + (size_t)D_ * S_ * 2;
constexpr size_t OFF_ST1 = OFF_HF + (size_t)D_ * S_ * 2;
constexpr size_t OFF_CS  = OFF_ST1 + 2048 * 8;
constexpr size_t OFF_FLG = OFF_CS + 4 * 2 * 4608 * 4;
constexpr size_t OFF_ST2 = OFF_FLG + 36864;
constexpr size_t OFF_HH  = OFF_ST2 + 131072;
constexpr size_t OFF_PART = OFF_HH + (size_t)S_ * DF_ * 2;
constexpr size_t WS_USED = OFF_PART + 64 * 4 * 4 * 2 * 4608;
static_assert(WS_USED - OFF_HF <= (64u << 20), "page touch covers 64 MiB");

DI float bflo(u32 w) { return __uint_as_float(w << 16); }
DI float bfhi(u32 w) { return __uint_as_float(w & 0xffff0000u); }
DI u32 pack2(float a, float b) { bf16x2 p; p[0] = (__bf16)a; p[1] = (__bf16)b; return __builtin_bit_cast(u32, p); }

template <int I> struct IC { static constexpr int value = I; };
template <int B, class F, int... I> DI void static_for_impl(F&& f, std::integer_sequence<int, I...>) { (f(IC<B + I>{}), ...); }
template <int B, int E, class F> DI void static_for(F&& f) { static_for_impl<B>(f, std::make_integer_sequence<int, E - B>{}); }
template <int N> DI void wait_vm() { asm volatile("s_waitcnt vmcnt(%0)" ::"n"(N) : "memory"); }
#define BARRIER() do { asm volatile("" ::: "memory"); __builtin_amdgcn_s_barrier(); asm volatile("" ::: "memory"); } while (0)
#define LDS_BARRIER() do { asm volatile("s_waitcnt lgkmcnt(0)" ::: "memory"); __builtin_amdgcn_s_barrier(); asm volatile("" ::: "memory"); } while (0)
#define SCHED0() __builtin_amdgcn_sched_barrier(0)
constexpr int cmin(int a, int b) { return a < b ? a : b; }

struct Frag { s16x4 lo, hi; };
DI s16x4 tr_half(ldsp a) { return __builtin_amdgcn_ds_read_tr16_b64_v4i16((LAS s16x4*)a); }
DI u32x4 frag_of(const Frag& f) { return __builtin_bit_cast(u32x4, __builtin_shufflevector(f.lo, f.hi, 0, 1, 2, 3, 4, 5, 6, 7)); }
template <bool FIRST> DI void mma(f32x4& c, const u32x4& a, const u32x4& b) {
  if constexpr (FIRST) asm volatile("v_mfma_f32_16x16x32_bf16 %0, %1, %2, 0" : "=&v"(c) : "v"(a), "v"(b));
  else asm volatile("v_mfma_f32_16x16x32_bf16 %0, %1, %2, %0" : "+v"(c) : "v"(a), "v"(b));
}
#define MFMA_DRAIN() asm volatile("s_nop 15" ::: "memory")
DI void poll_issue(u32& f, u32 voff, const char* sb) { asm volatile("global_load_dword %0, %1, %2 sc1" : "=v"(f) : "v"(voff), "s"(sb) : "memory"); }
template <int N, int PH> DI void poll_check(u32& f, u32 voff, const char* sb) {
  u32 cnt = 1u << 18;
  asm volatile("s_waitcnt vmcnt(%4)\n"
               "1:\n\tv_cmp_gt_u32_e32 vcc, %5, %0\n\ts_nop 4\n\ts_cbranch_vccz 2f\n\t"
               "s_sleep 1\n\tglobal_load_dword %0, %2, %3 sc1\n\ts_waitcnt vmcnt(0)\n\t"
               "s_sub_u32 %1, %1, 1\n\ts_cbranch_scc0 1b\n"
               "2:" : "+v"(f), "+s"(cnt) : "v"(voff), "s"(sb), "n"(N), "n"(PH) : "memory", "vcc", "scc");
}
DI void swap_halves(u32x4& x, u32x4& y) {
  u32 a = x[2], b = x[3], c = y[0], d = y[1];
  asm volatile("v_swap_b32 %0, %2\n\tv_swap_b32 %1, %3" : "+v"(a), "+v"(b), "+v"(c), "+v"(d));
  x[2] = a; x[3] = b; y[0] = c; y[1] = d;
}
DI void flag_store(int w, u32 zero_off, u32 val, const char* sb) {
  asm volatile("s_cmp_lg_u32 %3, 0\n\ts_cbranch_scc1 1f\n\tglobal_store_dword %0, %1, %2 sc1\n1:" ::"v"(zero_off), "v"(val), "s"(sb), "s"(w) : "memory", "scc");
}
template <int OFF> DI void gload16(u32x4& dst, u32 voff, const char* sbase) {
  asm volatile("global_load_dwordx4 %0, %1, %2 offset:%3" : "=v"(dst) : "v"(voff), "s"(sbase), "n"(OFF) : "memory");
}
DI void gload8(u32x2& dst, u32 voff, const char* sbase) { asm volatile("global_load_dwordx2 %0, %1, %2" : "=v"(dst) : "v"(voff), "s"(sbase) : "memory"); }
template <int N> DI void wait_vm2(u32x4& a, u32x4& b) { asm volatile("s_waitcnt vmcnt(%2)" : "+v"(a), "+v"(b) : "n"(N) : "memory"); }
DI void dma16(const char* sbase, u32 voff, u32 lds_dst) {
  asm volatile("s_mov_b32 m0, %2\n\ts_nop 0\n\tglobal_load_lds_dwordx4 %0, %1" ::"v"(voff), "s"(sbase), "s"(lds_dst) : "memory");
}
typedef __amdgpu_buffer_rsrc_t rsrc_t;
DI rsrc_t mk_rsrc(const void* p, u32 bytes) { return __builtin_amdgcn_make_buffer_rsrc((void*)p, (short)0, (int)bytes, 0x00020000); }
template <int LOFF> DI void bdma(rsrc_t r, u32 voff, u32 soff, u32 ldsb) {
  asm volatile("s_add_u32 m0, %3, %4\n\ts_nop 0\n\tbuffer_load_dwordx4 %0, %1, %2 offen lds" ::"v"(voff), "s"(r), "s"(soff), "s"(ldsb), "n"(LOFF) : "memory", "scc");
}
template <int LOFF, u32 SOFF> DI void bdma_i(rsrc_t r, u32 voff, u32 ldsb) {
  u32 so;
  asm volatile("s_add_u32 m0, %3, %4\n\ts_mov_b32 %0, %5\n\tbuffer_load_dwordx4 %1, %2, %0 offen lds" : "=&s"(so) : "v"(voff), "s"(r), "s"(ldsb), "n"(LOFF), "n"(SOFF) : "memory", "scc");
}
template <int LOFF, u32 SOFF> DI void bdma_a(rsrc_t r, u32 voff, u32 sbase, u32 ldsb) {
  u32 so;
  asm volatile("s_add_u32 m0, %3, %4\n\ts_add_u32 %0, %6, %5\n\tbuffer_load_dwordx4 %1, %2, %0 offen lds" : "=&s"(so) : "v"(voff), "s"(r), "s"(ldsb), "n"(LOFF), "n"(SOFF), "s"(sbase) : "memory", "scc");
}
template <int X> DI ldsp lds_base_t(u32 rb, u32 off) {
  u32 r;
  if constexpr (X == 96) { asm volatile("v_xor_b32_e32 %0, 0x60, %1\n\tv_add_u32_e32 %0, %2, %0" : "=&v"(r) : "v"(rb), "s"(off)); }
  else asm volatile("v_xad_u32 %0, %1, %2, %3" : "=v"(r) : "v"(rb), "n"(X), "s"(off));
  return (ldsp)(uintptr_t)r;
}
DI ldsp lds_base(u32 rb, int x, u32 off) { return x == 0 ? lds_base_t<0>(rb, off) : x == 32 ? lds_base_t<32>(rb, off) : x == 64 ? lds_base_t<64>(rb, off) : lds_base_t<96>(rb, off); }
DI void touch(const Frag& f) { asm volatile("" ::"v"(f.lo), "v"(f.hi)); }
template <int OFF> DI void bload(u32x4& d, rsrc_t r, u32 voff, u32 soff) {
  asm volatile("buffer_load_dwordx4 %0, %1, %2, %3 offen offset:%4" : "=v"(d) : "v"(voff), "s"(r), "s"(soff), "n"(OFF) : "memory");
}
template <int OFF> DI void bload_sc1(u32x4& d, rsrc_t r, u32 voff, u32 soff) {
  asm volatile("buffer_load_dwordx4 %0, %1, %2, %3 offen offset:%4 sc1" : "=v"(d) : "v"(voff), "s"(r), "s"(soff), "n"(OFF) : "memory");
}
DI void stat_loads(int fg, u32x4 (&x)[6], rsrc_t r, u32 voff, u32 soff, u32 soff2) {
  asm volatile("s_cmp_gt_u32 %10, 5\n\ts_cbranch_scc1 1f\n\t"
               "buffer_load_dwordx4 %0, %6, %7, %8 offen\n\tbuffer_load_dwordx4 %1, %6, %7, %8 offen offset:1024\n\t"
               "buffer_load_dwordx4 %2, %6, %7, %8 offen offset:2048\n\tbuffer_load_dwordx4 %3, %6, %7, %8 offen offset:3072\n\t"
               "buffer_load_dwordx4 %4, %6, %7, %9 offen\n\tbuffer_load_dwordx4 %5, %6, %7, %9 offen offset:1024\n1:"
               : "=&v"(x[0]), "=&v"(x[1]), "=&v"(x[2]), "=&v"(x[3]), "=&v"(x[4]), "=&v"(x[5])
               : "v"(voff), "s"(r), "s"(soff), "s"(soff2), "s"(fg) : "memory", "scc");
}
DI void code_touch(const char* base, u32 voff, u32 lds_dst) {
  asm volatile("s_mov_b32 m0, %2\n\ts_nop 0\n\tglobal_load_lds_dword %0, %1" ::"v"(voff), "s"(base), "s"(lds_dst) : "memory");
}
#define CODE_RANGE(base, len, endlabel) \
  asm volatile("s_getpc_b64 s[40:41]\n\ts_mov_b64 %0, s[40:41]\n\ts_mov_b32 %1, " endlabel "@rel32@lo-4" : "=s"(base), "=s"(len)::"s40", "s41")
DI ldsp opaque(ldsp p) { asm volatile("" : "+v"(p)); return p; }
DI u32 opq(u32 p) { asm volatile("" : "+v"(p)); return p; }
DI void pin(u32& p) { asm volatile("" : "+v"(p)); }
DI void pin6(u32x4 (&x)[6]) { asm volatile("" : "+v"(x[0]), "+v"(x[1]), "+v"(x[2]), "+v"(x[3]), "+v"(x[4]), "+v"(x[5])); }
DI void pinf(float& p) { asm volatile("" : "+v"(p)); }
DI float hide(float p) { asm("" : "+v"(p)); return p; }
DI float swap16_sum(float x) { const auto r = __builtin_amdgcn_permlane16_swap(__float_as_uint(x), __float_as_uint(x), false, false); return __uint_as_float(r[0]) + __uint_as_float(r[1]); }
DI float swap32_sum(float x) { const auto r = __builtin_amdgcn_permlane32_swap(__float_as_uint(x), __float_as_uint(x), false, false); return __uint_as_float(r[0]) + __uint_as_float(r[1]); }
#define STAT1(R, A, B) "v_lshlrev_b32_e32 %0, 16, " R "\n\tv_and_b32_e32 %1, %10, " R "\n\tv_add_f32_e32 " A ", " A ", %0\n\tv_fmac_f32_e32 " B ", %0, %0\n\tv_add_f32_e32 " A ", " A ", %1\n\tv_fmac_f32_e32 " B ", %1, %1\n\t"
DI void stat_acc(int fg, const u32x4& v, float (&s1)[2], float (&s2)[2]) {
  float x, y;
  asm volatile("s_cmp_gt_u32 %11, 5\n\ts_cbranch_scc1 1f\n\t" STAT1("%6", "%2", "%3") STAT1("%8", "%4", "%5") STAT1("%7", "%2", "%3") STAT1("%9", "%4", "%5") "\n1:"
               : "=&v"(x), "=&v"(y), "+v"(s1[0]), "+v"(s2[0]), "+v"(s1[1]), "+v"(s2[1]) : "v"(v[0]), "v"(v[1]), "v"(v[2]), "v"(v[3]), "s"(0xffff0000u), "s"(fg) : "scc");
}
DI void ld_sc1_x4(u32x4& d, u32 voff, const char* sb) { asm volatile("global_load_dwordx4 %0, %1, %2 sc1" : "=v"(d) : "v"(voff), "s"(sb) : "memory"); }
template <int OFF> DI void ld_sc1_x2(u32x2& d, u32 voff, const char* sb) { asm volatile("global_load_dwordx2 %0, %1, %2 offset:%3 sc1" : "=v"(d) : "v"(voff), "s"(sb), "n"(OFF) : "memory"); }
DI void wait0_x6(u32x4& a, u32x2& b) { asm volatile("s_waitcnt vmcnt(0)" : "+v"(a), "+v"(b)::"memory"); }
DI void st_sc1_x4(char* sb, u32 voff, const u32x4& v) { asm volatile("global_store_dwordx4 %0, %1, %2 sc1\n\ts_nop 1" ::"v"(voff), "v"(v), "s"(sb) : "memory"); }
DI void gstore16(char* sb, u32 voff, const u32x4& v) { asm volatile("global_store_dwordx4 %0, %1, %2\n\ts_nop 1" ::"v"(voff), "v"(v), "s"(sb) : "memory"); }
DI void st_sc1_x2_if(int skip, const char* sb, u32 voff, const u32x2& v) {
  asm volatile("s_cmp_gt_u32 %3, 5\n\ts_cbranch_scc1 1f\n\tglobal_store_dwordx2 %0, %1, %2 sc1\n1:" ::"v"(voff), "v"(v), "s"(sb), "s"(skip) : "memory", "scc");
}
DI void st_sc1_x2(char* sb, u32 voff, const u32x2& v) { asm volatile("global_store_dwordx2 %0, %1, %2 sc1" ::"v"(voff), "v"(v), "s"(sb) : "memory"); }


DI void colsum_slice(const u16* __restrict__ wg, const u16* __restrict__ wu, float* __restrict__ cs, int sl, int l) {
  const int mat = sl & 1, qr = (sl >> 1) & 3, cb = sl >> 3;
  const u16* p = (mat ? wu : wg) + (size_t)(qr * 144 + (l >> 3)) * DF_ + cb * 64 + (l & 7) * 8;
  u32x4 x[18];
#pragma unroll
  for (int i = 0; i < 18; ++i) x[i] = *(const u32x4*)(p + (size_t)i * 8 * DF_);
  float a[8];
#pragma unroll
  for (int e = 0; e < 8; ++e) a[e] = 0.f;
#pragma unroll
  for (int i = 0; i < 18; ++i)
#pragma unroll
    for (int e = 0; e < 4; ++e) { a[2 * e] += bflo(x[i][e]); a[2 * e + 1] += bfhi(x[i][e]); }
#pragma unroll
  for (int e = 0; e < 8; ++e) { a[e] += __shfl_xor(a[e], 8); a[e] = swap32_sum(swap16_sum(a[e])); }
  if (l < 8) {
    float* d = cs + qr * 9216 + (cb * 32 + l * 4) * 4 + mat * 2;
#pragma unroll
    for (int e = 0; e < 4; ++e) { f32x2 o = {a[2 * e], a[2 * e + 1]}; *(f32x2*)(d + e * 4) = o; }
  }
}

constexpr int K0_GRID = 64 + 288;
template <bool NT> DI void dma16p(const char* sbase, u32 voff, u32 lds_dst) {
  if constexpr (NT) asm volatile("s_mov_b32 m0, %2\n\ts_nop 0\n\tglobal_load_lds_dwordx4 %0, %1 nt" ::"v"(voff), "s"(sbase), "s"(lds_dst) : "memory");
  else dma16(sbase, voff, lds_dst);
}
extern "C" __global__ __launch_bounds__(256) void k0_prep(const u16* __restrict__ q, const u16* __restrict__ k, const u16* __restrict__ v,
                                                          const u16* __restrict__ shk, const u16* __restrict__ ssm, char* __restrict__ ws) {
  __shared__ __attribute__((aligned(128))) char lds[(72 + 64 + 64 + 64) * 128];
  asm volatile("" ::"s"(q), "s"(k), "s"(v), "s"(shk), "s"(ssm), "s"(ws));
  const int tid = threadIdx.x, l = tid & 63;
  const int w = __builtin_amdgcn_readfirstlane(tid >> 6);
  if (blockIdx.x >= 64) {
    const int b = blockIdx.x - 64;
    const int r8 = b >> 3, tb = r8 < 32 ? r8 : (b & 7) * 4 + (r8 - 32), cb = r8 < 32 ? (b & 7) : 8;
    const ldsp LP = (ldsp)lds;
    const u32 L0 = (u32)(uintptr_t)LP;
    constexpr int KT = 0, VT = 72 * 128, QT = VT + 8192, OT = QT + 8192;
    const int col = cb * 128 + (l & 7) * 16, rl = l >> 3;
    {
      const int r0 = tb * 64 - 8 + w * 8 + rl;
      dma16p<true>((const char*)k, (u32)(max(r0, 0) * (D_ * 2) + col), L0 + KT + w * 1024);
      const u32 vo = (u32)((tb * 64 + w * 8 + rl) * (D_ * 2) + col);
      dma16p<true>((const char*)k + 24 * D_ * 2, vo, L0 + KT + (w + 4) * 1024);
      if (w == 0) dma16p<true>((const char*)k + 56 * D_ * 2, vo, L0 + KT + 8 * 1024);
      dma16p<true>((const char*)v, vo, L0 + VT + w * 1024);
      dma16p<true>((const char*)v + 32 * D_ * 2, vo, L0 + VT + (w + 4) * 1024);
    }
    const int sl = w * 16 + (l >> 4) * 4, cq = l & 15;
    u32x4 w0, w1, pf0, pf1;
    gload16<0>(w0, (u32)((cb * 64 + cq * 4) * 8), (const char*)shk);
    gload16<16>(w1, (u32)((cb * 64 + cq * 4) * 8), (const char*)shk);
    {
      const u32 vo = (u32)((tb * 64 + w * 8 + rl) * (D_ * 2) + col);
      dma16p<false>((const char*)q, vo, L0 + QT + w * 1024);
      dma16p<false>((const char*)q + 32 * D_ * 2, vo, L0 + QT + (w + 4) * 1024);
    }
    wait_vm2<2>(w0, w1);
    BARRIER();
    u32x2 kr[7], vr[4];
#pragma unroll
    for (int i = 0; i < 7; ++i) kr[i] = *(const LAS u32x2*)(LP + (KT + (sl + 5 + i) * 128 + cq * 8));
#pragma unroll
    for (int i = 0; i < 4; ++i) vr[i] = *(const LAS u32x2*)(LP + (VT + (sl + i) * 128 + cq * 8));
    const u32 po = (u32)((r8 < 32 ? (b & 7) * 64 + r8 * 2 : 512 + (b & 7) * 8 + (r8 - 32) * 2) * 4096 + tid * 16);
    gload16<0>(pf0, po, (const char*)ssm);
    gload16<0>(pf1, po + 4096, (const char*)ssm);
    if (tb == 0 && sl == 0) { kr[0] = (u32x2){0u, 0u}; kr[1] = kr[0]; kr[2] = kr[0]; }
#pragma unroll
    for (int c = 0; c < 4; ++c) {
      const u32 wa = (c < 2) ? w0[2 * (c & 1)] : w1[2 * (c & 1)], wb = (c < 2) ? w0[2 * (c & 1) + 1] : w1[2 * (c & 1) + 1];
      const float f0 = bflo(wa), f1 = bfhi(wa), f2 = bflo(wb), f3 = bfhi(wb);
      float kk[7], kvv[4];
#pragma unroll
      for (int i = 0; i < 7; ++i) kk[i] = (c & 1) ? bfhi(kr[i][c >> 1]) : bflo(kr[i][c >> 1]);
#pragma unroll
      for (int i = 0; i < 4; ++i) {
        const float ks = kk[i + 3] * f0 + kk[i + 2] * f1 + kk[i + 1] * f2 + kk[i] * f3;
        const float vv = (c & 1) ? bfhi(vr[i][c >> 1]) : bflo(vr[i][c >> 1]);
        kvv[i] = ks * vv;
      }
      u32x2 o = {pack2(kvv[3], kvv[2]), pack2(kvv[1], kvv[0])};
      *(LAS u32x2*)(LP + (OT + (cq * 4 + c) * 128 + (((sl >> 3) ^ (cq & 7)) * 16) + ((sl & 4) ? 0 : 8))) = o;
    }
    wait_vm<2>();
    LDS_BARRIER();
#pragma unroll
    for (int i = 0; i < 2; ++i) {
      const int ch = w * 16 + i * 8 + (l >> 3);
      const u32x4 o = *(const LAS u32x4*)(LP + (OT + (w * 16 + i * 8) * 128 + l * 16));
      gstore16(ws + OFF_KVT, (u32)((cb * 64 + ch) * 4096 + (tb * 8 + ((l & 7) ^ ((ch >> 2) & 7))) * 16), o);
    }
#pragma unroll
    for (int i = 0; i < 2; ++i) {
      const int gl = i * 8 + (l >> 3), t2 = w * 16 + (l & 7) * 2;
      const u32x2 a = *(const LAS u32x2*)(LP + (QT + t2 * 128 + gl * 8)), c = *(const LAS u32x2*)(LP + (QT + (t2 + 1) * 128 + gl * 8));
      u32x4 o = {a[0], a[1], c[0], c[1]};
      gstore16(ws + OFF_XP, (u32)(((cb * 16 + gl) * S_ + tb * 64 + t2) * 8), o);
    }
    if (b < 41) {
      const u32 f = b < 9 ? 0u : 0xffffffffu;
      u32x4 z = {f, f, f, f};
      gstore16(ws + OFF_FLG, (u32)(b * 4096 + tid * 16), z);
    }
    asm volatile("s_waitcnt vmcnt(0)" : "+v"(pf0), "+v"(pf1)::"memory");
  } else {
    if (w == 0) {
      unsigned long long c0;
      asm volatile("s_getpc_b64 s[40:41]\n\ts_add_u32 s40, s40, k1_conv@rel32@lo+4\n\ts_addc_u32 s41, s41, k1_conv@rel32@hi+12\n\ts_mov_b64 %0, s[40:41]" : "=s"(c0)::"s40", "s41", "scc");
      code_touch((const char*)c0, (u32)(l * 128), (u32)(uintptr_t)(ldsp)lds);
    }
    const int row = blockIdx.x * 32 + w * 8 + (l >> 3);
    const u16* p = q + (size_t)row * D_ + (l & 7) * 8;
    u32x4 x[9];
#pragma unroll
    for (int i = 0; i < 9; ++i) x[i] = *(const u32x4*)(p + i * 64);
    float sum = 0.f;
#pragma unroll
    for (int i = 0; i < 9; ++i)
#pragma unroll
      for (int j = 0; j < 4; ++j) sum += bflo(x[i][j]) + bfhi(x[i][j]);
    sum += __shfl_xor(sum, 1); sum += __shfl_xor(sum, 2); sum += __shfl_xor(sum, 4);
    const float mu = sum * (1.f / D_);
    float sq = 0.f;
#pragma unroll
    for (int i = 0; i < 9; ++i)
#pragma unroll
      for (int j = 0; j < 4; ++j) { const float a = bflo(x[i][j]) - mu, c = bfhi(x[i][j]) - mu; sq += a * a + c * c; }
    sq += __shfl_xor(sq, 1); sq += __shfl_xor(sq, 2); sq += __shfl_xor(sq, 4);
    if ((l & 7) == 0) { f32x2 o = {mu, rsqrtf(sq * (1.f / D_) + 1e-5f)}; ((f32x2*)(ws + OFF_ST1))[row] = o; }
  }
}

constexpr int K1_UPLANE = 2304, K1_UCH = 4608, K1_TOFF = 4 * K1_UCH, K1_TCOPY = 4160, K1_TCH = 4 * K1_TCOPY;
constexpr int K1_YOFF = K1_TOFF + 4 * K1_TCH, K1_YBUF = 8192;
constexpr int K1_LDS = K1_YOFF + 8 * K1_YBUF;
constexpr int K1_GRID = 256;

extern "C" __global__ __launch_bounds__(512) void k1_conv(const u16* __restrict__ ssm, const u16* __restrict__ ssmD, const u16* __restrict__ wg,
                                                          const u16* __restrict__ wu, char* __restrict__ ws, const u16* __restrict__ wd, const u16* __restrict__ outp) {
  extern __shared__ __attribute__((aligned(128))) char lds[];
  asm volatile("" ::"s"(ssm), "s"(ssmD), "s"(wg), "s"(wu), "s"(ws), "s"(wd), "s"(outp));
  const int tid = threadIdx.x, l = tid & 63;
  const int w = __builtin_amdgcn_readfirstlane(tid >> 6);
  const int b = blockIdx.x;
  const ldsp LP = (ldsp)lds;
  const u32 L0 = (u32)(uintptr_t)LP;
  if (b >= 144) {
    if (w == 6 && b < 152) {
      code_touch(ws + OFF_HF, min((u32)l << 20, (u32)(WS_USED - OFF_HF - 4)), L0 + 256);
      code_touch((const char*)wd, min((u32)l << 20, (u32)(D_ * DF_ * 2 - 4)), L0 + 256);
      code_touch((const char*)outp, min((u32)l << 20, (u32)(S_ * D_ * 2 - 4)), L0 + 256);
    }
    if (w == 7) {
      unsigned long long c0; u32 cl;
      asm volatile("s_getpc_b64 s[40:41]\n\ts_add_u32 s40, s40, ffn_fused@rel32@lo+4\n\ts_addc_u32 s41, s41, ffn_fused@rel32@hi+12\n\ts_mov_b64 %0, s[40:41]\n\ts_mov_b32 %1, .Lffn_code_end-ffn_fused"
                   : "=s"(c0), "=s"(cl)::"s40", "s41", "scc");
#pragma unroll
      for (int i = 0; i < 5; ++i) code_touch((const char*)c0, min((u32)(l * 128 + i * 8192), cl - 4u), L0);
    }
    __builtin_amdgcn_s_sleep(32);
    const int j = ((b - 144) >> 3) + 14 * w, jj = j * 2 + ((b >> 2) & 1), cbl = jj >> 3;
    if (j < 72) colsum_slice(wg, wu, (float*)(ws + OFF_CS), (cbl < 16 ? (b & 3) * 16 + cbl : 64 + (b & 3) * 2 + (cbl - 16)) * 8 + (jj & 7), l);
  } else {
    const u16* __restrict__ kvt = (const u16*)(ws + OFF_KVT);
    const u16* __restrict__ xp = (const u16*)(ws + OFF_XP);
    const f32x2* __restrict__ st1 = (const f32x2*)(ws + OFF_ST1);
    const int g = (b >> 3) < 16 ? (b & 7) * 16 + (b >> 3) : 128 + (b & 7) * 2 + ((b >> 3) - 16), ch = w & 3, hf_ = w >> 2;
    const int gch = g * 4 + ch;
    {
      const char* src = (const char*)(kvt + (size_t)gch * S_);
      const u32 vo = (2 * l + hf_) * 16;
      const u32 dst = L0 + ch * K1_UCH + hf_ * K1_UPLANE + 256;
      dma16(src, vo, dst);
      dma16(src + 2048, vo, dst + 1024);
    }
    const u16* krow = ssm + (size_t)gch * S_;
    u32x4 wv[2]; u32x2 wn[2];
#pragma unroll
    for (int it = 0; it < 2; ++it) {
      const int idx = hf_ * 128 + it * 64 + l;
      wv[it] = *(const u32x4*)(krow + (idx > 0 ? idx - 1 : 0) * 8);
      wn[it] = *(const u32x2*)(krow + idx * 8);
    }
    const u32x4 wl = *(const u32x4*)(krow + opq(255 * 8));
    const u32 dv1 = ssmD[opq(gch)];
    SCHED0();
    u32x2 xr[4]; f32x2 st[4];
#pragma unroll
    for (int i = 0; i < 4; ++i) { const int s = (tid >> 4) * 32 + (tid & 15) + (i & 1) * 16 + (i >> 1) * 1024; xr[i] = *(const u32x2*)(xp + ((size_t)g * S_ + s) * 4); st[i] = st1[s]; }
    SCHED0();
    const u32x4 z4 = {0u, 0u, 0u, 0u};
    if (l < 16) *(LAS u32x4*)(LP + (ch * K1_UCH + hf_ * K1_UPLANE + l * 16)) = z4;
    const ldsp tb = LP + (K1_TOFF + ch * K1_TCH);
    auto put = [&](int pos  , u32x4 a, u32x2 n) {
      const u32 o0 = __builtin_amdgcn_alignbit(a[1], a[0], 16), o1 = __builtin_amdgcn_alignbit(a[2], a[1], 16), o2 = __builtin_amdgcn_alignbit(a[3], a[2], 16),
                o3 = __builtin_amdgcn_alignbit(n[0], a[3], 16), o4 = __builtin_amdgcn_alignbit(n[1], n[0], 16);
      u32x4 c1 = {o0, o1, o2, o3}, c2 = {a[1], a[2], a[3], n[0]}, c3 = {o1, o2, o3, o4};
      *(LAS u32x4*)(tb + pos * 16) = a;
      *(LAS u32x4*)(tb + (K1_TCOPY + pos * 16)) = c1;
      *(LAS u32x4*)(tb + (2 * K1_TCOPY + pos * 16)) = c2;
      *(LAS u32x4*)(tb + (3 * K1_TCOPY + pos * 16)) = c3;
    };
    if (hf_ == 0 && l == 0) wv[0] = z4;
#pragma unroll
    for (int it = 0; it < 2; ++it) put(hf_ * 128 + it * 64 + l + 1, wv[it], wn[it]);
    if (hf_ == 1 && l < 2) { u32x2 z2 = {0u, 0u}; put(l ? 257 : 0, l ? wl : z4, z2); }
    const int a_ = l & 15, q_ = l >> 4;
    const int cpy = (1 + a_) & 3;
    const ldsp taddr = opaque(LP + (K1_TOFF + ch * K1_TCH + cpy * K1_TCOPY + 2 * (1 + a_ + 8 * q_ - cpy) + hf_ * 256));
    const ldsp uaddr = opaque(LP + (ch * K1_UCH + ((q_ + 1) & 1) * K1_UPLANE + (16 + a_ - (q_ >> 1) - 8 * hf_ - 6) * 16));
    f32x4 acc[8];
    u32x4 dg;
    {
      const int j = 15 - a_ - 8 * q_;
      const u32 dval = hf_ ? 0u : dv1;
#pragma unroll
      for (int e = 0; e < 4; ++e) dg[e] = (j == 2 * e) ? dval : ((j == 2 * e + 1) ? (dval << 16) : 0u);
    }
    asm volatile("" : "+v"(dg));
    wait_vm<8>();
    LDS_BARRIER();
    u32x4 U[2][8], T[2][8];
    auto ldU = [&](int e, int j) { return *(const LAS u32x4*)(uaddr + ((3 - e) * 32 + 256 * j)); };
    auto ldT = [&](int e, int m) {
      const u32x2 lo = *(volatile const LAS u32x2*)(taddr + (64 * e + 512 * m)), hi = *(volatile const LAS u32x2*)(taddr + (8 + 64 * e + 512 * m));
      u32x4 t = {lo[0], lo[1], hi[0], hi[1]};
      return t;
    };
#pragma unroll
    for (int j = 0; j < 8; ++j) U[0][j] = ldU(0, j);
#pragma unroll
    for (int j = 0; j < 8; ++j) T[0][j] = ldT(0, j);
    SCHED0();
#pragma unroll
    for (int n = 0; n < 8; ++n) mma<true>(acc[n], dg, U[0][n]);
    SCHED0();
    float xs[16], xn[16];
    const ldsp yw = LP + (K1_YOFF + (hf_ * 4 + ch) * K1_YBUF + ((4 * a_ + q_) ^ ((a_ >> 1) & 3) ^ ((a_ & 2) << 1)) * 16);
    static_for<0, 144>([&](auto ic) {
      constexpr int e = ic.value / 36, i = ic.value % 36;
      constexpr auto mn = [] { int k = 0; for (int m = 0; m < 8; ++m) for (int n = m; n < 8; ++n) { if (k == i) return m * 8 + n; ++k; } return 0; }();
      constexpr int m = mn >> 3, n = mn & 7;
      if constexpr (e < 3 && i < 32 && (i & 1) == 0) {
        if constexpr ((i & 2) == 0) U[(e + 1) & 1][i >> 2] = ldU(e + 1, i >> 2); else T[(e + 1) & 1][i >> 2] = ldT(e + 1, i >> 2);
      }
      mma<false>(acc[n], T[e & 1][m], U[e & 1][n - m]);
      if constexpr (e == 3 && i < 16) {
        constexpr int ti = i >> 2, c = i & 3;
        xs[i] = (c & 1) ? bfhi(xr[ti][c >> 1]) : bflo(xr[ti][c >> 1]);
        xn[i] = hide(xs[i] - st[ti][0]) * st[ti][1];
        pinf(xs[i]); pinf(xn[i]);
      }
      if constexpr (e == 3 && n == 7 && m >= 1 && m <= 4) *(LAS f32x4*)(yw + (m - 1) * 1024) = acc[m - 1];
      SCHED0();
    });
    MFMA_DRAIN(); SCHED0();
#pragma unroll
    for (int n = 4; n < 8; ++n) *(LAS f32x4*)(yw + n * 1024) = acc[n];
    LDS_BARRIER();
    {
      float y[4][8];
#pragma unroll
      for (int i = 0; i < 4; ++i) {
        const int s = (tid >> 4) * 32 + (tid & 15) + (i & 1) * 16, sl = s >> 2;
        const ldsp yb = LP + (K1_YOFF + (sl ^ ((sl >> 3) & 3) ^ ((sl >> 1) & 4)) * 16 + (s & 3) * 4 + (i >> 1) * 4096);
#pragma unroll
        for (int c = 0; c < 8; ++c) y[i][c] = *(const LAS float*)(yb + c * K1_YBUF);
      }
#pragma unroll
      for (int j = 0; j < 2; ++j) {
        u32x4 o;
#pragma unroll
        for (int e = 0; e < 2; ++e) {
          const int i = 2 * j + e;
          float hv[4];
#pragma unroll
          for (int c = 0; c < 4; ++c) hv[c] = fmaf(xn[4 * i + c], hide(y[i][c] + y[i][4 + c]), xs[4 * i + c]);
          o[2 * e] = pack2(hv[0], hv[1]); o[2 * e + 1] = pack2(hv[2], hv[3]);
        }
        gstore16(ws + OFF_HF + (size_t)j * 32 * 36864 + (g >> 3) * 2048 + (g & 1) * 1024 + ((g >> 1) & 3) * 256, (u32)((tid >> 4) * 36864 + (tid & 15) * 16), o);
      }
    }
  }
}

constexpr int G1_QE = 147456;
constexpr int G1_QL = 14 * 8192;
constexpr int G1_CS = G1_QE + 8192;
constexpr int G1_CP = G1_CS + 2048;
constexpr int FF_LDS = G1_CP + 5120 + 256;
constexpr int PF_ISS0 = 86;
constexpr int STG = 18432, NST = 8;
constexpr int G1_D = 4, G1_DW = 6, HD = 5, HB = HD + 1;
constexpr int p1_ops(int x, bool even_only) { return (x + G1_DW < 18 ? 1 : 0) + (x + G1_D < 18 ? (even_only ? 1 : 2) : 0) + (x == 18 - G1_D ? 3 : 0); }
constexpr int p1_wait(int g) {
  int n = 0;
  if (g + 1 < G1_D) n += 3 * (G1_D - (g + 2)) + (G1_DW - G1_D);
  for (int x = (g + 2 - G1_D > 0 ? g + 2 - G1_D : 0); x < g; ++x) n += p1_ops(x, false);
  return n + p1_ops(g, true);
}
constexpr bool p1_bar(int g) { return g < G1_D - 1 || (g - (G1_D - 1)) % (G1_DW - G1_D + 1) == 0; }
constexpr float GC = -2.3022081983f;
constexpr int U2 = 216;
constexpr int g1_q_issue(int r) { return 8 + 6 * r; }
constexpr int g1_b_issue(int j) { return j < 14 ? 29 + 3 * j : 73 + 3 * (j - 14); }
constexpr int G1_E0 = 9;
constexpr int epi_start(int i) { return G1_E0 + 26 * i; }
static_assert(G1_E0 + 26 * 7 + 24 < 216, "epilogue stream");
constexpr bool epi_stores(int i) { return i == 1 || i == 3 || i == 4 || i == 6; }
constexpr int w_issue(int n, int k) {
  return n < 4 ? 191 + 4 * n + 2 * k : n < 6 ? 209 + 4 * (n - 4) + 2 * k : (n & 1) ? U2 + 9 * (n - 6) + 4 * k : U2 + 9 * (n - 6) + 7 + k;
}
constexpr int h_issue(int n, int k) { return n < 5 ? 192 + 4 * n + 2 * k : U2 + 9 * (n - (n < 10 ? HD : 4)) + 2 + 3 * k; }
constexpr int dq_issue(int r) { return r == 0 ? 183 : r < 4 ? U2 + 9 * (4 * r - 3) + 1 : U2 + 9 * 11 + 1; }
constexpr int poll_iss(int p) { return p == 0 ? 180 : p == 1 ? U2 + 9 * 2 + 3 : U2 + 9 * 7 + 3; }
constexpr int poll_chk(int p) { return p == 0 ? 191 : p == 1 ? U2 + 9 * 6 + 1 : U2 + 9 * 10 + 1; }
constexpr int RES_ISS = U2 + 9 * 16 + 7;
template <class F> constexpr int find_at(int n, int u, F f) { for (int i = 0; i < n; ++i) if (f(i) == u) return i; return -1; }
constexpr int wait_k(int k0, int k1) {
  int n = 0;
  auto in = [&](int key) { return key > k0 && key < k1 ? 1 : 0; };
  for (int r = 0; r < 4; ++r) n += in(4 * g1_q_issue(r) + 2);
  for (int j = 0; j < 18; ++j) n += in(4 * g1_b_issue(j) + 2);
  for (int i = 0; i < 8; ++i) if (epi_stores(i)) n += in(4 * (epi_start(i) + 18)) + in(4 * (epi_start(i) + 24));
  n += in(4 * (U2 + 11)) + in(4 * (U2 + 14));
  for (int s = 0; s < 18; ++s) for (int k = 0; k < 2; ++k) n += in(4 * w_issue(s, k) + 2) + in(4 * h_issue(s, k) + 2);
  for (int p = 0; p < 3; ++p) n += in(4 * poll_iss(p) + 2);
  n += 3 * in(4 * RES_ISS + 2);
  for (int r = 0; r < 5; ++r) n += in(4 * (PF_ISS0 + 4 * r) + 2);
  for (int r = 0; r < 5; ++r) n += in(4 * dq_issue(r) + 2);
  return n;
}
constexpr int wait_q(int r, int tw) { return wait_k(4 * g1_q_issue(r) + 2, 4 * tw + 1); }
constexpr int wait_b(int j, int tw) { return wait_k(4 * g1_b_issue(j) + 2, 4 * tw + 1); }
constexpr int cmax(int a, int b) { return a > b ? a : b; }
static_assert(p1_bar(6) && p1_bar(9), "column-sum hand-over uses these barriers");
static_assert(w_issue(1, 1) > epi_start(6) + 24 && h_issue(0, 0) > poll_chk(0) && h_issue(10, 0) > poll_chk(1) && h_issue(14, 0) > poll_chk(2), "schedule");
constexpr int kk_of(int s) { return s < 8 ? 9 * ((s >> 1) & 1) + 4 * (s & 1) + (s >> 2) : s < 10 ? 9 * (s - 8) + 8 : 9 * (((s - 10) >> 1) & 1) + 4 * (s & 1) + 2 + ((s - 10) >> 2); }
constexpr u32 rowc(int kk) { return (kk % 9 < 8 ? (kk / 9) * 256 + (kk % 9) * 32 : (kk / 9) * 32) * (D_ * 2); }
struct Acc { f32x4 a[2][2]; };

extern "C" __global__ __launch_bounds__(512, 2) void ffn_fused(const u16* __restrict__ wg, const u16* __restrict__ wu, const u16* __restrict__ wd,
                                                               char* __restrict__ ws, u16* __restrict__ out) {
  extern __shared__ __attribute__((aligned(128))) char lds[];
  asm volatile("" ::"s"(wg), "s"(wu), "s"(wd), "s"(ws), "s"(out));
  const int tid = threadIdx.x, l = tid & 63;
  const int w = __builtin_amdgcn_readfirstlane(tid >> 6);
  const int b = blockIdx.x;
  const int fg = (b & 3) * 8 + ((b >> 3) & 7), tg = ((b >> 2) & 1) * 4 + (b >> 6);
  const int tt = tg * 8 + w;
  const ldsp LP = (ldsp)lds;
  const u32 L0 = (u32)(uintptr_t)LP;
  constexpr u32 RS = DF_ * 2, GS = 32 * RS;
  const int wblk = w & 1, whr = (w >> 1) & 1, whalf = w >> 2;
  const rsrc_t rW = mk_rsrc((w & 1) ? wu : wg, D_ * DF_ * 2), rS = mk_rsrc(ws, (u32)WS_USED), rD = mk_rsrc(wd, D_ * DF_ * 2);
  const u32 ab_voff = (u32)((16 * whalf + 8 * whr + (l >> 3)) * RS + ((((l & 7) >> 1) ^ (((l >> 4) & 1) | (whr << 1))) * 32) + (l & 1) * 16 + fg * 256);
  const u32 ab_dst = L0 + whalf * 4096 + wblk * 2048 + whr * 1024;
  auto mk_qvoff = [&](u32 l2) { return (u32)(((l2 >> 1) ^ ((l2 >> 2) & 4)) * RS + (l2 & 1) * 16 + (w >> 1) * GS + 8192 + fg * 32); };
  u32x4 h[2][18];
  const u32 lo16 = l * 16;
  const u32 h_voff = lo16 + (u32)OFF_HF + tt * 36864;
  auto issue_dma = [&](auto ic) { constexpr int g = ic.value; bdma_i<g * 8192, g * GS>(rW, ab_voff, ab_dst); };
  auto issue_h = [&](auto ic, auto tc) { constexpr int g = ic.value, tl = tc.value; bload<(g & 1) * 2048 + tl * 1024>(h[tl][g], rS, h_voff, (g >> 1) * 4096); };
  static_for<0, G1_D>([&](auto ic) { issue_dma(ic); issue_h(ic, IC<0>{}); issue_h(ic, IC<1>{}); });
  static_for<G1_D, G1_DW>(issue_dma);
  const char* code0; u32 codelen;
  CODE_RANGE(code0, codelen, ".Lffn_code_end");
  u32x4 sx[6];
  stat_loads(fg, sx, rS, h_voff, fg * 6144, fg * 6144 + 4096);
  const u32 lw = L0 + w * 1024;
  const int rq = l >> 4, rr = (l >> 2) & 3, rp = l & 3;
  const u32 rb0 = L0 + (rq >> 1) * 4096 + (rq & 1) * 1024 + rr * 128 + rp * 8 + (((rr >> 1) | ((rq & 1) << 1)) * 32);
#define abase(c, off) lds_base(rb0 , (c) * 32, (off))
  Acc acc[4];
  const char* stb = ws + OFF_ST2;
  u32x4 sg4; u32x2 sg2;
  {
    float s1[2] = {0.f, 0.f}, s2[2] = {0.f, 0.f};
    ldsp pb[3];
    Frag pf[2][3];
    auto fetch1 = [&](auto ic, int c, int hi) {
      constexpr int s = ic.value;
      if constexpr ((s & 15) == 0) { if (hi == 0) pb[c] = abase(c, s * 4096); }
      constexpr int o = ((s & 15) >> 1) * 8192 + (s & 1) * 2048;
      if (hi) pf[s & 1][c].hi = tr_half(pb[c] + (o + 512)); else pf[s & 1][c].lo = tr_half(pb[c] + o);
    };
    wait_vm2<3 * (G1_D - 1) + G1_DW - G1_D>(h[0][0], h[1][0]);
    BARRIER();
    swap_halves(h[0][0], h[1][0]);
#pragma unroll
    for (int c = 0; c < 3; ++c) { fetch1(IC<0>{}, c, 0); fetch1(IC<0>{}, c, 1); }
    static_for<0, 36>([&](auto ic) {
      constexpr int s = ic.value, g = s >> 1, mt = s & 1;
      constexpr int SQ = 2 * (18 - G1_D);
      auto mm = [&](int c) {
#pragma unroll
        for (int tl = 0; tl < 2; ++tl) {
          mma<g == 0>(acc[c].a[mt][tl], frag_of(pf[s & 1][c]), h[tl][g]);
          SCHED0();
          if constexpr (s < 35) fetch1(IC<s + 1>{}, c, tl);
          SCHED0();
        }
      };
      if constexpr (mt == 1 && s < 35) {
        wait_vm2<p1_wait(g)>(h[0][g + 1], h[1][g + 1]);
        if constexpr (p1_bar(g)) BARRIER();
      }
      SCHED0();
      touch(pf[s & 1][1]);
      mm(0);
      if constexpr (mt == 0 && g + G1_DW < 18) issue_dma(IC<g + G1_DW>{});
      if constexpr (mt == 1 && g + G1_D < 18) issue_h(IC<g + G1_D>{}, IC<1>{});
      if constexpr (s == 3) {
        const int ii = min(tid, 287), qd = ii / 72, r = ii - qd * 72;
        if (w < 5) dma16(ws + OFF_CS, (u32)(qd * 2304 + (r < 64 ? fg * 64 + r : 2048 + fg * 8 + (r - 64))) * 16u, L0 + G1_CP + w * 1024);
      }
      if constexpr (s == 16) {
        const u32 pi = min(opq(tid), 71u);
        const ldsp cp = LP + (G1_CP + pi * 16);
        const f32x4 r = (*(const LAS f32x4*)cp + *(const LAS f32x4*)(cp + 1152)) + (*(const LAS f32x4*)(cp + 2304) + *(const LAS f32x4*)(cp + 3456));
        *(LAS f32x4*)(LP + (G1_CS + pi * 16)) = r;
      }
      if constexpr (s == SQ) {
        bdma<G1_QE>(rW, mk_qvoff(l), 0, lw);
      }
      SCHED0();
      mm(1);
      if constexpr (g + G1_D < 18 && mt == 0) issue_h(IC<g + G1_D>{}, IC<0>{});
      if constexpr (s == SQ) { const u32 so = tt * 2048 + ((rq >> 1) * 16 + (l & 15)) * 64 + (rq & 1) * 24; ld_sc1_x4(sg4, so, stb); ld_sc1_x2<16>(sg2, so, stb); }
      if constexpr (mt == 1 && s < 35) swap_halves(h[0][g + 1], h[1][g + 1]);
      if constexpr (s >= 8 && s < 14) { if constexpr (s == 8) pin6(sx); stat_acc(fg, sx[s - 8], s1, s2); }
      SCHED0();
      mm(2);
      if constexpr (s == 14) {
        const float a0 = swap32_sum(swap16_sum(s1[0])), c0 = swap32_sum(swap16_sum(s2[0]));
        const float a1 = swap32_sum(swap16_sum(s1[1])), c1 = swap32_sum(swap16_sum(s2[1]));
        u32x2 gr = {__float_as_uint((l & 16) ? a1 : a0), __float_as_uint((l & 16) ? c1 : c0)};
        st_sc1_x2_if(fg, stb, (u32)(tt * 2048 + fg * 8 + (l & 31) * 64), gr);
      }
      SCHED0();
    });
  }
  Frag wf[4];
  ldsp fb = nullptr, fb2 = nullptr;
  auto fetch = [&](auto ic, int hi) {
    constexpr int i = ic.value, p = i / 36, s = i % 36;
    if constexpr (p != 1) {
      constexpr int c = (p == 0) ? 3 : p - 2;
      constexpr int pos = (p == 0) ? (s + 28) % 36 : s;
      if constexpr (s == 0 || (pos & 15) == 0) { if (!hi) fb = abase(c, (pos & ~15) * 4096); }
      constexpr int o = ((pos & 15) >> 1) * 8192 + (pos & 1) * 2048;
      if (hi) wf[i & 3].hi = tr_half(fb + (o + 512)); else wf[i & 3].lo = tr_half(fb + o);
    } else {
      if constexpr (s == 0 || s == 8) {
        if (!hi) {
          const u32 l2 = opq(tid);
          const int qo = (l2 >> 4) & 1;
          const int qb = (8 * ((l2 >> 4) & 3) + ((l2 >> 2) & 3)) * 32 + (l2 & 3) * 8 + (s == 0 ? G1_QE : G1_QL);
          fb = opaque(LP + (qb + qo * 128)); fb2 = opaque(LP + (qb + (1 - qo) * 128));
        }
      }
      constexpr int o = (s < 8 ? s : s - 8) * 1024;
      if (hi) wf[i & 3].hi = tr_half(fb2 + o); else wf[i & 3].lo = tr_half(fb + o);
    }
  };
  static_for<0, 3>([&](auto ic) { fetch(ic, 0); fetch(ic, 1); });
  SCHED0();
  float nmu[2], k1[2], k2[2], kd[2];
  auto ln2_stats = [&]() {
    wait0_x6(sg4, sg2);
#pragma unroll 1
    for (int it = 0; it < (1 << 16); ++it) {
      const bool bad = (sg4[0] & sg4[1]) == 0xffffffffu || (sg4[2] & sg4[3]) == 0xffffffffu || (sg2[0] & sg2[1]) == 0xffffffffu;
      if (!__any(bad)) break;
      __builtin_amdgcn_s_sleep(2);
      const u32 so = tt * 2048 + ((rq >> 1) * 16 + (l & 15)) * 64 + (rq & 1) * 24;
      ld_sc1_x4(sg4, so, stb); ld_sc1_x2<16>(sg2, so, stb);
      wait0_x6(sg4, sg2);
    }
    {
      const float t1 = swap16_sum(__uint_as_float(sg4[0]) + __uint_as_float(sg4[2]) + __uint_as_float(sg2[0]));
      const float t2 = swap16_sum(__uint_as_float(sg4[1]) + __uint_as_float(sg4[3]) + __uint_as_float(sg2[1]));
      const float mu = hide(t1 * (1.f / D_));
      const float var = fmaxf(t2 * (1.f / D_) - mu * mu, 0.f) + 1e-5f;
      const auto m2 = __builtin_amdgcn_permlane32_swap(__float_as_uint(-mu), __float_as_uint(-mu), false, false);
      const auto v2 = __builtin_amdgcn_permlane32_swap(__float_as_uint(var), __float_as_uint(var), false, false);
#pragma unroll
      for (int tl = 0; tl < 2; ++tl) {
        kd[tl] = __uint_as_float(v2[tl]);
        const float r = __builtin_amdgcn_rsqf(kd[tl]);
        nmu[tl] = __uint_as_float(m2[tl]);
        k1[tl] = hide(GC * r);
        k2[tl] = hide(k1[tl] * 0.044715f * r * r);
      }
    }
  };
  char* hdst = ws + OFF_HH + ((size_t)tt * 288 + (fg >> 1) * 9 + (fg & 1) * 4) * 1024;
  const ldsp csb = opaque(LP + (G1_CS + rq * 32));
  f32x4 cc;
  auto ldcs = [&](int lc, int hfx) { cc = *(const LAS f32x4*)(csb + (lc * 128 + hfx * 16)); };
  u32x4 eo[2];
  u32x2 eq[2];
  float m0, m1, tu0, tu1, y0, y1;
  auto emit = [&](int lc, int i, u32 pk) {
    const int tl = i & 1, hx = i >> 1;
    if (lc == 8) {
      eq[tl][hx] = pk;
      if (hx) st_sc1_x2(hdst + (size_t)tl * 147456 + (8 - (fg & 1) * 4) * 1024 + (fg & 1) * 8, lo16, eq[tl]);
    } else {
      eo[tl][(lc & 1) * 2 + hx] = pk;
      if ((lc & 1) && hx) st_sc1_x4(hdst + (size_t)tl * 147456 + (lc >> 1) * 1024, lo16, eo[tl]);
    }
  };
  auto epi = [&](const Acc& a, int lc, int st, int nlc, int part) {
    const int i = st / 6, sg = st % 6, tl = i & 1, e = (i >> 1) * 2;
    if (sg == 0) {
      if (part == 0) { if (i > 0) { u32 pk = pack2(tu0, tu1 * y1); pin(pk); emit(lc, i - 1, pk); } }
      else if (i < 4) { m0 = fmaf(nmu[tl], cc[0], a.a[0][tl][e]); m1 = fmaf(nmu[tl], cc[1], a.a[0][tl][e + 1]); pinf(m0); pinf(m1); }
    } else if (sg == 1) {
      if (part == 0) { tu0 = fmaf(nmu[tl], cc[2], a.a[1][tl][e]); tu1 = fmaf(nmu[tl], cc[3], a.a[1][tl][e + 1]); pinf(tu0); pinf(tu1); }
      else {
        y0 = hide(m0 * m0); y1 = m1 * m1; pinf(y0); pinf(y1);
        if (i == 1) ldcs(lc, 1);
        if (i == 3 && nlc >= 0) ldcs(nlc, 0);
      }
    } else if (sg == 2) {
      if (part == 0) { tu0 = hide(tu0 * m0); tu1 = tu1 * m1; pinf(tu0); pinf(tu1); }
      else { y0 = hide(fmaf(y0, k2[tl], k1[tl])); y1 = fmaf(y1, k2[tl], k1[tl]); pinf(y0); pinf(y1); }
    } else if (sg == 3) {
      if (part == 0) { y0 = hide(y0 * m0); y1 = y1 * m1; pinf(y0); pinf(y1); }
      else { y0 = __builtin_amdgcn_exp2f(y0); pinf(y0); }
    } else if (sg == 4) {
      if (part == 0) { y1 = __builtin_amdgcn_exp2f(y1); pinf(y1); }
      else { y0 = fmaf(y0, kd[tl], kd[tl]); pinf(y0); }
    } else {
      if (part == 0) { y0 = __builtin_amdgcn_rcpf(y0); y1 = fmaf(y1, kd[tl], kd[tl]); pinf(y0); pinf(y1); }
      else { y1 = __builtin_amdgcn_rcpf(y1); tu0 = tu0 * y0; pinf(y1); pinf(tu0); }
    }
  };
  const int r5 = b >> 3;
  const int ks = b & 3, tg2 = ((b >> 2) & 1) * 8 + (r5 & 7), dt = r5 >> 3;
  const int tq = w & 3, kh = w >> 2;
  constexpr u32 RD = D_ * 2;
  const u32 d_dst = L0 + kh * 9216 + tq * 2048;
  const int mb = ks * 4 + kh * 2;
  const u32 rowA = mb * 256 * RD, rowQ = (4096 + mb * 32) * RD;
  auto row0 = [&](int kk) { const int q = kk / 9, i = kk - 9 * q; return i < 8 ? rowA + (q * 256 + i * 32) * RD : rowQ + q * 32 * RD; };
  u32x4 hb[HB][2];
  u32 hv0 = 0, hv1 = 0;
  const char* myflag = ws + OFF_FLG + 16384 + b * 64;
  const char* pflags = ws + OFF_FLG + 16384 + ((b & 7) + kh * 32 + ((r5 & 7) >> 1) * 64) * 64;
  u32 d_voff = 0, dq_voff = 0, p_voff = 0, pv = 0, q_voff = 0;
  f32x4 ac2[9][2];
  ldsp pb[5];
  u32 rb2 = 0, rbq = 0;
  auto fetch2 = [&](auto ic, int hi) {
    constexpr int i = ic.value, s = i / 9, c = i % 9, stg = s % NST;
    if constexpr (stg % 3 == 0 && c < 4) { if (!hi) pb[c] = lds_base(rb2, c * 32, stg * STG); }
    if constexpr (c == 8 && (stg % 3 == 0 || s == 4)) { if (!hi) pb[4] = opaque((ldsp)(uintptr_t)(opq(rbq) + (s < 4 ? (u32)(G1_QE - 8192) - kh * (9216 - 1024) : (u32)((stg - stg % 3) * STG)))); }
    constexpr int o = c < 8 ? (stg % 3) * STG + (c >> 2) * 1024 : (s < 4 ? s * 2048 : (stg % 3) * STG);
    if (hi) wf[i & 3].hi = tr_half(pb[c < 8 ? (c & 3) : 4] + (o + (c < 8 ? 4096 : 512))); else wf[i & 3].lo = tr_half(pb[c < 8 ? (c & 3) : 4] + o);
  };
  auto issue2 = [&](auto uc) {
    constexpr int u = uc.value;
    constexpr int nw0 = find_at(18, u, [](int n) { return w_issue(n, 0); }), nw1 = find_at(18, u, [](int n) { return w_issue(n, 1); });
    constexpr int nh0 = find_at(18, u, [](int n) { return h_issue(n, 0); }), nh1 = find_at(18, u, [](int n) { return h_issue(n, 1); });
    constexpr int r = find_at(5, u, [](int n) { return dq_issue(n); });
    constexpr int pi = find_at(3, u, [](int n) { return poll_iss(n); }), pc = find_at(3, u, [](int n) { return poll_chk(n); });
    if constexpr (nw0 >= 0) bdma_a<(nw0 % NST) * STG, rowc(kk_of(nw0))>(rD, d_voff, kk_of(nw0) % 9 < 8 ? rowA : rowQ, d_dst);
    if constexpr (nw1 >= 0) bdma_a<(nw1 % NST) * STG + 1024, rowc(kk_of(nw1)) + 128>(rD, d_voff, kk_of(nw1) % 9 < 8 ? rowA : rowQ, d_dst);
    if constexpr (nh0 >= 0) bload_sc1<(kk_of(nh0) & 3) * 1024>(hb[nh0 % HB][0], rS, hv0, (kk_of(nh0) >> 2) * 4096);
    if constexpr (nh1 >= 0) bload_sc1<(kk_of(nh1) & 3) * 1024>(hb[nh1 % HB][1], rS, hv1, (kk_of(nh1) >> 2) * 4096);
    if constexpr (r >= 0) {
      const int tsel = r == 4 ? (tq & 1) : tq;
      const u32 ra = row0(kk_of(4 * r)), rb = row0(kk_of(4 * r + 1)), rc_ = row0(kk_of(r == 4 ? 16 : 4 * r + 2)), rd = row0(kk_of(r == 4 ? 17 : 4 * r + 3));
      const u32 rsel = (tsel & 2) ? ((tsel & 1) ? rd : rc_) : ((tsel & 1) ? rb : ra);
      bdma<0>(rD, dq_voff, rsel, r == 0 ? L0 + G1_QE + (tq * 2 + kh) * 1024 : L0 + (((4 * r) % NST) + tsel) * STG + kh * 9216 + 8192);
    }
    if constexpr (pi >= 0) poll_issue(pv, p_voff, pflags);
    if constexpr (pc >= 0) poll_check<wait_k(4 * poll_iss(pc) + 2, 4 * u + 2), pc + 1>(pv, p_voff, pflags);
  };
  static_for<0, 216>([&](auto ic) {
    constexpr int t = ic.value, p = t / 36, s = t % 36;
    constexpr int slot = (p + 3) & 3;
    constexpr int hg = (p == 0) ? ((s >> 1) + 14) % 18 : (s >> 1);
    constexpr int ci = (t - G1_E0) / 26, st = (t - G1_E0) % 26;
    constexpr int lc = ci < 4 ? ci : (ci == 4 ? 8 : ci - 1), nlc = ci < 3 ? ci + 1 : (ci == 3 ? 8 : ci);
#pragma unroll
    for (int tl = 0; tl < 2; ++tl) {
      if constexpr ((t & 1) == 0) { if (tl == 0) touch(wf[(t + 1) & 3]); }
      mma<(s < 2)>(acc[slot].a[s & 1][tl], frag_of(wf[t & 3]), h[tl][hg]);
      SCHED0();
      if constexpr (t + 3 < 216) fetch(IC<t + 3>{}, tl);
      SCHED0();
      if constexpr (t >= G1_E0 && ci < 8 && st <= 24) epi(acc[ci & 3], lc, st, nlc, tl);
      SCHED0();
    }
    if constexpr (t == 2) ln2_stats();
    if constexpr (t == G1_E0 - 1) ldcs(0, 0);
    if constexpr (t == 7 || t == 28 || t == 72 || t == 190) { SCHED0(); BARRIER(); }
    if constexpr (t == 40) { wait_vm<wait_q(1, 40)>(); BARRIER(); }
    if constexpr (t == 56) { wait_vm<wait_q(3, 56)>(); BARRIER(); }
    if constexpr (t == 68) { wait_vm<wait_b(9, 68)>(); BARRIER(); }
    if constexpr (t == 88) { wait_vm<wait_b(14, 88)>(); BARRIER(); }
    if constexpr (t == 98) { wait_vm<wait_b(17, 98)>(); BARRIER(); }
    if constexpr (t == 160) { wait_vm<wait_k(4 * (epi_start(4) + 24), 4 * t + 1)>(); BARRIER(); flag_store(w, opq(0u), opq(1u), myflag); }
    if constexpr (t == 198) { SCHED0(); BARRIER(); }
    if constexpr (t == 208) { SCHED0(); wait_vm<wait_k(4 * w_issue(1, 1) + 2, 4 * t + 1)>(); BARRIER(); flag_store(w, opq(0u), opq(2u), myflag); }
    if constexpr (t == 7) q_voff = mk_qvoff(opq(tid) & 63);
    if constexpr (t == 179) {
      const u32 l2 = opq(tid) & 63;
      d_voff = (u32)((8 * tq + (l2 >> 3)) * RD + ((((l2 & 7) >> 1) ^ ((l2 >> 4) & 3)) * 32) + (l2 & 1) * 16 + dt * 256);
      dq_voff = (u32)((l2 >> 1) * RD + (l2 & 1) * 16 + 1024 + dt * 32);
      p_voff = (l2 & 3) * 512;
      hv0 = l2 * 16 + (u32)OFF_HH + ((tg2 * 8 + tq * 2 + kh) * 144 + mb * 9) * 1024;
      hv1 = l2 * 16 + (u32)OFF_HH + ((tg2 * 8 + tq * 2 + (kh ^ 1)) * 144 + mb * 9) * 1024;
      pin(d_voff); pin(dq_voff); pin(p_voff); pin(hv0); pin(hv1);
    }
    {
      constexpr int r = find_at(4, t, [](int n) { return g1_q_issue(n); }), j = find_at(18, t, [](int n) { return g1_b_issue(n); });
      if constexpr (r >= 0) bdma_i<G1_QL + r * 8192, (4 + 4 * r) * GS>(rW, q_voff, lw);
      if constexpr (j >= 0) bdma_i<j * 8192, j * GS + 128>(rW, ab_voff, ab_dst);
    }
    if constexpr (t >= PF_ISS0 && t < PF_ISS0 + 20 && (t - PF_ISS0) % 4 == 0) {
      const int piece = ((b >> 3) * 5 + (t - PF_ISS0) / 4) * 8 + w;
      const int byte = piece * 1024;
      bdma<G1_QE>(rD, lo16, (u32)(byte < 1024 * 1152 ? (b & 3) * 1024 * 1152 + byte : 4096 * 1152 + (b & 3) * 128 * 1152 + (byte - 1024 * 1152)), lw);
    }
    if constexpr (t == 3 || t == 102) code_touch(code0, min(opq((u32)tid) * 64u + (t == 3 ? 8192u : 18432u), codelen - 4u), L0 + FF_LDS - 256);
    if constexpr (t >= 180) issue2(ic);
    SCHED0();
  });
  {
    const u32 l2 = opq(tid) & 63;
    const u32 q2 = l2 >> 4, r2_ = (l2 >> 2) & 3, p2 = l2 & 3;
    rb2 = L0 + kh * 9216 + (q2 >> 1) * 2048 + (4 * (q2 & 1) + r2_) * 128 + ((2 * (q2 & 1) + (r2_ >> 1)) * 32) + p2 * 8;
    rbq = L0 + kh * 9216 + 8192 + (4 * q2 + r2_) * 32 + p2 * 8;
  }
  const int ft = tg2 * 8 + ks * 2 + kh;
  const int dcol = dt * 128 + tq * 32 + 4 * rq;
  const int qcol = 512 + dt * 16 + 4 * rq;
  u32x2 r0, r1, r2;
  const ldsp xw = LP + (2 * STG + w * 9216 + l * 16);
  static_for<0, 3>([&](auto ic) { fetch2(ic, 0); fetch2(ic, 1); });
  wait_vm2<wait_k(4 * h_issue(0, 1) + 2, 4 * U2 - 1)>(hb[0][0], hb[0][1]);
  static_for<0, 162>([&](auto ic) {
    constexpr int i = ic.value, s = i / 9, c = i % 9, u = U2 + i;
    if constexpr (c == ((s & 1) ? 6 : 4) && s < 17) {
      constexpr int s2 = cmin(s + 2, 17);
      constexpr int tgt = (s & 1) ? 4 * h_issue(s + 1, 1) + 2
                                  : cmax(cmax(cmax(4 * h_issue(s + 1, 1) + 2, 4 * w_issue(s2, 1) + 2), 4 * dq_issue((s + 1) >> 2) + 2), s == 4 ? 4 * (U2 + 14) : 0);
      wait_vm2<wait_k(tgt, 4 * u - 1)>(hb[(s + 1) % HB][0], hb[(s + 1) % HB][1]);
      if constexpr ((s & 1) == 0) BARRIER();
      if constexpr (s == 4) flag_store(w, opq(0u), opq(3u), myflag);
    }
    SCHED0();
#pragma unroll
    for (int tl = 0; tl < 2; ++tl) {
      if constexpr ((i & 1) == 0 && i + 1 < 162) { if (tl == 0) touch(wf[(i + 1) & 3]); }
      mma<s == 0>(ac2[c][tl], frag_of(wf[i & 3]), hb[s % HB][tl]);
      SCHED0();
      if constexpr (i + 3 < 162) fetch2(IC<i + 3>{}, tl);
      SCHED0();
      if constexpr (i >= 2 && i <= 14) { if (i < 14 || tl == 0) { epi(acc[0], 7, 2 * (i - 2) + tl, -1, 0); epi(acc[0], 7, 2 * (i - 2) + tl, -1, 1); } }
      SCHED0();
    }
    if constexpr (s == 17 && c >= 3) *(LAS f32x4*)(xw + (c - 3) * 1024) = ac2[c - 3][1];
    issue2(IC<u>{});
    if constexpr (u == RES_ISS) {
      const char* hb_ = ws + OFF_HF + (size_t)(ft >> 1) * 36864 + (ft & 1) * 8;
      auto po = [&](int d) { return (u32)((d >> 5) * 2048 + ((d >> 2) & 1) * 1024 + ((d >> 3) & 3) * 256 + (l & 15) * 16); };
      gload8(r0, po(dcol), hb_); gload8(r1, po(dcol + 16), hb_); gload8(r2, po(qcol), hb_);
    }
    SCHED0();
  });
  MFMA_DRAIN(); SCHED0();
#pragma unroll
  for (int c = 6; c < 9; ++c) *(LAS f32x4*)(xw + c * 1024) = ac2[c][1];
  LDS_BARRIER();
  f32x4 sm[9];
  {
    const ldsp xr = LP + (2 * STG + (w ^ 4) * 9216 + l * 16);
#pragma unroll
    for (int c = 0; c < 9; ++c) sm[c] = ac2[c][0] + *(const LAS f32x4*)(xr + c * 1024);
  }
  char* part = ws + OFF_PART;
  if (tq != ks) {
    char* pdst = part + ((((size_t)(tg2 * 4 + dt) * 4 + tq) * 4 + ks) * 2 + kh) * 4608;
#pragma unroll
    for (int pc = 0; pc < 4; ++pc) {
      u32x4 v = {pack2(sm[2 * pc][0], sm[2 * pc][1]), pack2(sm[2 * pc][2], sm[2 * pc][3]), pack2(sm[2 * pc + 1][0], sm[2 * pc + 1][1]), pack2(sm[2 * pc + 1][2], sm[2 * pc + 1][3])};
      st_sc1_x4(pdst + pc * 1024, lo16, v);
    }
    u32x2 v2 = {pack2(sm[8][0], sm[8][1]), pack2(sm[8][2], sm[8][3])};
    st_sc1_x2(pdst + 4096, l * 8, v2);
  } else {
    const ldsp ow = LP + (kh * 9216 + l * 16);
#pragma unroll
    for (int c = 0; c < 9; ++c) *(LAS f32x4*)(ow + c * 1024) = sm[c];
  }
  asm volatile("s_waitcnt vmcnt(0)" : "+v"(r0), "+v"(r1), "+v"(r2)::"memory");
  LDS_BARRIER();
  const char* fl = ws + OFF_FLG + (tg2 * 4 + dt) * 256;
  flag_store(w, opq(0u), opq(1u), fl + ks * 64);
  const ldsp orp = LP + (kh * 9216 + l * 16);
  f32x4 o0 = *(const LAS f32x4*)(orp + tq * 2048), o1 = *(const LAS f32x4*)(orp + (tq * 2048 + 1024)), o2 = *(const LAS f32x4*)(orp + 8192);
  o0[0] += bflo(r0[0]); o0[1] += bfhi(r0[0]); o0[2] += bflo(r0[1]); o0[3] += bfhi(r0[1]);
  o1[0] += bflo(r1[0]); o1[1] += bfhi(r1[0]); o1[2] += bflo(r1[1]); o1[3] += bfhi(r1[1]);
  o2[0] += bflo(r2[0]); o2[1] += bfhi(r2[0]); o2[2] += bflo(r2[1]); o2[3] += bfhi(r2[1]);
  {
    const u32 fo = (l & 3) * 64;
    poll_issue(pv, fo, fl);
    poll_check<0, 1>(pv, fo, fl);
  }
  {
    const char* psrc = part + (((size_t)(tg2 * 4 + dt) * 4 + ks) * 4 * 2 + kh) * 4608;
    u32x4 v[3]; u32x2 vq[3];
#pragma unroll
    for (int j = 0; j < 3; ++j) {
      const int src = (ks + 1 + j) & 3;
      ld_sc1_x4(v[j], lo16 + tq * 1024, psrc + src * 9216);
      ld_sc1_x2<0>(vq[j], l * 8 + 4096, psrc + src * 9216);
    }
    asm volatile("s_waitcnt vmcnt(0)" : "+v"(v[0]), "+v"(v[1]), "+v"(v[2]), "+v"(vq[0]), "+v"(vq[1]), "+v"(vq[2])::"memory");
#pragma unroll
    for (int j = 0; j < 3; ++j) {
      o0[0] += bflo(v[j][0]); o0[1] += bfhi(v[j][0]); o0[2] += bflo(v[j][1]); o0[3] += bfhi(v[j][1]);
      o1[0] += bflo(v[j][2]); o1[1] += bfhi(v[j][2]); o1[2] += bflo(v[j][3]); o1[3] += bfhi(v[j][3]);
      o2[0] += bflo(vq[j][0]); o2[1] += bfhi(vq[j][0]); o2[2] += bflo(vq[j][1]); o2[3] += bfhi(vq[j][1]);
    }
    u16* op = out + (size_t)(ft * 16 + (l & 15)) * D_;
    u32x2 oa = {pack2(o0[0], o0[1]), pack2(o0[2], o0[3])}, ob = {pack2(o1[0], o1[1]), pack2(o1[2], o1[3])}, oc = {pack2(o2[0], o2[1]), pack2(o2[2], o2[3])};
    *(u32x2*)(op + dcol) = oa; *(u32x2*)(op + dcol + 16) = ob;
    if (tq == 0) *(u32x2*)(op + qcol) = oc;
  }
  asm volatile(".Lffn_code_end:");
}

extern "C" void kernel_launch(void* const* d_in, const int* in_sizes, int n_in, void* d_out, int out_size, void* d_ws, size_t ws_size,
                              hipStream_t stream) {
  const u16* q = (const u16*)d_in[0]; const u16* k = (const u16*)d_in[1]; const u16* v = (const u16*)d_in[2];
  const u16* shk = (const u16*)d_in[3]; const u16* ssm = (const u16*)d_in[4]; const u16* ssmD = (const u16*)d_in[5];
  const u16* wg = (const u16*)d_in[7]; const u16* wu = (const u16*)d_in[8]; const u16* wd = (const u16*)d_in[9];
  char* ws = (char*)d_ws;
  (void)hipFuncSetAttribute((const void*)k1_conv, hipFuncAttributeMaxDynamicSharedMemorySize, K1_LDS);
  (void)hipFuncSetAttribute((const void*)ffn_fused, hipFuncAttributeMaxDynamicSharedMemorySize, FF_LDS);
  k0_prep<<<K0_GRID, 256, 0, stream>>>(q, k, v, shk, ssm, ws);
  k1_conv<<<K1_GRID, 512, K1_LDS, stream>>>(ssm, ssmD, wg, wu, ws, wd, (const u16*)d_out);
  ffn_fused<<<256, 512, FF_LDS, stream>>>(wg, wu, wd, ws, (u16*)d_out);
}
```
